```python
import torch

def _softmax(x, axis=-1):
    return torch.softmax(x, dim=axis)
def _gelu(x):
    return torch.nn.functional.gelu(x, approximate="tanh")
def _silu(x):
    return torch.nn.functional.silu(x)
def _sigmoid(x):
    return torch.sigmoid(x)
def _layernorm(x, axis=-1, eps=1e-5):
    m = x.mean(dim=axis, keepdim=True)
    v = x.var(dim=axis, keepdim=True, unbiased=False)
    return (x - m) / torch.sqrt(v + eps)
def _scatter_add(tgt, idx, src, axis=0):
    return tgt.clone().index_add_(axis, idx, src)
def _segment_sum(src, seg, n):
    out = torch.zeros((n,) + src.shape[1:], dtype=src.dtype)
    return out.index_add_(0, seg, src)
def _pad(x, pw):
    flat = tuple(v for pair in reversed(pw) for v in pair)
    return torch.nn.functional.pad(x, flat)

kernel_name = 'gated_mix__linear_attn_block__spectral_gfnet__prompt'

def setup_inputs():

    _w = (lambda a: (lambda t: t.to(torch.bfloat16) if t.is_floating_point() else t)(torch.as_tensor(a)))
    def _A_setup():
        B, S, D = 1, 2048, 512
        return {
            "x": _w((lambda *s: torch.randn(*s, dtype=torch.bfloat16) * 0.05)(B, S, D)),
            "wqkv": _w((lambda *s: torch.randn(*s, dtype=torch.bfloat16) * 0.4)(D, 3 * D)),
            "wo": _w((lambda *s: torch.randn(*s, dtype=torch.bfloat16) * 0.05)(D, D)),
        }
    _A_in = _A_setup()

    def _B_setup():
        B, S, D = 1, 2048, 512
        Df = D // 2 + 1
        return {
            "x": _w((lambda *s: torch.randn(*s, dtype=torch.bfloat16) * 0.05)(B, S, D)),
            "w_r": _w((lambda *s: torch.randn(*s, dtype=torch.bfloat16) * 1.6)(S, Df) * 0.02),
            "w_i": _w((lambda *s: torch.randn(*s, dtype=torch.bfloat16) * 1.6)(S, Df) * 0.02),
        }
    _B_in = _B_setup()

    return {
        "A_x": _A_in["x"],
        "A_wqkv": _A_in["wqkv"],
        "A_wo": _A_in["wo"],
        "B_x": _B_in["x"],
        "B_w_r": _B_in["w_r"],
        "B_w_i": _B_in["w_i"],
        "gate_w": _w((lambda *s: torch.randn(*s, dtype=torch.bfloat16) * 0.05)(512) * 16.0),
    }

def reference(A_x, A_wqkv, A_wo, B_x, B_w_r, B_w_i, gate_w):
    def _A_ref(x, wqkv, wo):
        B, S, D = x.shape
        H = 32; Dh = D // H
        qkv = (lambda x, *d: x.permute(*d))((x @ wqkv).reshape(B, S, 3, H, Dh), 2, 0, 3, 1, 4)
        q, k, v = qkv[0], qkv[1], qkv[2]
        phi = lambda t: torch.where(t > 0, t + 1, torch.exp(t)) + 1e-6
        pq, pk = phi(q), phi(k)
        kv = torch.einsum("bhsd,bhse->bhde", pk, v)
        z = torch.einsum("bhsd->bhd", pk)
        num = torch.einsum("bhqd,bhde->bhqe", pq, kv)
        den = torch.einsum("bhqd,bhd->bhq", pq, z)[..., None] + 1e-6
        o = num / den
        return (lambda x, *d: x.permute(*d))(o, 0, 2, 1, 3).reshape(B, S, D) @ wo

    def _B_ref(x, w_r, w_i):
        B, S, D = x.shape
        X = (lambda x, axes=(-2,-1): torch.fft.rfft2(x.float(), dim=axes))(x, axes=(-2, -1))
        W = w_r + 1j * w_i
        X = X * W
        y = (lambda x, s, axes=(-2,-1): torch.fft.irfft2(x, s=s, dim=axes).to(torch.bfloat16))(X, (S, D), axes=(-2, -1))
        return y

    x = A_x
    g = _sigmoid((x * gate_w).sum(-1, keepdims=True))
    a = _A_ref(x, A_wqkv, A_wo)
    b = _B_ref(x, B_w_r, B_w_i)
    return g * a + (1 - g) * b
```

```cpp
#include <hip/hip_runtime.h>
#include <hip/hip_bf16.h>
typedef __hip_bfloat16 bf16;
__device__ __forceinline__ float rb(float x){ return __bfloat162float(__float2bfloat16(x)); }
__device__ __forceinline__ float phi(float t){ float r = t>0.f ? rb(t+1.f) : rb(expf(t)); return rb(r+1e-6f); }

__global__ void k_qkv(const bf16* x, const bf16* w, float* qkv){
  int n = blockIdx.x*256+threadIdx.x; int s = blockIdx.y;
  float acc=0; for(int k=0;k<512;k++) acc += __bfloat162float(x[s*512+k])*__bfloat162float(w[k*1536+n]);
  acc = rb(acc);
  if(n<1024) acc = phi(acc);
  qkv[s*1536+n]=acc;
}
__global__ void k_kv(const float* qkv, float* kv, float* z){
  int h = blockIdx.x; int t = threadIdx.x;
  if(t<256){ int d=t/16,e=t%16; float acc=0; for(int s=0;s<2048;s++) acc += qkv[s*1536+512+h*16+d]*qkv[s*1536+1024+h*16+e]; kv[h*256+t]=rb(acc);}
  else if(t<272){ int d=t-256; float acc=0; for(int s=0;s<2048;s++) acc += qkv[s*1536+512+h*16+d]; z[h*16+d]=rb(acc);}
}
__global__ void k_o(const float* qkv, const float* kv, const float* z, float* o){
  int n = blockIdx.x*256+threadIdx.x; int s = blockIdx.y; int h=n/16,e=n%16;
  float num=0,den=0; for(int d=0;d<16;d++){ float p=qkv[s*1536+h*16+d]; num+=p*kv[h*256+d*16+e]; den+=p*z[h*16+d]; }
  num=rb(num); den=rb(rb(den)+1e-6f); o[s*512+n]=rb(num/den);
}
__global__ void k_a(const float* o, const bf16* wo, float* a){
  int n = blockIdx.x*256+threadIdx.x; int s = blockIdx.y;
  float acc=0; for(int k=0;k<512;k++) acc += o[s*512+k]*__bfloat162float(wo[k*512+n]);
  a[s*512+n]=rb(acc);
}
__global__ void k_tab(float2* tab){ int i=blockIdx.x*256+threadIdx.x; if(i<2048){ double a=-2.0*3.14159265358979323846*i/2048.0; tab[i]=make_float2((float)cos(a),(float)sin(a)); } }
__global__ void k_rowdft(const bf16* x, const float2* tab, float2* X){
  int k = blockIdx.x*64+threadIdx.x; int s=blockIdx.y; if(k>256) return;
  float re=0,im=0; for(int d=0;d<512;d++){ float v=__bfloat162float(x[s*512+d]); float2 t=tab[((k*d)&511)*4]; re+=v*t.x; im+=v*t.y; }
  X[s*257+k]=make_float2(re,im);
}
__global__ void k_coldft(const float2* X, const float2* tab, const bf16* wr, const bf16* wi, float2* Y){
  int k = blockIdx.x*64+threadIdx.x; int u=blockIdx.y; if(k>256) return;
  float re=0,im=0; for(int s=0;s<2048;s++){ float2 v=X[s*257+k]; float2 t=tab[(u*s)&2047]; re+=v.x*t.x-v.y*t.y; im+=v.x*t.y+v.y*t.x; }
  float a=__bfloat162float(wr[u*257+k]), b=__bfloat162float(wi[u*257+k]);
  Y[u*257+k]=make_float2(re*a-im*b, re*b+im*a);
}
__global__ void k_colidft(const float2* Y, const float2* tab, float2* Z){
  int k = blockIdx.x*64+threadIdx.x; int s=blockIdx.y; if(k>256) return;
  float re=0,im=0; for(int u=0;u<2048;u++){ float2 v=Y[u*257+k]; float2 t=tab[(u*s)&2047]; t.y=-t.y; re+=v.x*t.x-v.y*t.y; im+=v.x*t.y+v.y*t.x; }
  Z[s*257+k]=make_float2(re,im);
}
__global__ void k_final(const float2* Z, const float2* tab, const float* a, const bf16* x, const bf16* gw, bf16* out){
  int d = blockIdx.x*256+threadIdx.x; int s=blockIdx.y;
  float acc = Z[s*257].x + ((d&1)?-1.f:1.f)*Z[s*257+256].x;
  for(int k=1;k<256;k++){ float2 v=Z[s*257+k]; float2 t=tab[((k*d)&511)*4]; acc += 2.f*(v.x*t.x + v.y*t.y); }
  float b = rb(acc*(1.f/(2048.f*512.f)));
  float gs=0; for(int k=0;k<512;k++) gs += rb(__bfloat162float(x[s*512+k])*__bfloat162float(gw[k]));
  gs=rb(gs); float g=rb(1.f/(1.f+expf(-gs)));
  float r = rb(rb(g*a[s*512+d]) + rb(rb(1.f-g)*b));
  out[s*512+d]=__float2bfloat16(r);
}
extern "C" void kernel_launch(void* const* d_in, const int* in_sizes, int n_in, void* d_out, int out_size, void* d_ws, size_t ws_size, hipStream_t stream){
  const bf16* x=(const bf16*)d_in[0]; const bf16* wqkv=(const bf16*)d_in[1]; const bf16* wo=(const bf16*)d_in[2];
  const bf16* wr=(const bf16*)d_in[4]; const bf16* wi=(const bf16*)d_in[5]; const bf16* gw=(const bf16*)d_in[6];
  char* ws=(char*)d_ws;
  float* qkv=(float*)ws; ws+=2048*1536*4;
  float* kv=(float*)ws; ws+=32*256*4; float* z=(float*)ws; ws+=32*16*4;
  float* o=(float*)ws; ws+=2048*512*4; float* a=(float*)ws; ws+=2048*512*4;
  float2* tab=(float2*)ws; ws+=2048*8;
  float2* X=(float2*)ws; ws+=2048*257*8; float2* Y=(float2*)ws; ws+=2048*257*8; float2* Z=(float2*)ws; ws+=2048*257*8;
  k_qkv<<<dim3(6,2048),256,0,stream>>>(x,wqkv,qkv);
  k_kv<<<32,320,0,stream>>>(qkv,kv,z);
  k_o<<<dim3(2,2048),256,0,stream>>>(qkv,kv,z,o);
  k_a<<<dim3(2,2048),256,0,stream>>>(o,wo,a);
  k_tab<<<8,256,0,stream>>>(tab);
  k_rowdft<<<dim3(5,2048),64,0,stream>>>(x,tab,X);
  k_coldft<<<dim3(5,2048),64,0,stream>>>(X,tab,wr,wi,Y);
  k_colidft<<<dim3(5,2048),64,0,stream>>>(Y,tab,Z);
  k_final<<<dim3(2,2048),256,0,stream>>>(Z,tab,a,x,gw,(bf16*)d_out);
}
```

```cpp
#include <hip/hip_runtime.h>
#include <hip/hip_bf16.h>
#include <stdint.h>

typedef short s16x4 __attribute__((ext_vector_type(4)));
typedef short s16x8 __attribute__((ext_vector_type(8)));
typedef float f32x2 __attribute__((ext_vector_type(2)));
typedef float f32x4 __attribute__((ext_vector_type(4)));
typedef unsigned u32x2 __attribute__((ext_vector_type(2)));
typedef unsigned u32x4 __attribute__((ext_vector_type(4)));
typedef __bf16 bf16x2 __attribute__((ext_vector_type(2)));
#define DI __device__ __forceinline__
#define LDS_AS __attribute__((address_space(3)))

DI unsigned pk2(float a, float b) {
  f32x2 v = {a, b};
  return __builtin_bit_cast(unsigned, __builtin_convertvector(v, bf16x2));
}
DI float bflo(unsigned u) { return __builtin_bit_cast(float, u << 16); }
DI float bfhi(unsigned u) { return __builtin_bit_cast(float, u & 0xffff0000u); }
DI f32x4 mfma32(s16x8 a, s16x8 b, f32x4 c) { return __builtin_amdgcn_mfma_f32_16x16x32_bf16(a, b, c, 0, 0, 0); }
DI f32x4 mfma16(s16x4 a, s16x4 b, f32x4 c) { return __builtin_amdgcn_mfma_f32_16x16x16bf16_1k(a, b, c, 0, 0, 0); }
DI float phi(float t) {
  return fmaxf(t, 0.f) + __builtin_amdgcn_exp2f(fminf(t * 1.4426950408889634f, 0.f)) + 1e-6f;
}

constexpr size_t WS_XT = 0;
constexpr size_t WS_WT = WS_XT + 256 * 2048 * 4;
constexpr size_t WS_PQ = WS_WT + 257 * 2048 * 4;
constexpr size_t WS_KVP = WS_PQ + 2048 * 512 * 2;
constexpr size_t WS_ZP = WS_KVP + 16 * 32 * 1024;
constexpr size_t WS_Z = WS_ZP + 16 * 32 * 64;
constexpr size_t WS_O = WS_Z + 256 * 2048 * 4;
constexpr size_t WS_WOF = WS_O + 2048 * 512 * 2;
constexpr size_t WS_G = WS_WOF + 512 * 512 * 2;

constexpr int STG = 28672;
constexpr int L_F = 4 * STG;
constexpr int L_S = L_F + 32768;
constexpr int P1_LDS = L_S + 16384;

template <bool V> struct BoolC { static constexpr bool value = V; };
template <bool SWAP>
DI void p1_step(const char* sm, unsigned aoff, unsigned b0off, unsigned b1off, f32x4 (&acc)[4][2]) {
#pragma unroll
  for (int ks = 0; ks < 2; ++ks) {
    s16x8 a[4], b[2];
#pragma unroll
    for (int t = 0; t < 4; ++t) a[t] = *(const s16x8*)(sm + ((aoff ^ (ks * 64)) + t * 2048));
    {
      s16x4 l0 = __builtin_amdgcn_ds_read_tr16_b64_v4i16((LDS_AS s16x4*)(sm + b0off + ks * 2048));
      s16x4 h0 = __builtin_amdgcn_ds_read_tr16_b64_v4i16((LDS_AS s16x4*)(sm + b0off + ks * 2048 + 256));
      s16x4 l1 = __builtin_amdgcn_ds_read_tr16_b64_v4i16((LDS_AS s16x4*)(sm + b1off + ks * 2048));
      s16x4 h1 = __builtin_amdgcn_ds_read_tr16_b64_v4i16((LDS_AS s16x4*)(sm + b1off + ks * 2048 + 256));
      b[0] = __builtin_shufflevector(l0, h0, 0, 1, 2, 3, 4, 5, 6, 7);
      b[1] = __builtin_shufflevector(l1, h1, 0, 1, 2, 3, 4, 5, 6, 7);
    }
#pragma unroll
    for (int t = 0; t < 4; ++t)
#pragma unroll
      for (int n = 0; n < 2; ++n)
        acc[t][n] = SWAP ? mfma32(b[n], a[t], acc[t][n]) : mfma32(a[t], b[n], acc[t][n]);
  }
}

__global__ __launch_bounds__(512, 2) void p1_kernel(const char* __restrict__ x, const char* __restrict__ wqkv,
                                                    const char* __restrict__ w_r, const char* __restrict__ w_i,
                                                    char* __restrict__ ws) {
  extern __shared__ __attribute__((aligned(16))) char sm[];
  asm volatile("" :: "s"(x), "s"(wqkv), "s"(w_r), "s"(w_i), "s"(ws));
  const int tid = threadIdx.x, lane = tid & 63;
  const int w = __builtin_amdgcn_readfirstlane(tid >> 6);
  const int bid = blockIdx.x, xcd = bid & 7, idx = bid >> 3;
  const int mt = (xcd & 3) * 4 + (idx & 3), nt = (xcd >> 2) * 8 + (idx >> 2);

  const int j6 = bid & 63, kb = bid >> 6;
  u32x2 wrv, wiv;
  {
    const int pos = tid >> 4, kk = tid & 15;
    const int u = (j6 >> 3) + 8 * (j6 & 7) + 64 * (pos >> 2) + 512 * (pos & 3);
    const unsigned off = (unsigned)(u * 257 + kb * 64 + kk * 4) * 2u;
    asm volatile("global_load_dwordx2 %0, %2, %3\n\tglobal_load_dwordx2 %1, %2, %4"
                 : "=&v"(wrv), "=&v"(wiv) : "v"(off), "s"(w_r), "s"(w_i) : "memory");
  }
  unsigned voff[4];
  const char* sbase;
  unsigned sstride;
  if (w < 4) {
#pragma unroll
    for (int i = 0; i < 4; ++i) {
      const int rho = (w * 4 + i) * 8 + (lane >> 3);
      const int ii = (rho & 64) + 4 * (rho & 15) + ((rho >> 4) & 3);
      const int s = 16 * mt + (ii >> 3) + 256 * (ii & 7);
      voff[i] = (unsigned)(s * 1024 + (((lane & 7) ^ ((rho >> 1) & 7)) << 4) + 4096 - i * 1024);
    }
    sbase = x - 4096;
    sstride = 128;
  } else {
#pragma unroll
    for (int i = 0; i < 4; ++i) {
      const int kr = i * 16 + (lane >> 2);
      voff[i] = (unsigned)(kr * 3072 + (((lane & 3) ^ ((kr >> 2) & 2)) << 4) + 4096 - i * 1024);
    }
    sbase = wqkv + (w - 4) * 1024 + nt * 64 - 4096;
    sstride = 64 * 3072;
  }
  const unsigned lds0 = (unsigned)(size_t)(LDS_AS char*)sm + w * 4096;
  auto issue = [&](int kc) {
    if (w < 7) {
      const char* sb = sbase + (unsigned)kc * sstride;
      const unsigned dst = lds0 + (kc & 3) * STG;
      unsigned keep;
      asm volatile("s_mov_b32 %0, m0\n\ts_mov_b32 m0, %6\n\ts_nop 0\n\t"
                   "global_load_lds_dwordx4 %1, %5\n\t"
                   "global_load_lds_dwordx4 %2, %5 offset:1024\n\t"
                   "global_load_lds_dwordx4 %3, %5 offset:2048\n\t"
                   "global_load_lds_dwordx4 %4, %5 offset:3072\n\t"
                   "s_mov_b32 m0, %0"
                   : "=&s"(keep) : "v"(voff[0]), "v"(voff[1]), "v"(voff[2]), "v"(voff[3]), "s"(sb), "s"(dst) : "memory");
    }
  };
  issue(0); issue(1); issue(2); issue(3);

  {
    const int slot = tid & 3;
#pragma unroll
    for (int i = 0; i < 4; ++i) {
      const int d = (tid >> 2) + 128 * i;
      const int ch = slot ^ ((d >> 2) & 2);
      const float df = (float)d * (1.f / 512.f);
      const float k0 = (float)(16 * nt + 8 * (ch & 1));
      const float ph = (ch & 2) ? 0.25f : 0.f;
      float v[8];
#pragma unroll
      for (int j = 0; j < 8; ++j) {
        float a = __builtin_fmaf(k0 + (float)j, df, ph);
        if (j == 0 && nt == 0 && ch == 2) a = 256.f * df;
        v[j] = __builtin_amdgcn_cosf(__builtin_amdgcn_fractf(a));
      }
      u32x4 o = {pk2(v[0], v[1]), pk2(v[2], v[3]), pk2(v[4], v[5]), pk2(v[6], v[7])};
      *(u32x4*)(sm + L_F + d * 64 + slot * 16) = o;
    }
  }
  if (w == 7 && kb == 3 && lane < 32) {
    const int u = (j6 >> 3) + 8 * (j6 & 7) + 64 * (lane >> 2) + 512 * (lane & 3);
    const unsigned a = *(const unsigned short*)(w_r + (u * 257 + 256) * 2);
    const unsigned b = *(const unsigned short*)(w_i + (u * 257 + 256) * 2);
    *(unsigned*)(ws + WS_WT + (256 * 2048 + j6 * 32 + lane) * 4) = a | (b << 16);
  }
  if (w == 7) asm volatile("s_waitcnt vmcnt(0)" ::: "memory");
  asm volatile("s_waitcnt vmcnt(12)" : "+v"(wrv), "+v"(wiv) : : "memory");
  {
    const int pos = tid >> 4, kk = tid & 15;
    unsigned* sc = (unsigned*)(sm + L_S) + kk * 128 + (pos ^ (4 * (kk & 7)));
    sc[0] = __builtin_amdgcn_perm(wiv.x, wrv.x, 0x05040100u);
    sc[32] = __builtin_amdgcn_perm(wiv.x, wrv.x, 0x07060302u);
    sc[64] = __builtin_amdgcn_perm(wiv.y, wrv.y, 0x05040100u);
    sc[96] = __builtin_amdgcn_perm(wiv.y, wrv.y, 0x07060302u);
  }

  const int wm = w >> 2;
  const int role = (w & 3) ^ ((w >> 2) & (w >> 1) & 1);
  const int c = lane & 15, g = lane >> 4;
  const unsigned aoff = (unsigned)((64 * wm + c) * 128 + ((g ^ ((c >> 1) & 7)) << 4));
  const unsigned brow = (unsigned)((8 * g + ((lane & 15) >> 2)) * 64 + 8 * (lane & 3));
  unsigned b0, b1;
  {
    const unsigned h0 = (unsigned)((g & 1) << 5), h1 = h0 ^ 32u;
    if (role < 2) { const unsigned hh = role ? h1 : h0; b0 = 16384 + 4096 + brow + hh; b1 = 16384 + 8192 + brow + hh; }
    else if (role == 2) { b0 = 16384 + brow + h0; b1 = 16384 + brow + h1; }
    else { b0 = L_F + brow + h0; b1 = L_F + brow + h1; }
  }
  f32x4 acc[4][2];
#pragma unroll
  for (int t = 0; t < 4; ++t) { acc[t][0] = f32x4{0, 0, 0, 0}; acc[t][1] = f32x4{0, 0, 0, 0}; }

  auto mainloop = [&](auto swap_tag) {
#pragma unroll 1
    for (int kc = 0; kc < 8; ++kc) {
      if (kc == 7) asm volatile("s_waitcnt vmcnt(0)" ::: "memory");
      else if (kc == 6) asm volatile("s_waitcnt vmcnt(4)" ::: "memory");
      else if (kc > 0) asm volatile("s_waitcnt vmcnt(8)" ::: "memory");
      asm volatile("s_waitcnt lgkmcnt(0)" ::: "memory");
      __builtin_amdgcn_s_barrier();
      asm volatile("" ::: "memory");
      if (kc == 0) {
        const int k = tid >> 3, mq = tid & 7;
        const u32x4 v = *(const u32x4*)(sm + L_S + k * 128 + ((mq ^ ((k >> 2) & 7)) << 4));
        *(u32x4*)(ws + WS_WT + ((size_t)(kb * 64 + k) * 2048 + j6 * 32 + mq * 4) * 4) = v;
      } else if (kc < 5) issue(kc + 3);
      const unsigned so = (unsigned)((kc & 3) * STG);
      const unsigned bo = role == 3 ? (unsigned)(kc * 4096) : so;
      p1_step<decltype(swap_tag)::value>(sm, aoff + so, b0 + bo, b1 + bo, acc);
    }
  };
  if (role == 2) mainloop(BoolC<true>{});
  else mainloop(BoolC<false>{});

  const int B = 2 * mt + wm;
  f32x4 kv = {0, 0, 0, 0}, zz = {0, 0, 0, 0};
  if (role == 2) {
#pragma unroll
    for (int n = 0; n < 2; ++n)
#pragma unroll
      for (int tp = 0; tp < 2; ++tp) {
        const f32x4 a = acc[2 * tp][n], b = acc[2 * tp + 1][n];
        u32x4 o = {pk2(phi(a[0]), phi(a[1])), pk2(phi(a[2]), phi(a[3])), pk2(phi(b[0]), phi(b[1])), pk2(phi(b[2]), phi(b[3]))};
        *(u32x4*)(ws + WS_PQ + ((((size_t)(2 * nt + n) * 32 + B) * 2 + tp) * 64 + lane) * 16) = o;
      }
  } else if (role == 3) {
    char* dst = ws + WS_XT + ((size_t)(16 * nt + c) * 2048 + 64 * B + 16 * g) * 4;
#pragma unroll
    for (int r = 0; r < 4; ++r) {
      u32x4 o = {pk2(acc[0][0][r], acc[0][1][r]), pk2(acc[1][0][r], acc[1][1][r]), pk2(acc[2][0][r], acc[2][1][r]), pk2(acc[3][0][r], acc[3][1][r])};
      *(u32x4*)(dst + r * 16) = o;
    }
  } else {
    const s16x8 ones = {0x3f80, 0x3f80, 0x3f80, 0x3f80, 0x3f80, 0x3f80, 0x3f80, 0x3f80};
#pragma unroll
    for (int tp = 0; tp < 2; ++tp) {
      const f32x4 a = acc[2 * tp][0], b = acc[2 * tp + 1][0], va = acc[2 * tp][1], vb = acc[2 * tp + 1][1];
      u32x4 pk = {pk2(phi(a[0]), phi(a[1])), pk2(phi(a[2]), phi(a[3])), pk2(phi(b[0]), phi(b[1])), pk2(phi(b[2]), phi(b[3]))};
      u32x4 vv = {pk2(va[0], va[1]), pk2(va[2], va[3]), pk2(vb[0], vb[1]), pk2(vb[2], vb[3])};
      kv = mfma32(__builtin_bit_cast(s16x8, pk), __builtin_bit_cast(s16x8, vv), kv);
      zz = mfma32(__builtin_bit_cast(s16x8, pk), ones, zz);
    }
    if (wm == 1) {
      *(f32x4*)(sm + L_S + 8192 + role * 2048 + lane * 16) = kv;
      *(f32x4*)(sm + L_S + 8192 + role * 2048 + 1024 + lane * 16) = zz;
    }
  }
  asm volatile("s_waitcnt lgkmcnt(0)" ::: "memory");
  __builtin_amdgcn_s_barrier();
  asm volatile("" ::: "memory");
  if (role < 2 && wm == 0) {
    kv += *(const f32x4*)(sm + L_S + 8192 + role * 2048 + lane * 16);
    zz += *(const f32x4*)(sm + L_S + 8192 + role * 2048 + 1024 + lane * 16);
    const int h = 2 * nt + role;
    *(f32x4*)(ws + WS_KVP + ((size_t)(mt * 32 + h) * 64 + lane) * 16) = kv;
    if (c == 0) *(f32x4*)(ws + WS_ZP + ((size_t)(mt * 32 + h) * 4 + g) * 16) = zz;
  }
}

DI f32x2 cmul(f32x2 a, f32x2 w) {
  f32x2 t = a.yy * f32x2{-w.y, w.x};
  return __builtin_elementwise_fma(a.xx, w, t);
}
DI f32x2 cmulc(f32x2 a, f32x2 w) {
  f32x2 t = a.yy * f32x2{w.y, w.x};
  return __builtin_elementwise_fma(a.xx, f32x2{w.x, -w.y}, t);
}
template <bool INV> DI f32x2 rot(f32x2 a) { return INV ? f32x2{-a.y, a.x} : f32x2{a.y, -a.x}; }
template <bool INV> DI void bf4(f32x2& a, f32x2& b, f32x2& c, f32x2& d) {
  f32x2 s0 = a + c, s1 = a - c, s2 = b + d, s3 = rot<INV>(b - d);
  a = s0 + s2; c = s0 - s2; b = s1 + s3; d = s1 - s3;
}
template <bool INV> DI void bf8(f32x2 (&v)[8]) {
  const float r = 0.70710678118654752f;
  f32x2 e0 = v[0], e1 = v[2], e2 = v[4], e3 = v[6], o0 = v[1], o1 = v[3], o2 = v[5], o3 = v[7];
  bf4<INV>(e0, e1, e2, e3);
  bf4<INV>(o0, o1, o2, o3);
  o1 = (o1 + rot<INV>(o1)) * r;
  o2 = rot<INV>(o2);
  o3 = (rot<INV>(o3) - o3) * r;
  v[0] = e0 + o0; v[4] = e0 - o0; v[1] = e1 + o1; v[5] = e1 - o1;
  v[2] = e2 + o2; v[6] = e2 - o2; v[3] = e3 + o3; v[7] = e3 - o3;
}
DI f32x2 twid(float rev) { return f32x2{__builtin_amdgcn_cosf(rev), -__builtin_amdgcn_sinf(rev)}; }
DI f32x2 unpk(unsigned u) { return f32x2{bflo(u), bfhi(u)}; }
DI int wt_pos(int u) { return (((u & 7) * 8 + ((u >> 3) & 7)) * 8 + ((u >> 6) & 7)) * 4 + (u >> 9); }

constexpr int P2_LDS = 8 * 288 * 8 + 16384;

__global__ __launch_bounds__(256, 2) void p2_kernel(const char* __restrict__ x, const char* __restrict__ wo,
                                                    const char* __restrict__ gate_w, char* __restrict__ ws) {
  __shared__ __attribute__((aligned(16))) char sm[P2_LDS];
  const int tid = threadIdx.x, lane = tid & 63;
  const int w = __builtin_amdgcn_readfirstlane(tid >> 6);
  if (blockIdx.x < 256) {
    const int k = blockIdx.x;
    const u32x4* xp = (const u32x4*)(ws + WS_XT + (size_t)k * 8192 + tid * 32);
    const u32x4* wp = (const u32x4*)(ws + WS_WT + (size_t)k * 8192 + tid * 32);
    const u32x4 x0 = xp[0], x1 = xp[1], w0 = wp[0], w1 = wp[1];
    f32x2 t1[7], t2[7], t3[7];
    {
      const float a1 = (float)tid * (1.f / 2048.f), a2 = (float)(tid & 31) * (1.f / 256.f), a3 = (float)(tid & 3) * (1.f / 32.f);
#pragma unroll
      for (int j = 1; j < 8; ++j) { t1[j - 1] = twid(a1 * j); t2[j - 1] = twid(a2 * j); t3[j - 1] = twid(a3 * j); }
    }
    f32x2* L = (f32x2*)sm;
    f32x2* Lg = L + (tid >> 5) * 288;
    const int t1i = tid & 31, k1 = t1i >> 2, q = tid & 3;
    f32x2 v[8] = {unpk(x0.x), unpk(x0.y), unpk(x0.z), unpk(x0.w), unpk(x1.x), unpk(x1.y), unpk(x1.z), unpk(x1.w)};
    bf8<false>(v);
#pragma unroll
    for (int j = 1; j < 8; ++j) v[j] = cmul(v[j], t1[j - 1]);
#pragma unroll
    for (int j = 0; j < 8; ++j) L[j * 288 + tid] = v[j];
    __syncthreads();
#pragma unroll
    for (int j = 0; j < 8; ++j) v[j] = Lg[t1i + 32 * j];
    bf8<false>(v);
#pragma unroll
    for (int j = 1; j < 8; ++j) v[j] = cmul(v[j], t2[j - 1]);
    __builtin_amdgcn_wave_barrier();
#pragma unroll
    for (int j = 0; j < 8; ++j) Lg[j * 36 + t1i] = v[j];
    __builtin_amdgcn_wave_barrier();
#pragma unroll
    for (int j = 0; j < 8; ++j) v[j] = Lg[k1 * 36 + q + 4 * j];
    bf8<false>(v);
#pragma unroll
    for (int j = 1; j < 8; ++j) v[j] = cmul(v[j], t3[j - 1]);
    __builtin_amdgcn_wave_barrier();
#pragma unroll
    for (int j = 0; j < 8; ++j) Lg[(j >> 1) * 65 + (j & 1) * 32 + t1i] = v[j];
    __builtin_amdgcn_wave_barrier();
#pragma unroll
    for (int j = 0; j < 8; ++j) v[j] = Lg[q * 65 + (j >> 2) * 32 + 4 * k1 + (j & 3)];
    bf4<false>(v[0], v[1], v[2], v[3]);
    bf4<false>(v[4], v[5], v[6], v[7]);
    const unsigned wv[8] = {w0.x, w0.y, w0.z, w0.w, w1.x, w1.y, w1.z, w1.w};
    if (k != 0) {
#pragma unroll
      for (int j = 0; j < 8; ++j) v[j] = cmul(v[j], unpk(wv[j]));
    } else {
      f32x2* E = (f32x2*)(sm + 8 * 288 * 8);
      const unsigned* wt0 = (const unsigned*)(ws + WS_WT);
      const unsigned* wtn = wt0 + 256 * 2048;
      const int ub = (tid >> 5) + 8 * k1 + 128 * q;
#pragma unroll
      for (int j = 0; j < 8; ++j) E[ub + 64 * (j >> 2) + 512 * (j & 3)] = v[j];
      __syncthreads();
#pragma unroll
      for (int j = 0; j < 8; ++j) {
        const int u = ub + 64 * (j >> 2) + 512 * (j & 3), un = (2048 - u) & 2047, pn = wt_pos(un);
        const f32x2 cn = E[un];
        const f32x2 a0 = unpk(wv[j]), a1 = unpk(wt0[pn]), b0 = unpk(wtn[tid * 8 + j]), b1 = unpk(wtn[pn]);
        const f32x2 s0 = f32x2{a0.x + a1.x, a0.y - a1.y}, sn = f32x2{b0.x + b1.x, b0.y - b1.y};
        const f32x2 A = (s0 + sn) * 0.25f, Bc = (s0 - sn) * 0.25f;
        v[j] = cmul(v[j], A) + cmul(f32x2{cn.x, -cn.y}, Bc);
      }
    }
    bf4<true>(v[0], v[1], v[2], v[3]);
    bf4<true>(v[4], v[5], v[6], v[7]);
    __builtin_amdgcn_wave_barrier();
#pragma unroll
    for (int j = 0; j < 8; ++j) Lg[q * 65 + (j >> 2) * 32 + 4 * k1 + (j & 3)] = v[j];
    __builtin_amdgcn_wave_barrier();
#pragma unroll
    for (int j = 0; j < 8; ++j) v[j] = Lg[(j >> 1) * 65 + (j & 1) * 32 + t1i];
#pragma unroll
    for (int j = 1; j < 8; ++j) v[j] = cmulc(v[j], t3[j - 1]);
    bf8<true>(v);
    __builtin_amdgcn_wave_barrier();
#pragma unroll
    for (int j = 0; j < 8; ++j) Lg[k1 * 36 + q + 4 * j] = v[j];
    __builtin_amdgcn_wave_barrier();
#pragma unroll
    for (int j = 0; j < 8; ++j) v[j] = Lg[j * 36 + t1i];
#pragma unroll
    for (int j = 1; j < 8; ++j) v[j] = cmulc(v[j], t2[j - 1]);
    bf8<true>(v);
    __builtin_amdgcn_wave_barrier();
#pragma unroll
    for (int j = 0; j < 8; ++j) Lg[t1i + 32 * j] = v[j];
    __syncthreads();
#pragma unroll
    for (int j = 0; j < 8; ++j) v[j] = L[j * 288 + tid];
#pragma unroll
    for (int j = 1; j < 8; ++j) v[j] = cmulc(v[j], t1[j - 1]);
    bf8<true>(v);
    u32x4* zp = (u32x4*)(ws + WS_Z + (size_t)k * 8192 + tid * 32);
    zp[0] = u32x4{pk2(v[0].x, v[0].y), pk2(v[1].x, v[1].y), pk2(v[2].x, v[2].y), pk2(v[3].x, v[3].y)};
    zp[1] = u32x4{pk2(v[4].x, v[4].y), pk2(v[5].x, v[5].y), pk2(v[6].x, v[6].y), pk2(v[7].x, v[7].y)};
  } else {
    const int ob = blockIdx.x - 256, mt = ob & 15, hp = ob >> 4;
    const int c = lane & 15, g = lane >> 4;
    f32x4 s[4] = {{0, 0, 0, 0}, {0, 0, 0, 0}, {0, 0, 0, 0}, {0, 0, 0, 0}};
#pragma unroll
    for (int i = 0; i < 4; ++i)
#pragma unroll
      for (int n = 0; n < 2; ++n) {
        const int rec = (4 * w + i) * 32 + 2 * hp + n;
        s[n] += *(const f32x4*)(ws + WS_KVP + ((size_t)rec * 64 + lane) * 16);
        s[2 + n] += *(const f32x4*)(ws + WS_ZP + ((size_t)rec * 4 + g) * 16);
      }
    const int B = 2 * mt + (w >> 1), tp = w & 1;
    u32x4 pq[2];
#pragma unroll
    for (int n = 0; n < 2; ++n) pq[n] = *(const u32x4*)(ws + WS_PQ + ((((size_t)(2 * hp + n) * 32 + B) * 2 + tp) * 64 + lane) * 16);
    const u32x4 gw = *(const u32x4*)(gate_w + lane * 16);
    u32x4 xr[2];
#pragma unroll
    for (int i = 0; i < 2; ++i) xr[i] = *(const u32x4*)(x + (size_t)(ob * 8 + w * 2 + i) * 1024 + lane * 16);
    unsigned wg[4];
    {
      const int f = 2 * ob + (tid >> 7), l2 = (tid >> 1) & 63, hf = tid & 1;
      const int n3 = f >> 6, st = (f >> 2) & 15, u = f & 3, c2 = l2 & 15, g2 = l2 >> 4;
      const int d = 256 * (u >> 1) + 32 * n3 + 8 * (c2 >> 2) + 4 * (u & 1) + (c2 & 3);
      const int kk = (2 * st + hf) * 16 + 4 * g2;
#pragma unroll
      for (int j = 0; j < 4; ++j) wg[j] = *(const unsigned short*)(wo + ((kk + j) * 512 + d) * 2);
    }
    f32x4* R = (f32x4*)sm;
#pragma unroll
    for (int i = 0; i < 4; ++i) R[(w * 4 + i) * 64 + lane] = s[i];
    __syncthreads();
    s16x4 fa[4];
#pragma unroll
    for (int i = 0; i < 4; ++i) {
      const f32x4 t = (R[i * 64 + lane] + R[(4 + i) * 64 + lane]) + (R[(8 + i) * 64 + lane] + R[(12 + i) * 64 + lane]);
      fa[i] = __builtin_bit_cast(s16x4, u32x2{pk2(t[0], t[1]), pk2(t[2], t[3])});
    }
#pragma unroll
    for (int tt = 0; tt < 2; ++tt) {
      unsigned o[4];
#pragma unroll
      for (int n = 0; n < 2; ++n) {
        const s16x4 b = __builtin_bit_cast(s16x4, tt ? u32x2{pq[n].z, pq[n].w} : u32x2{pq[n].x, pq[n].y});
        const f32x4 zero = {0, 0, 0, 0};
        const f32x4 num = mfma16(fa[n], b, zero), den = mfma16(fa[2 + n], b, zero);
        const float r = 1.f / (den[0] + 1e-6f);
        o[2 * n] = pk2(num[0] * r, num[1] * r);
        o[2 * n + 1] = pk2(num[2] * r, num[3] * r);
      }
      *(u32x4*)(ws + WS_O + ((((size_t)B * 16 + hp) * 4 + 2 * tp + tt) * 64 + lane) * 16) = u32x4{o[0], o[1], o[2], o[3]};
    }
    *(u32x2*)(ws + WS_WOF + (size_t)ob * 2048 + tid * 8) = u32x2{wg[0] | (wg[1] << 16), wg[2] | (wg[3] << 16)};
#pragma unroll
    for (int i = 0; i < 2; ++i) {
      float a = 0.f;
#pragma unroll
      for (int j = 0; j < 4; ++j) a += bflo(xr[i][j]) * bflo(gw[j]) + bfhi(xr[i][j]) * bfhi(gw[j]);
#pragma unroll
      for (int m = 32; m >= 1; m >>= 1) a += __shfl_xor(a, m);
      if (lane == 0) ((float*)(ws + WS_G))[ob * 8 + w * 2 + i] = 1.f / (1.f + __expf(-a));
    }
  }
}

__global__ __launch_bounds__(256, 1) void p3_kernel(const char* __restrict__ ws, char* __restrict__ out) {
  __shared__ __attribute__((aligned(16))) f32x4 R[48 * 64];
  const int tid = threadIdx.x, lane = tid & 63;
  const int w = __builtin_amdgcn_readfirstlane(tid >> 6);
  const int bid = blockIdx.x, xcd = bid & 7, idx = bid >> 3;
  const int B = xcd * 4 + (idx & 3), n3 = idx >> 2;
  const int c = lane & 15, g = lane >> 4;
  const int p = 64 * B + 4 * c + w, srow = (p >> 3) + 256 * (p & 7);
  const float gate = ((const float*)(ws + WS_G))[srow];
  if (w < 2) {
    const s16x8* op = (const s16x8*)(ws + WS_O + ((size_t)(B * 16 + 8 * w) * 4 * 64 + lane) * 16);
    const s16x8* wp = (const s16x8*)(ws + WS_WOF + ((size_t)(n3 * 16 + 8 * w) * 4 * 64 + lane) * 16);
    s16x8 ob[8][4], wa[8][4];
#pragma unroll
    for (int s = 0; s < 8; ++s)
#pragma unroll
      for (int i = 0; i < 4; ++i) { ob[s][i] = op[(s * 4 + i) * 64]; wa[s][i] = wp[(s * 4 + i) * 64]; }
    __builtin_amdgcn_sched_barrier(0);
    f32x4 acc[4][4];
#pragma unroll
    for (int u = 0; u < 4; ++u)
#pragma unroll
      for (int t = 0; t < 4; ++t) acc[u][t] = f32x4{0, 0, 0, 0};
#pragma unroll
    for (int s = 0; s < 8; ++s)
#pragma unroll
      for (int u = 0; u < 4; ++u)
#pragma unroll
        for (int t = 0; t < 4; ++t) acc[u][t] = mfma32(wa[s][u], ob[s][t], acc[u][t]);
#pragma unroll
    for (int u = 0; u < 4; ++u)
#pragma unroll
      for (int t = 0; t < 4; ++t) R[(t * 12 + w * 4 + u) * 64 + lane] = acc[u][t];
  } else {
    const int par = w - 2;
    const char* zp = ws + WS_Z + ((size_t)(8 * g + par) * 2048 + 64 * B + 4 * c) * 4;
    u32x4 z[8][4];
#pragma unroll
    for (int s = 0; s < 8; ++s)
#pragma unroll
      for (int i = 0; i < 4; ++i) z[s][i] = *(const u32x4*)(zp + (size_t)(32 * s + 2 * i) * 8192);
    __builtin_amdgcn_sched_barrier(0);
    f32x4 acc[2][4];
#pragma unroll
    for (int u = 0; u < 2; ++u)
#pragma unroll
      for (int t = 0; t < 4; ++t) acc[u][t] = f32x4{0, 0, 0, 0};
    const int d0 = 32 * n3 + 8 * (c >> 2) + (c & 3);
    const float kb = (float)(8 * g + par);
#pragma unroll
    for (int s = 0; s < 8; ++s) {
      s16x8 ga[2];
#pragma unroll
      for (int u = 0; u < 2; ++u) {
        const float df = (float)(d0 + 4 * u) * (1.f / 512.f);
        unsigned e[4];
#pragma unroll
        for (int i = 0; i < 4; ++i) {
          const float a = __builtin_amdgcn_fractf((kb + (float)(32 * s + 2 * i)) * df);
          float cs = __builtin_amdgcn_cosf(a), sn = -__builtin_amdgcn_sinf(a);
          if (s == 0 && i == 0 && kb == 0.f) { cs = 0.5f; sn = (c & 1) ? -0.5f : 0.5f; }
          e[i] = pk2(cs, sn);
        }
        ga[u] = __builtin_bit_cast(s16x8, u32x4{e[0], e[1], e[2], e[3]});
      }
#pragma unroll
      for (int t = 0; t < 4; ++t) {
        const s16x8 zb = __builtin_bit_cast(s16x8, u32x4{z[s][0][t], z[s][1][t], z[s][2][t], z[s][3][t]});
#pragma unroll
        for (int u = 0; u < 2; ++u) acc[u][t] = mfma32(ga[u], zb, acc[u][t]);
      }
    }
#pragma unroll
    for (int u = 0; u < 2; ++u)
#pragma unroll
      for (int t = 0; t < 4; ++t) R[(t * 12 + 8 + par * 2 + u) * 64 + lane] = acc[u][t];
  }
  __syncthreads();
  const f32x4* Rw = R + w * 12 * 64 + lane;
  const float sc = (1.f - gate) * (2.f / (2048.f * 512.f));
#pragma unroll
  for (int hf = 0; hf < 2; ++hf) {
    unsigned o[4];
#pragma unroll
    for (int u = 0; u < 2; ++u) {
      const f32x4 a = Rw[(2 * hf + u) * 64] + Rw[(4 + 2 * hf + u) * 64];
      const f32x4 e = Rw[(8 + u) * 64], od = Rw[(10 + u) * 64];
      const f32x4 b = hf ? e - od : e + od;
      const f32x4 r = a * gate + b * sc;
      o[2 * u] = pk2(r[0], r[1]);
      o[2 * u + 1] = pk2(r[2], r[3]);
    }
    *(u32x4*)(out + ((size_t)srow * 512 + 256 * hf + 32 * n3 + 8 * g) * 2) = u32x4{o[0], o[1], o[2], o[3]};
  }
}

extern "C" void kernel_launch(void* const* d_in, const int* in_sizes, int n_in, void* d_out, int out_size,
                              void* d_ws, size_t ws_size, hipStream_t stream) {
  const char* x = (const char*)d_in[0];
  const char* wqkv = (const char*)d_in[1];
  const char* wo = (const char*)d_in[2];
  const char* w_r = (const char*)d_in[4];
  const char* w_i = (const char*)d_in[5];
  const char* gw = (const char*)d_in[6];
  char* ws = (char*)d_ws;
  (void)hipFuncSetAttribute((const void*)p1_kernel, hipFuncAttributeMaxDynamicSharedMemorySize, P1_LDS);
  p1_kernel<<<256, 512, P1_LDS, stream>>>(x, wqkv, w_r, w_i, ws);
  p2_kernel<<<512, 256, 0, stream>>>(x, wo, gw, ws);
  p3_kernel<<<256, 256, 0, stream>>>(ws, (char*)d_out);
}
```

```cpp
#include <hip/hip_runtime.h>
#include <hip/hip_bf16.h>
#include <stdint.h>

typedef short s16x4 __attribute__((ext_vector_type(4)));
typedef short s16x8 __attribute__((ext_vector_type(8)));
typedef float f32x2 __attribute__((ext_vector_type(2)));
typedef float f32x4 __attribute__((ext_vector_type(4)));
typedef unsigned u32x2 __attribute__((ext_vector_type(2)));
typedef unsigned u32x4 __attribute__((ext_vector_type(4)));
typedef __bf16 bf16x2 __attribute__((ext_vector_type(2)));
#define DI __device__ __forceinline__
#define LDS_AS __attribute__((address_space(3)))

DI unsigned pk2(float a, float b) {
  f32x2 v = {a, b};
  return __builtin_bit_cast(unsigned, __builtin_convertvector(v, bf16x2));
}
DI float bflo(unsigned u) { return __builtin_bit_cast(float, u << 16); }
DI float bfhi(unsigned u) { return __builtin_bit_cast(float, u & 0xffff0000u); }
DI f32x4 mfma32(s16x8 a, s16x8 b, f32x4 c) { return __builtin_amdgcn_mfma_f32_16x16x32_bf16(a, b, c, 0, 0, 0); }
DI f32x4 mfma16(s16x4 a, s16x4 b, f32x4 c) { return __builtin_amdgcn_mfma_f32_16x16x16bf16_1k(a, b, c, 0, 0, 0); }
DI float phi(float t) {
  return fmaxf(t, 0.f) + __builtin_amdgcn_exp2f(fminf(t * 1.4426950408889634f, 0.f)) + 1e-6f;
}

#define ST(kern, i)
#define ST_DECL
#define ST_FLUSH(kern)
constexpr size_t WS_XT = 0;
constexpr size_t WS_WT = WS_XT + 256 * 2048 * 4;
constexpr size_t WS_PQ = WS_WT + 257 * 2048 * 4;
constexpr size_t WS_KVP = WS_PQ + 2048 * 512 * 2;
constexpr size_t WS_ZP = WS_KVP + 16 * 32 * 1024;
constexpr size_t WS_Z = WS_ZP + 16 * 32 * 64;
constexpr size_t WS_O = WS_Z + 256 * 2048 * 4;
constexpr size_t WS_WOF = WS_O + 2048 * 512 * 2;
constexpr size_t WS_G = WS_WOF + 512 * 512 * 2;

constexpr int STG = 28672;
constexpr int L_F = 4 * STG;
constexpr int L_S = L_F + 32768;
constexpr int P1_LDS = L_S + 16384;

template <bool V> struct BoolC { static constexpr bool value = V; };
template <bool SWAP>
DI void p1_step(const char* sm, unsigned aoff, unsigned b0off, unsigned b1off, f32x4 (&acc)[4][2]) {
#pragma unroll
  for (int ks = 0; ks < 2; ++ks) {
    s16x8 a[4], b[2];
#pragma unroll
    for (int t = 0; t < 4; ++t) a[t] = *(const s16x8*)(sm + ((aoff ^ (ks * 64)) + t * 2048));
    {
      s16x4 l0 = __builtin_amdgcn_ds_read_tr16_b64_v4i16((LDS_AS s16x4*)(sm + b0off + ks * 2048));
      s16x4 h0 = __builtin_amdgcn_ds_read_tr16_b64_v4i16((LDS_AS s16x4*)(sm + b0off + ks * 2048 + 256));
      s16x4 l1 = __builtin_amdgcn_ds_read_tr16_b64_v4i16((LDS_AS s16x4*)(sm + b1off + ks * 2048));
      s16x4 h1 = __builtin_amdgcn_ds_read_tr16_b64_v4i16((LDS_AS s16x4*)(sm + b1off + ks * 2048 + 256));
      b[0] = __builtin_shufflevector(l0, h0, 0, 1, 2, 3, 4, 5, 6, 7);
      b[1] = __builtin_shufflevector(l1, h1, 0, 1, 2, 3, 4, 5, 6, 7);
    }
#pragma unroll
    for (int t = 0; t < 4; ++t)
#pragma unroll
      for (int n = 0; n < 2; ++n)
        acc[t][n] = SWAP ? mfma32(b[n], a[t], acc[t][n]) : mfma32(a[t], b[n], acc[t][n]);
  }
}

__global__ __launch_bounds__(512, 2) void p1_kernel(const char* __restrict__ x, const char* __restrict__ wqkv,
                                                    const char* __restrict__ w_r, const char* __restrict__ w_i,
                                                    char* __restrict__ ws) {
  extern __shared__ __attribute__((aligned(16))) char sm[];
  asm volatile("" :: "s"(x), "s"(wqkv), "s"(w_r), "s"(w_i), "s"(ws));
  const int tid = threadIdx.x, lane = tid & 63;
  const int w = __builtin_amdgcn_readfirstlane(tid >> 6);
  const int bid = blockIdx.x, xcd = bid & 7, idx = bid >> 3;
  const int mt = (xcd & 3) * 4 + (idx & 3), nt = (xcd >> 2) * 8 + (idx >> 2);

  ST_DECL ST(0, 0);
  const int j6 = bid & 63, kb = bid >> 6;
  u32x2 wrv, wiv;
  {
    const int pos = tid >> 4, kk = tid & 15;
    const int u = (j6 >> 3) + 8 * (j6 & 7) + 64 * (pos >> 2) + 512 * (pos & 3);
    const unsigned off = (unsigned)(u * 257 + kb * 64 + kk * 4) * 2u;
    asm volatile("global_load_dwordx2 %0, %2, %3\n\tglobal_load_dwordx2 %1, %2, %4"
                 : "=&v"(wrv), "=&v"(wiv) : "v"(off), "s"(w_r), "s"(w_i) : "memory");
  }
  unsigned voff[4];
  const char* sbase;
  unsigned sstride;
  if (w < 4) {
#pragma unroll
    for (int i = 0; i < 4; ++i) {
      const int rho = (w * 4 + i) * 8 + (lane >> 3);
      const int ii = (rho & 64) + 4 * (rho & 15) + ((rho >> 4) & 3);
      const int s = 16 * mt + (ii >> 3) + 256 * (ii & 7);
      voff[i] = (unsigned)(s * 1024 + (((lane & 7) ^ ((rho >> 1) & 7)) << 4) + 4096 - i * 1024);
    }
    sbase = x - 4096;
    sstride = 128;
  } else {
#pragma unroll
    for (int i = 0; i < 4; ++i) {
      const int kr = i * 16 + (lane >> 2);
      voff[i] = (unsigned)(kr * 3072 + (((lane & 3) ^ ((kr >> 2) & 2)) << 4) + 4096 - i * 1024);
    }
    sbase = wqkv + (w - 4) * 1024 + nt * 64 - 4096;
    sstride = 64 * 3072;
  }
  const unsigned lds0 = (unsigned)(size_t)(LDS_AS char*)sm + w * 4096;
  auto issue = [&](int kc) {
    if (w < 7) {
      const char* sb = sbase + (unsigned)kc * sstride;
      const unsigned dst = lds0 + (kc & 3) * STG;
      unsigned keep;
      asm volatile("s_mov_b32 %0, m0\n\ts_mov_b32 m0, %6\n\ts_nop 0\n\t"
                   "global_load_lds_dwordx4 %1, %5\n\t"
                   "global_load_lds_dwordx4 %2, %5 offset:1024\n\t"
                   "global_load_lds_dwordx4 %3, %5 offset:2048\n\t"
                   "global_load_lds_dwordx4 %4, %5 offset:3072\n\t"
                   "s_mov_b32 m0, %0"
                   : "=&s"(keep) : "v"(voff[0]), "v"(voff[1]), "v"(voff[2]), "v"(voff[3]), "s"(sb), "s"(dst) : "memory");
    }
  };
  issue(0); issue(1); issue(2); issue(3);
  ST(0, 1);

  {
    const int slot = tid & 3;
#pragma unroll
    for (int i = 0; i < 4; ++i) {
      const int d = (tid >> 2) + 128 * i;
      const int ch = slot ^ ((d >> 2) & 2);
      const float df = (float)d * (1.f / 512.f);
      const float k0 = (float)(16 * nt + 8 * (ch & 1));
      const float ph = (ch & 2) ? 0.25f : 0.f;
      float v[8];
#pragma unroll
      for (int j = 0; j < 8; ++j) {
        float a = __builtin_fmaf(k0 + (float)j, df, ph);
        if (j == 0 && nt == 0 && ch == 2) a = 256.f * df;
        v[j] = __builtin_amdgcn_cosf(__builtin_amdgcn_fractf(a));
      }
      u32x4 o = {pk2(v[0], v[1]), pk2(v[2], v[3]), pk2(v[4], v[5]), pk2(v[6], v[7])};
      *(u32x4*)(sm + L_F + d * 64 + slot * 16) = o;
    }
  }
  ST(0, 2);
  if (w == 7 && kb == 3 && lane < 32) {
    const int u = (j6 >> 3) + 8 * (j6 & 7) + 64 * (lane >> 2) + 512 * (lane & 3);
    const unsigned a = *(const unsigned short*)(w_r + (u * 257 + 256) * 2);
    const unsigned b = *(const unsigned short*)(w_i + (u * 257 + 256) * 2);
    *(unsigned*)(ws + WS_WT + (256 * 2048 + j6 * 32 + lane) * 4) = a | (b << 16);
  }
  if (w == 7) asm volatile("s_waitcnt vmcnt(0)" ::: "memory");
  asm volatile("s_waitcnt vmcnt(12)" : "+v"(wrv), "+v"(wiv) : : "memory");
  {
    const int pos = tid >> 4, kk = tid & 15;
    unsigned* sc = (unsigned*)(sm + L_S) + kk * 128 + (pos ^ (4 * (kk & 7)));
    sc[0] = __builtin_amdgcn_perm(wiv.x, wrv.x, 0x05040100u);
    sc[32] = __builtin_amdgcn_perm(wiv.x, wrv.x, 0x07060302u);
    sc[64] = __builtin_amdgcn_perm(wiv.y, wrv.y, 0x05040100u);
    sc[96] = __builtin_amdgcn_perm(wiv.y, wrv.y, 0x07060302u);
  }

  ST(0, 3);
  const int wm = w >> 2;
  const int role = (w & 3) ^ ((w >> 2) & (w >> 1) & 1);
  const int c = lane & 15, g = lane >> 4;
  const unsigned aoff = (unsigned)((64 * wm + c) * 128 + ((g ^ ((c >> 1) & 7)) << 4));
  const unsigned brow = (unsigned)((8 * g + ((lane & 15) >> 2)) * 64 + 8 * (lane & 3));
  unsigned b0, b1;
  {
    const unsigned h0 = (unsigned)((g & 1) << 5), h1 = h0 ^ 32u;
    if (role < 2) { const unsigned hh = role ? h1 : h0; b0 = 16384 + 4096 + brow + hh; b1 = 16384 + 8192 + brow + hh; }
    else if (role == 2) { b0 = 16384 + brow + h0; b1 = 16384 + brow + h1; }
    else { b0 = L_F + brow + h0; b1 = L_F + brow + h1; }
  }
  f32x4 acc[4][2];
#pragma unroll
  for (int t = 0; t < 4; ++t) { acc[t][0] = f32x4{0, 0, 0, 0}; acc[t][1] = f32x4{0, 0, 0, 0}; }

  auto mainloop = [&](auto swap_tag) {
#pragma unroll 1
    for (int kc = 0; kc < 8; ++kc) {
      if (kc == 7) asm volatile("s_waitcnt vmcnt(0)" ::: "memory");
      else if (kc == 6) asm volatile("s_waitcnt vmcnt(4)" ::: "memory");
      else if (kc > 0) asm volatile("s_waitcnt vmcnt(8)" ::: "memory");
      asm volatile("s_waitcnt lgkmcnt(0)" ::: "memory");
      __builtin_amdgcn_s_barrier();
      asm volatile("" ::: "memory");
      ST(0, 4 + kc);
      if (kc == 0) {
        const int k = tid >> 3, mq = tid & 7;
        const u32x4 v = *(const u32x4*)(sm + L_S + k * 128 + ((mq ^ ((k >> 2) & 7)) << 4));
        *(u32x4*)(ws + WS_WT + ((size_t)(kb * 64 + k) * 2048 + j6 * 32 + mq * 4) * 4) = v;
      } else if (kc < 5) issue(kc + 3);
      const unsigned so = (unsigned)((kc & 3) * STG);
      const unsigned bo = role == 3 ? (unsigned)(kc * 4096) : so;
      p1_step<decltype(swap_tag)::value>(sm, aoff + so, b0 + bo, b1 + bo, acc);
    }
  };
  if (role == 2) mainloop(BoolC<true>{});
  else mainloop(BoolC<false>{});

  ST(0, 12);
  const int B = 2 * mt + wm;
  f32x4 kv = {0, 0, 0, 0}, zz = {0, 0, 0, 0};
  if (role == 2) {
#pragma unroll
    for (int n = 0; n < 2; ++n)
#pragma unroll
      for (int tp = 0; tp < 2; ++tp) {
        const f32x4 a = acc[2 * tp][n], b = acc[2 * tp + 1][n];
        u32x4 o = {pk2(phi(a[0]), phi(a[1])), pk2(phi(a[2]), phi(a[3])), pk2(phi(b[0]), phi(b[1])), pk2(phi(b[2]), phi(b[3]))};
        *(u32x4*)(ws + WS_PQ + ((((size_t)(2 * nt + n) * 32 + B) * 2 + tp) * 64 + lane) * 16) = o;
      }
  } else if (role == 3) {
    char* dst = ws + WS_XT + ((size_t)(16 * nt + c) * 2048 + 64 * B + 16 * g) * 4;
#pragma unroll
    for (int r = 0; r < 4; ++r) {
      u32x4 o = {pk2(acc[0][0][r], acc[0][1][r]), pk2(acc[1][0][r], acc[1][1][r]), pk2(acc[2][0][r], acc[2][1][r]), pk2(acc[3][0][r], acc[3][1][r])};
      *(u32x4*)(dst + r * 16) = o;
    }
  } else {
    const s16x8 ones = {0x3f80, 0x3f80, 0x3f80, 0x3f80, 0x3f80, 0x3f80, 0x3f80, 0x3f80};
#pragma unroll
    for (int tp = 0; tp < 2; ++tp) {
      const f32x4 a = acc[2 * tp][0], b = acc[2 * tp + 1][0], va = acc[2 * tp][1], vb = acc[2 * tp + 1][1];
      u32x4 pk = {pk2(phi(a[0]), phi(a[1])), pk2(phi(a[2]), phi(a[3])), pk2(phi(b[0]), phi(b[1])), pk2(phi(b[2]), phi(b[3]))};
      u32x4 vv = {pk2(va[0], va[1]), pk2(va[2], va[3]), pk2(vb[0], vb[1]), pk2(vb[2], vb[3])};
      kv = mfma32(__builtin_bit_cast(s16x8, pk), __builtin_bit_cast(s16x8, vv), kv);
      zz = mfma32(__builtin_bit_cast(s16x8, pk), ones, zz);
    }
    if (wm == 1) {
      *(f32x4*)(sm + L_S + 8192 + role * 2048 + lane * 16) = kv;
      *(f32x4*)(sm + L_S + 8192 + role * 2048 + 1024 + lane * 16) = zz;
    }
  }
  asm volatile("s_waitcnt lgkmcnt(0)" ::: "memory");
  __builtin_amdgcn_s_barrier();
  asm volatile("" ::: "memory");
  if (role < 2 && wm == 0) {
    kv += *(const f32x4*)(sm + L_S + 8192 + role * 2048 + lane * 16);
    zz += *(const f32x4*)(sm + L_S + 8192 + role * 2048 + 1024 + lane * 16);
    const int h = 2 * nt + role;
    *(f32x4*)(ws + WS_KVP + ((size_t)(mt * 32 + h) * 64 + lane) * 16) = kv;
    if (c == 0) *(f32x4*)(ws + WS_ZP + ((size_t)(mt * 32 + h) * 4 + g) * 16) = zz;
  }
  ST(0, 13);
  ST(0, 14);
  ST_FLUSH(0);
}

DI f32x2 cmul(f32x2 a, f32x2 w) {
  f32x2 t = a.yy * f32x2{-w.y, w.x};
  return __builtin_elementwise_fma(a.xx, w, t);
}
DI f32x2 cmulc(f32x2 a, f32x2 w) {
  f32x2 t = a.yy * f32x2{w.y, w.x};
  return __builtin_elementwise_fma(a.xx, f32x2{w.x, -w.y}, t);
}
template <bool INV> DI f32x2 rot(f32x2 a) { return INV ? f32x2{-a.y, a.x} : f32x2{a.y, -a.x}; }
template <bool INV> DI void bf4(f32x2& a, f32x2& b, f32x2& c, f32x2& d) {
  f32x2 s0 = a + c, s1 = a - c, s2 = b + d, s3 = rot<INV>(b - d);
  a = s0 + s2; c = s0 - s2; b = s1 + s3; d = s1 - s3;
}
template <bool INV> DI void bf8(f32x2 (&v)[8]) {
  const float r = 0.70710678118654752f;
  f32x2 e0 = v[0], e1 = v[2], e2 = v[4], e3 = v[6], o0 = v[1], o1 = v[3], o2 = v[5], o3 = v[7];
  bf4<INV>(e0, e1, e2, e3);
  bf4<INV>(o0, o1, o2, o3);
  o1 = (o1 + rot<INV>(o1)) * r;
  o2 = rot<INV>(o2);
  o3 = (rot<INV>(o3) - o3) * r;
  v[0] = e0 + o0; v[4] = e0 - o0; v[1] = e1 + o1; v[5] = e1 - o1;
  v[2] = e2 + o2; v[6] = e2 - o2; v[3] = e3 + o3; v[7] = e3 - o3;
}
DI f32x2 twid(float rev) { return f32x2{__builtin_amdgcn_cosf(rev), -__builtin_amdgcn_sinf(rev)}; }
DI f32x2 unpk(unsigned u) { return f32x2{bflo(u), bfhi(u)}; }
DI int wt_pos(int u) { return (((u & 7) * 8 + ((u >> 3) & 7)) * 8 + ((u >> 6) & 7)) * 4 + (u >> 9); }

constexpr int P2_LDS = 8 * 288 * 8 + 16384;

__global__ __launch_bounds__(256, 2) void p2_kernel(const char* __restrict__ x, const char* __restrict__ wo,
                                                    const char* __restrict__ gate_w, char* __restrict__ ws) {
  __shared__ __attribute__((aligned(16))) char sm[P2_LDS];
  const int tid = threadIdx.x, lane = tid & 63;
  const int w = __builtin_amdgcn_readfirstlane(tid >> 6);
  ST_DECL ST(1, 0);
  if (blockIdx.x < 256) {
    const int k = blockIdx.x;
    const u32x4* xp = (const u32x4*)(ws + WS_XT + (size_t)k * 8192 + tid * 32);
    const u32x4* wp = (const u32x4*)(ws + WS_WT + (size_t)k * 8192 + tid * 32);
    const u32x4 x0 = xp[0], x1 = xp[1], w0 = wp[0], w1 = wp[1];
    f32x2 t1[7], t2[7], t3[7];
    {
      const float a1 = (float)tid * (1.f / 2048.f), a2 = (float)(tid & 31) * (1.f / 256.f), a3 = (float)(tid & 3) * (1.f / 32.f);
#pragma unroll
      for (int j = 1; j < 8; ++j) { t1[j - 1] = twid(a1 * j); t2[j - 1] = twid(a2 * j); t3[j - 1] = twid(a3 * j); }
    }
    f32x2* L = (f32x2*)sm;
    f32x2* Lg = L + (tid >> 5) * 288;
    const int t1i = tid & 31, k1 = t1i >> 2, q = tid & 3;
    ST(1, 1);
    ST(1, 2);
    f32x2 v[8] = {unpk(x0.x), unpk(x0.y), unpk(x0.z), unpk(x0.w), unpk(x1.x), unpk(x1.y), unpk(x1.z), unpk(x1.w)};
    bf8<false>(v);
#pragma unroll
    for (int j = 1; j < 8; ++j) v[j] = cmul(v[j], t1[j - 1]);
#pragma unroll
    for (int j = 0; j < 8; ++j) L[j * 288 + tid] = v[j];
    __syncthreads();
#pragma unroll
    for (int j = 0; j < 8; ++j) v[j] = Lg[t1i + 32 * j];
    bf8<false>(v);
#pragma unroll
    for (int j = 1; j < 8; ++j) v[j] = cmul(v[j], t2[j - 1]);
    __builtin_amdgcn_wave_barrier();
#pragma unroll
    for (int j = 0; j < 8; ++j) Lg[j * 36 + t1i] = v[j];
    __builtin_amdgcn_wave_barrier();
#pragma unroll
    for (int j = 0; j < 8; ++j) v[j] = Lg[k1 * 36 + q + 4 * j];
    bf8<false>(v);
#pragma unroll
    for (int j = 1; j < 8; ++j) v[j] = cmul(v[j], t3[j - 1]);
    __builtin_amdgcn_wave_barrier();
#pragma unroll
    for (int j = 0; j < 8; ++j) Lg[(j >> 1) * 65 + (j & 1) * 32 + t1i] = v[j];
    __builtin_amdgcn_wave_barrier();
#pragma unroll
    for (int j = 0; j < 8; ++j) v[j] = Lg[q * 65 + (j >> 2) * 32 + 4 * k1 + (j & 3)];
    bf4<false>(v[0], v[1], v[2], v[3]);
    bf4<false>(v[4], v[5], v[6], v[7]);
    ST(1, 3);
    const unsigned wv[8] = {w0.x, w0.y, w0.z, w0.w, w1.x, w1.y, w1.z, w1.w};
    if (k != 0) {
#pragma unroll
      for (int j = 0; j < 8; ++j) v[j] = cmul(v[j], unpk(wv[j]));
    } else {
      f32x2* E = (f32x2*)(sm + 8 * 288 * 8);
      const unsigned* wt0 = (const unsigned*)(ws + WS_WT);
      const unsigned* wtn = wt0 + 256 * 2048;
      const int ub = (tid >> 5) + 8 * k1 + 128 * q;
#pragma unroll
      for (int j = 0; j < 8; ++j) E[ub + 64 * (j >> 2) + 512 * (j & 3)] = v[j];
      __syncthreads();
#pragma unroll
      for (int j = 0; j < 8; ++j) {
        const int u = ub + 64 * (j >> 2) + 512 * (j & 3), un = (2048 - u) & 2047, pn = wt_pos(un);
        const f32x2 cn = E[un];
        const f32x2 a0 = unpk(wv[j]), a1 = unpk(wt0[pn]), b0 = unpk(wtn[tid * 8 + j]), b1 = unpk(wtn[pn]);
        const f32x2 s0 = f32x2{a0.x + a1.x, a0.y - a1.y}, sn = f32x2{b0.x + b1.x, b0.y - b1.y};
        const f32x2 A = (s0 + sn) * 0.25f, Bc = (s0 - sn) * 0.25f;
        v[j] = cmul(v[j], A) + cmul(f32x2{cn.x, -cn.y}, Bc);
      }
    }
    bf4<true>(v[0], v[1], v[2], v[3]);
    bf4<true>(v[4], v[5], v[6], v[7]);
    __builtin_amdgcn_wave_barrier();
#pragma unroll
    for (int j = 0; j < 8; ++j) Lg[q * 65 + (j >> 2) * 32 + 4 * k1 + (j & 3)] = v[j];
    __builtin_amdgcn_wave_barrier();
#pragma unroll
    for (int j = 0; j < 8; ++j) v[j] = Lg[(j >> 1) * 65 + (j & 1) * 32 + t1i];
#pragma unroll
    for (int j = 1; j < 8; ++j) v[j] = cmulc(v[j], t3[j - 1]);
    bf8<true>(v);
    __builtin_amdgcn_wave_barrier();
#pragma unroll
    for (int j = 0; j < 8; ++j) Lg[k1 * 36 + q + 4 * j] = v[j];
    __builtin_amdgcn_wave_barrier();
#pragma unroll
    for (int j = 0; j < 8; ++j) v[j] = Lg[j * 36 + t1i];
#pragma unroll
    for (int j = 1; j < 8; ++j) v[j] = cmulc(v[j], t2[j - 1]);
    bf8<true>(v);
    __builtin_amdgcn_wave_barrier();
#pragma unroll
    for (int j = 0; j < 8; ++j) Lg[t1i + 32 * j] = v[j];
    __syncthreads();
#pragma unroll
    for (int j = 0; j < 8; ++j) v[j] = L[j * 288 + tid];
#pragma unroll
    for (int j = 1; j < 8; ++j) v[j] = cmulc(v[j], t1[j - 1]);
    bf8<true>(v);
    ST(1, 4);
    u32x4* zp = (u32x4*)(ws + WS_Z + (size_t)k * 8192 + tid * 32);
    zp[0] = u32x4{pk2(v[0].x, v[0].y), pk2(v[1].x, v[1].y), pk2(v[2].x, v[2].y), pk2(v[3].x, v[3].y)};
    zp[1] = u32x4{pk2(v[4].x, v[4].y), pk2(v[5].x, v[5].y), pk2(v[6].x, v[6].y), pk2(v[7].x, v[7].y)};
  } else {
    const int ob = blockIdx.x - 256, mt = ob & 15, hp = ob >> 4;
    const int c = lane & 15, g = lane >> 4;
    f32x4 s[4] = {{0, 0, 0, 0}, {0, 0, 0, 0}, {0, 0, 0, 0}, {0, 0, 0, 0}};
#pragma unroll
    for (int i = 0; i < 4; ++i)
#pragma unroll
      for (int n = 0; n < 2; ++n) {
        const int rec = (4 * w + i) * 32 + 2 * hp + n;
        s[n] += *(const f32x4*)(ws + WS_KVP + ((size_t)rec * 64 + lane) * 16);
        s[2 + n] += *(const f32x4*)(ws + WS_ZP + ((size_t)rec * 4 + g) * 16);
      }
    const int B = 2 * mt + (w >> 1), tp = w & 1;
    u32x4 pq[2];
#pragma unroll
    for (int n = 0; n < 2; ++n) pq[n] = *(const u32x4*)(ws + WS_PQ + ((((size_t)(2 * hp + n) * 32 + B) * 2 + tp) * 64 + lane) * 16);
    const u32x4 gw = *(const u32x4*)(gate_w + lane * 16);
    u32x4 xr[2];
#pragma unroll
    for (int i = 0; i < 2; ++i) xr[i] = *(const u32x4*)(x + (size_t)(ob * 8 + w * 2 + i) * 1024 + lane * 16);
    unsigned wg[4];
    {
      const int f = 2 * ob + (tid >> 7), l2 = (tid >> 1) & 63, hf = tid & 1;
      const int n3 = f >> 6, st = (f >> 2) & 15, u = f & 3, c2 = l2 & 15, g2 = l2 >> 4;
      const int d = 256 * (u >> 1) + 32 * n3 + 8 * (c2 >> 2) + 4 * (u & 1) + (c2 & 3);
      const int kk = (2 * st + hf) * 16 + 4 * g2;
#pragma unroll
      for (int j = 0; j < 4; ++j) wg[j] = *(const unsigned short*)(wo + ((kk + j) * 512 + d) * 2);
    }
    ST(1, 1);
    ST(1, 2);
    f32x4* R = (f32x4*)sm;
#pragma unroll
    for (int i = 0; i < 4; ++i) R[(w * 4 + i) * 64 + lane] = s[i];
    __syncthreads();
    s16x4 fa[4];
#pragma unroll
    for (int i = 0; i < 4; ++i) {
      const f32x4 t = (R[i * 64 + lane] + R[(4 + i) * 64 + lane]) + (R[(8 + i) * 64 + lane] + R[(12 + i) * 64 + lane]);
      fa[i] = __builtin_bit_cast(s16x4, u32x2{pk2(t[0], t[1]), pk2(t[2], t[3])});
    }
#pragma unroll
    for (int tt = 0; tt < 2; ++tt) {
      unsigned o[4];
#pragma unroll
      for (int n = 0; n < 2; ++n) {
        const s16x4 b = __builtin_bit_cast(s16x4, tt ? u32x2{pq[n].z, pq[n].w} : u32x2{pq[n].x, pq[n].y});
        const f32x4 zero = {0, 0, 0, 0};
        const f32x4 num = mfma16(fa[n], b, zero), den = mfma16(fa[2 + n], b, zero);
        const float r = 1.f / (den[0] + 1e-6f);
        o[2 * n] = pk2(num[0] * r, num[1] * r);
        o[2 * n + 1] = pk2(num[2] * r, num[3] * r);
      }
      *(u32x4*)(ws + WS_O + ((((size_t)B * 16 + hp) * 4 + 2 * tp + tt) * 64 + lane) * 16) = u32x4{o[0], o[1], o[2], o[3]};
    }
    *(u32x2*)(ws + WS_WOF + (size_t)ob * 2048 + tid * 8) = u32x2{wg[0] | (wg[1] << 16), wg[2] | (wg[3] << 16)};
#pragma unroll
    for (int i = 0; i < 2; ++i) {
      float a = 0.f;
#pragma unroll
      for (int j = 0; j < 4; ++j) a += bflo(xr[i][j]) * bflo(gw[j]) + bfhi(xr[i][j]) * bfhi(gw[j]);
#pragma unroll
      for (int m = 32; m >= 1; m >>= 1) a += __shfl_xor(a, m);
      if (lane == 0) ((float*)(ws + WS_G))[ob * 8 + w * 2 + i] = 1.f / (1.f + __expf(-a));
    }
  }
  ST(1, 5);
  ST(1, 6);
  ST_FLUSH(1);
}

__global__ __launch_bounds__(256, 1) void p3_kernel(const char* __restrict__ ws, char* __restrict__ out) {
  __shared__ __attribute__((aligned(16))) f32x4 R[48 * 64];
  const int tid = threadIdx.x, lane = tid & 63;
  const int w = __builtin_amdgcn_readfirstlane(tid >> 6);
  const int bid = blockIdx.x, xcd = bid & 7, idx = bid >> 3;
  const int B = xcd * 4 + (idx & 3), n3 = idx >> 2;
  const int c = lane & 15, g = lane >> 4;
  const int p = 64 * B + 4 * c + w, srow = (p >> 3) + 256 * (p & 7);
  ST_DECL ST(2, 0);
  const float gate = ((const float*)(ws + WS_G))[srow];
  if (w < 2) {
    const s16x8* op = (const s16x8*)(ws + WS_O + ((size_t)(B * 16 + 8 * w) * 4 * 64 + lane) * 16);
    const s16x8* wp = (const s16x8*)(ws + WS_WOF + ((size_t)(n3 * 16 + 8 * w) * 4 * 64 + lane) * 16);
    s16x8 ob[8][4], wa[8][4];
#pragma unroll
    for (int s = 0; s < 8; ++s)
#pragma unroll
      for (int i = 0; i < 4; ++i) { ob[s][i] = op[(s * 4 + i) * 64]; wa[s][i] = wp[(s * 4 + i) * 64]; }
    __builtin_amdgcn_sched_barrier(0);
    ST(2, 1);
    f32x4 acc[4][4];
#pragma unroll
    for (int u = 0; u < 4; ++u)
#pragma unroll
      for (int t = 0; t < 4; ++t) acc[u][t] = f32x4{0, 0, 0, 0};
#pragma unroll
    for (int s = 0; s < 8; ++s)
#pragma unroll
      for (int u = 0; u < 4; ++u)
#pragma unroll
        for (int t = 0; t < 4; ++t) acc[u][t] = mfma32(wa[s][u], ob[s][t], acc[u][t]);
#pragma unroll
    for (int u = 0; u < 4; ++u)
#pragma unroll
      for (int t = 0; t < 4; ++t) R[(t * 12 + w * 4 + u) * 64 + lane] = acc[u][t];
  } else {
    const int par = w - 2;
    const char* zp = ws + WS_Z + ((size_t)(8 * g + par) * 2048 + 64 * B + 4 * c) * 4;
    u32x4 z[8][4];
#pragma unroll
    for (int s = 0; s < 8; ++s)
#pragma unroll
      for (int i = 0; i < 4; ++i) z[s][i] = *(const u32x4*)(zp + (size_t)(32 * s + 2 * i) * 8192);
    __builtin_amdgcn_sched_barrier(0);
    ST(2, 1);
    f32x4 acc[2][4];
#pragma unroll
    for (int u = 0; u < 2; ++u)
#pragma unroll
      for (int t = 0; t < 4; ++t) acc[u][t] = f32x4{0, 0, 0, 0};
    const int d0 = 32 * n3 + 8 * (c >> 2) + (c & 3);
    const float kb = (float)(8 * g + par);
#pragma unroll
    for (int s = 0; s < 8; ++s) {
      s16x8 ga[2];
#pragma unroll
      for (int u = 0; u < 2; ++u) {
        const float df = (float)(d0 + 4 * u) * (1.f / 512.f);
        unsigned e[4];
#pragma unroll
        for (int i = 0; i < 4; ++i) {
          const float a = __builtin_amdgcn_fractf((kb + (float)(32 * s + 2 * i)) * df);
          float cs = __builtin_amdgcn_cosf(a), sn = -__builtin_amdgcn_sinf(a);
          if (s == 0 && i == 0 && kb == 0.f) { cs = 0.5f; sn = (c & 1) ? -0.5f : 0.5f; }
          e[i] = pk2(cs, sn);
        }
        ga[u] = __builtin_bit_cast(s16x8, u32x4{e[0], e[1], e[2], e[3]});
      }
#pragma unroll
      for (int t = 0; t < 4; ++t) {
        const s16x8 zb = __builtin_bit_cast(s16x8, u32x4{z[s][0][t], z[s][1][t], z[s][2][t], z[s][3][t]});
#pragma unroll
        for (int u = 0; u < 2; ++u) acc[u][t] = mfma32(ga[u], zb, acc[u][t]);
      }
    }
#pragma unroll
    for (int u = 0; u < 2; ++u)
#pragma unroll
      for (int t = 0; t < 4; ++t) R[(t * 12 + 8 + par * 2 + u) * 64 + lane] = acc[u][t];
  }
  ST(2, 2);
  __syncthreads();
  ST(2, 3);
  const f32x4* Rw = R + w * 12 * 64 + lane;
  const float sc = (1.f - gate) * (2.f / (2048.f * 512.f));
#pragma unroll
  for (int hf = 0; hf < 2; ++hf) {
    unsigned o[4];
#pragma unroll
    for (int u = 0; u < 2; ++u) {
      const f32x4 a = Rw[(2 * hf + u) * 64] + Rw[(4 + 2 * hf + u) * 64];
      const f32x4 e = Rw[(8 + u) * 64], od = Rw[(10 + u) * 64];
      const f32x4 b = hf ? e - od : e + od;
      const f32x4 r = a * gate + b * sc;
      o[2 * u] = pk2(r[0], r[1]);
      o[2 * u + 1] = pk2(r[2], r[3]);
    }
    *(u32x4*)(out + ((size_t)srow * 512 + 256 * hf + 32 * n3 + 8 * g) * 2) = u32x4{o[0], o[1], o[2], o[3]};
  }
  ST(2, 4);
  ST(2, 5);
  ST_FLUSH(2);
}

extern "C" void kernel_launch(void* const* d_in, const int* in_sizes, int n_in, void* d_out, int out_size,
                              void* d_ws, size_t ws_size, hipStream_t stream) {
  const char* x = (const char*)d_in[0];
  const char* wqkv = (const char*)d_in[1];
  const char* wo = (const char*)d_in[2];
  const char* w_r = (const char*)d_in[4];
  const char* w_i = (const char*)d_in[5];
  const char* gw = (const char*)d_in[6];
  char* ws = (char*)d_ws;
  (void)hipFuncSetAttribute((const void*)p1_kernel, hipFuncAttributeMaxDynamicSharedMemorySize, P1_LDS);
  p1_kernel<<<256, 512, P1_LDS, stream>>>(x, wqkv, w_r, w_i, ws);
  p2_kernel<<<512, 256, 0, stream>>>(x, wo, gw, ws);
  p3_kernel<<<256, 256, 0, stream>>>(ws, (char*)d_out);
}
```

```cpp
#include <hip/hip_runtime.h>
#include <hip/hip_bf16.h>
#include <stdint.h>

typedef short s16x4 __attribute__((ext_vector_type(4)));
typedef short s16x8 __attribute__((ext_vector_type(8)));
typedef float f32x2 __attribute__((ext_vector_type(2)));
typedef float f32x4 __attribute__((ext_vector_type(4)));
typedef unsigned u32x2 __attribute__((ext_vector_type(2)));
typedef unsigned u32x4 __attribute__((ext_vector_type(4)));
typedef unsigned u32x4u __attribute__((ext_vector_type(4), aligned(2)));
typedef __bf16 bf16x2 __attribute__((ext_vector_type(2)));
typedef unsigned long long u64;
#define DI __device__ __forceinline__
#define LAS __attribute__((address_space(3)))
#define GAS __attribute__((address_space(1)))
#define RLX_AGENT __ATOMIC_RELAXED, __HIP_MEMORY_SCOPE_AGENT
#define RLX_WG __ATOMIC_RELAXED, __HIP_MEMORY_SCOPE_WORKGROUP
#define CFENCE() asm volatile("" ::: "memory")
#define PIN(v) asm volatile("" : "+v"(v))
#define DRAIN() asm volatile("s_waitcnt vmcnt(0)" ::: "memory")
#define LGKM0() __builtin_amdgcn_s_waitcnt(0xC07F)
#define SB() __builtin_amdgcn_sched_barrier(0)
#define BAR() do { CFENCE(); SB(); LGKM0(); __builtin_amdgcn_s_barrier(); SB(); CFENCE(); } while (0)
#define VMW(n) asm volatile("s_waitcnt vmcnt(" #n ")" ::: "memory")

DI unsigned pk2(float a, float b) {
  f32x2 v = {a, b};
  return __builtin_bit_cast(unsigned, __builtin_convertvector(v, bf16x2));
}
DI float bflo(unsigned u) { return __builtin_bit_cast(float, u << 16); }
DI float bfhi(unsigned u) { return __builtin_bit_cast(float, u & 0xffff0000u); }
DI f32x4 mfma32(s16x8 a, s16x8 b, f32x4 c) { return __builtin_amdgcn_mfma_f32_16x16x32_bf16(a, b, c, 0, 0, 0); }
DI f32x4 mfma16(u32x2 a, u32x2 b, f32x4 c) {
  return __builtin_amdgcn_mfma_f32_16x16x16bf16_1k(__builtin_bit_cast(s16x4, a), __builtin_bit_cast(s16x4, b), c, 0, 0, 0);
}
DI float phi(float t) {
  return fmaxf(t, 0.f) + __builtin_amdgcn_exp2f(fminf(t * 1.4426950408889634f, 0.f)) + 1e-6f;
}
DI u32x4 phi8(f32x4 a, f32x4 b) {
  return u32x4{pk2(phi(a[0]), phi(a[1])), pk2(phi(a[2]), phi(a[3])), pk2(phi(b[0]), phi(b[1])), pk2(phi(b[2]), phi(b[3]))};
}
DI s16x8 trfrag(const LAS char* p, int hi_off) {
  s16x4 l = __builtin_amdgcn_ds_read_tr16_b64_v4i16((LAS s16x4*)p);
  s16x4 h = __builtin_amdgcn_ds_read_tr16_b64_v4i16((LAS s16x4*)(p + hi_off));
  return __builtin_shufflevector(l, h, 0, 1, 2, 3, 4, 5, 6, 7);
}

#define WS_DBG (64u << 20)
#define ST(i)

constexpr int REP = 8;
constexpr int LN_FB = 8, LN_FA = LN_FB + 256, LN_FQ = LN_FA + 1024, LN_CZ = LN_FQ + 256, LN_CK = LN_CZ + 16 * REP;
constexpr int WS_XT = 256 * 1024;
constexpr int WS_WT = WS_XT + 256 * 8192;
constexpr int WS_KVP = WS_WT + 257 * 8192;
constexpr int WS_ZP = WS_KVP + 32 * 32 * 1024;
constexpr int WS_KVF = WS_ZP + 32 * 32 * 64;
constexpr int WS_ZF = WS_KVF + 32 * 512;
constexpr int WS_PQ = WS_ZF + 32 * 64;
constexpr int WS_WOF = WS_PQ + 2048 * 1024;
constexpr int WS_Z = WS_WOF + 512 * 1024;
constexpr int WS_END = WS_Z + 256 * 8192;
constexpr int L_XI = 0, L_RING = 65536, L_SCR = 131072, L_E = L_SCR + 18432, L_SYNC = L_SCR + 32768 - 64, L_GATE = L_SYNC - 128;
constexpr int L_ST = L_E + 12288;
constexpr int LDS_BYTES = 163840;
enum { C_A = 0, C_B = 1, C_Q = 2, C_W = 3, C_ZD = 4, F_B = 5, F_Z = 6, F_K = 7 };
constexpr unsigned SPIN_CAP = 1u << 16;

template <int SLP = 2> DI void lds_wait(LAS unsigned* p, unsigned target) {
  while (__hip_atomic_load(p, RLX_WG) < target) __builtin_amdgcn_s_sleep(SLP);
  CFENCE();
}
template <int SLP = 2> DI void subbar(LAS unsigned* p, unsigned target, int lane) {
  CFENCE();
  LGKM0();
  if (lane == 0) __hip_atomic_fetch_add(p, 1u, RLX_WG);
  lds_wait<SLP>(p, target);
}
template <int CTRL> DI float dpp_add(float v) {
  return v + __builtin_bit_cast(float, __builtin_amdgcn_update_dpp(0, __builtin_bit_cast(int, v), CTRL, 0xf, 0xf, false));
}
DI float add_x16(float v) {
  const unsigned u = __builtin_bit_cast(unsigned, v);
  auto r = __builtin_amdgcn_permlane16_swap(u, u, false, false);
  const unsigned a = r[0], b = r[1];
  return __uint_as_float(a) + __uint_as_float(b);
}
DI float add_x32(float v) {
  const unsigned u = __builtin_bit_cast(unsigned, v);
  auto r = __builtin_amdgcn_permlane32_swap(u, u, false, false);
  const unsigned a = r[0], b = r[1];
  return __uint_as_float(a) + __uint_as_float(b);
}
DI void poll_all(const char* ws, unsigned line, u64 want) {
  const GAS u64* p = (const GAS u64*)(ws + (size_t)line * 128);
  for (unsigned spins = 0; spins < SPIN_CAP; ++spins)
    if (__all(__hip_atomic_load(p, RLX_AGENT) == want)) break;
  __builtin_amdgcn_fence(__ATOMIC_ACQUIRE, "wavefront");
  CFENCE();
}
DI void put_flag(char* ws, unsigned line, u64 v) { __hip_atomic_store((GAS u64*)(ws + (size_t)line * 128), v, RLX_AGENT); }

DI void dma4(const unsigned (&voff)[4], const char* sb, unsigned dst) {
  unsigned keep;
  asm volatile("s_mov_b32 %0, m0\n\ts_mov_b32 m0, %6\n\ts_nop 0\n\t"
               "global_load_lds_dwordx4 %1, %5\n\t"
               "global_load_lds_dwordx4 %2, %5 offset:1024\n\t"
               "global_load_lds_dwordx4 %3, %5 offset:2048\n\t"
               "global_load_lds_dwordx4 %4, %5 offset:3072\n\t"
               "s_mov_b32 m0, %0"
               : "=&s"(keep) : "v"(voff[0]), "v"(voff[1]), "v"(voff[2]), "v"(voff[3]), "s"(sb), "s"(dst) : "memory");
}

DI f32x2 cmul(f32x2 a, f32x2 w) {
  f32x2 t, r;
  asm("v_pk_mul_f32 %0, %1, %2 op_sel:[1,1] op_sel_hi:[1,0] neg_lo:[0,1]" : "=v"(t) : "v"(a), "v"(w));
  asm("v_pk_fma_f32 %0, %1, %2, %3 op_sel:[0,0,0] op_sel_hi:[0,1,1]" : "=v"(r) : "v"(a), "v"(w), "v"(t));
  return r;
}
DI f32x2 cmulc(f32x2 a, f32x2 w) {
  f32x2 t, r;
  asm("v_pk_mul_f32 %0, %1, %2 op_sel:[1,1] op_sel_hi:[1,0]" : "=v"(t) : "v"(a), "v"(w));
  asm("v_pk_fma_f32 %0, %1, %2, %3 op_sel:[0,0,0] op_sel_hi:[0,1,1] neg_hi:[0,1,0]" : "=v"(r) : "v"(a), "v"(w), "v"(t));
  return r;
}
DI f32x2 add_mi(f32x2 a, f32x2 b) {
  f32x2 r;
  asm("v_pk_add_f32 %0, %1, %2 op_sel:[0,1] op_sel_hi:[1,0] neg_hi:[0,1]" : "=v"(r) : "v"(a), "v"(b));
  return r;
}
DI f32x2 add_pi(f32x2 a, f32x2 b) {
  f32x2 r;
  asm("v_pk_add_f32 %0, %1, %2 op_sel:[0,1] op_sel_hi:[1,0] neg_lo:[0,1]" : "=v"(r) : "v"(a), "v"(b));
  return r;
}
template <bool INV> DI f32x2 addr(f32x2 a, f32x2 b) { return INV ? add_pi(a, b) : add_mi(a, b); }
template <bool INV> DI f32x2 subr(f32x2 a, f32x2 b) { return INV ? add_mi(a, b) : add_pi(a, b); }
template <bool INV> DI void bf4(f32x2& a, f32x2& b, f32x2& c, f32x2& d) {
  const f32x2 s0 = a + c, s1 = a - c, s2 = b + d, s3 = b - d;
  a = s0 + s2; c = s0 - s2; b = addr<INV>(s1, s3); d = subr<INV>(s1, s3);
}
template <bool INV> DI void bf8(f32x2 (&v)[8]) {
  const f32x2 r = {0.70710678118654752f, 0.70710678118654752f}, nr = -r;
  f32x2 e0 = v[0], e1 = v[2], e2 = v[4], e3 = v[6], o0 = v[1], o1 = v[3], o2 = v[5], o3 = v[7];
  bf4<INV>(e0, e1, e2, e3);
  bf4<INV>(o0, o1, o2, o3);
  const f32x2 p1 = addr<INV>(o1, o1);
  const f32x2 p3 = subr<INV>(o3, o3);
  v[0] = e0 + o0; v[4] = e0 - o0;
  v[1] = __builtin_elementwise_fma(p1, r, e1); v[5] = __builtin_elementwise_fma(p1, nr, e1);
  v[2] = addr<INV>(e2, o2); v[6] = subr<INV>(e2, o2);
  v[3] = __builtin_elementwise_fma(p3, nr, e3); v[7] = __builtin_elementwise_fma(p3, r, e3);
}
DI f32x2 twid(float rev) { return f32x2{__builtin_amdgcn_cosf(rev), -__builtin_amdgcn_sinf(rev)}; }
DI f32x2 unpk(unsigned u) { return f32x2{bflo(u), bfhi(u)}; }
DI int wt_pos(int u) { return (((u & 7) * 8 + ((u >> 3) & 7)) * 8 + ((u >> 6) & 7)) * 4 + (u >> 9); }
DI int wt_row(int pos) { return (pos >> 8) + 8 * ((pos >> 5) & 7) + 64 * ((pos >> 2) & 7) + 512 * (pos & 3); }

#define LD128(off) __builtin_amdgcn_raw_buffer_load_b128(rs, (off), 0, 16)
#define LD64(off) __builtin_amdgcn_raw_buffer_load_b64(rs, (off), 0, 16)
#define LD32(off) __builtin_amdgcn_raw_buffer_load_b32(rs, (off), 0, 16)
#define ST128(v, off) __builtin_amdgcn_raw_buffer_store_b128((v), rs, (off), 0, 16)
#define ST64(v, off) __builtin_amdgcn_raw_buffer_store_b64((v), rs, (off), 0, 16)
#define ST32(v, off) __builtin_amdgcn_raw_buffer_store_b32((v), rs, (off), 0, 16)

__global__ __launch_bounds__(512, 2) void fused_kernel(const char* __restrict__ x, const char* __restrict__ wqkv,
                                                       const char* __restrict__ wo, const char* __restrict__ w_r,
                                                       const char* __restrict__ w_i, const char* __restrict__ gate_w,
                                                       char* __restrict__ ws, char* __restrict__ out) {
  extern __shared__ __attribute__((aligned(16))) char smem[];
  LAS char* sm = (LAS char*)smem;
  asm volatile("" :: "s"(x), "s"(wqkv), "s"(wo), "s"(w_r), "s"(w_i), "s"(gate_w), "s"(ws), "s"(out));
  const int tid = threadIdx.x, lane = tid & 63;
  const int w = __builtin_amdgcn_readfirstlane(tid >> 6);
  const int bid = blockIdx.x, xcd = bid & 7, idx = bid >> 3;
  const int mt = (xcd & 3) * 8 + (idx & 7), ct = (xcd >> 2) * 4 + (idx >> 3);
  const int rb = xcd * 8 + (idx & 7), cb = idx >> 3;
  const int c = lane & 15, g = lane >> 4;
  const unsigned lds0 = (unsigned)(size_t)sm;
  LAS unsigned* sync = (LAS unsigned*)(sm + L_SYNC);
  const __amdgpu_buffer_rsrc_t rs = __builtin_amdgcn_make_buffer_rsrc(ws, 0, WS_END, 0x00020000);
#define TAG_OF(e) ((e) * 0x9E3779B97F4A7C15ull + 0x632BE59BD9B4E019ull)
  ST(0);

  const int i8 = idx & 7, c0 = (idx + (idx >> 3)) & 3, flip = (idx >> 2) & 1;
  if (w >= 4) {
    const int wb = w - 4, bt = tid - 256;
    unsigned vx[4], vk[4];
    const unsigned dst0 = lds0 + wb * 4096;
#pragma unroll
    for (int i = 0; i < 4; ++i) {
      const int r = 16 * wb + 4 * i + g, p = 64 * mt + r;
      vx[i] = (unsigned)(((p >> 3) + 256 * (p & 7)) * 1024 + ((c ^ (r & 15)) << 4) + 4096 - i * 1024);
    }
    auto issue_x = [&](int i) { dma4(vx, x - 4096 + ((c0 - i) & 3) * 256, dst0 + i * 16384); };
    issue_x(0);
#pragma unroll
    for (int i = 0; i < 4; ++i) {
      const int r = 16 * wb + 4 * i + g, sl = c ^ ((g | ((i >> 1) << 2)) << 1);
      vk[i] = (unsigned)(r * 3072 + (sl >> 3) * 1024 + ((sl & 7) << 4) + 4096 - i * 1024);
    }
    auto issue_kv = [&](int j) {
      const int blk = 2 * ((c0 - (j >> 1)) & 3) + ((j & 1) ^ flip);
      dma4(vk, wqkv - 4096 + 1024 + ct * 128 + (unsigned)blk * (64 * 3072), dst0 + ((j + 4) & 7) * 16384);
    };
    issue_kv(0);
    issue_x(1);
    if (bt < 16) sync[bt] = 0;
    u64 epoch;
    asm volatile("s_load_dwordx2 %0, %1, 0x0\n\ts_waitcnt lgkmcnt(0)" : "=s"(epoch) : "s"(ws) : "memory");
    const u64 tag = TAG_OF(epoch);
    VMW(8); BAR();
    issue_x(2); issue_x(3);
#pragma unroll
    for (int i = 0; i < 4; ++i) {
      const int r = 16 * wb + 4 * i + g, sl = c ^ ((g | ((i >> 1) << 2)) << 1);
      vx[i] = (unsigned)(r * 3072 + ((sl >> 3) ^ flip) * (64 * 3072) + ((sl & 7) << 4) + 4096 - i * 1024);
    }
    auto issue_q = [&](int j) { dma4(vx, wqkv - 4096 + ct * 128 + (unsigned)((c0 - j) & 3) * (128 * 3072), dst0 + (j + 4) * 16384); };
    VMW(8); BAR();
    issue_kv(1);
    VMW(8); BAR();
    issue_kv(2);
    VMW(8); BAR();
    issue_kv(3); issue_kv(4);
    VMW(8); BAR();
    issue_kv(5); issue_kv(6);
    VMW(8); BAR();
    issue_kv(7); issue_q(0);
    VMW(8); BAR();
    issue_q(1); issue_q(2); issue_q(3);
    ST(1);
    VMW(12); BAR();
    f32x2 t1[7], t2[7], t3[7];
    {
      const float a1f = (float)bt * (1.f / 2048.f), a2f = (float)(bt & 31) * (1.f / 256.f), a3f = (float)(bt & 3) * (1.f / 32.f);
#pragma unroll
      for (int j = 1; j < 8; ++j) {
        t1[j - 1] = twid(a1f * j); t2[j - 1] = twid(a2f * j); t3[j - 1] = twid(a3f * j);
        PIN(t1[j - 1]); PIN(t2[j - 1]); PIN(t3[j - 1]);
      }
    }
    VMW(0); BAR();
    ST(2);
    __builtin_amdgcn_s_setprio(2);
    const int k = 32 * ct + mt;
    if (wb == 0) {
      poll_all(ws, LN_FB + ct * 32 + (lane & 31), tag);
      if (lane == 0) __hip_atomic_store(sync + F_B, 1u, RLX_WG);
    } else lds_wait<1>(sync + F_B, 1u);
    ST(3);
    const u32x4 x0 = LD128(WS_XT + k * 8192 + bt * 32), x1 = LD128(WS_XT + k * 8192 + bt * 32 + 16);
    const u32x4 w0 = LD128(WS_WT + k * 8192 + bt * 32), w1 = LD128(WS_WT + k * 8192 + bt * 32 + 16);
    const int k0 = bt >> 5, t1i = bt & 31, k1 = t1i >> 2, q = bt & 3;
    unsigned a1[8], b0[8], b1[8];
    int pn[8];
    if (k == 0) {
      const int ub = k0 + 8 * k1 + 128 * q;
#pragma unroll
      for (int j = 0; j < 8; ++j) {
        pn[j] = wt_pos((2048 - (ub + 64 * (j >> 2) + 512 * (j & 3))) & 2047);
        a1[j] = LD32(WS_WT + pn[j] * 4);
        b0[j] = LD32(WS_WT + 256 * 8192 + (bt * 8 + j) * 4);
        b1[j] = LD32(WS_WT + 256 * 8192 + pn[j] * 4);
      }
    }
    volatile LAS f32x2* L = (volatile LAS f32x2*)(sm + L_SCR);
    volatile LAS f32x2* Lg = L + k0 * 288;
    unsigned nb = 0;
    ST(4);
    f32x2 v[8] = {unpk(x0.x), unpk(x0.y), unpk(x0.z), unpk(x0.w), unpk(x1.x), unpk(x1.y), unpk(x1.z), unpk(x1.w)};
    bf8<false>(v);
#pragma unroll
    for (int j = 1; j < 8; ++j) v[j] = cmul(v[j], t1[j - 1]);
#pragma unroll
    for (int j = 0; j < 8; ++j) L[j * 288 + bt] = v[j];
    subbar<1>(sync + C_B, nb += 4, lane);
#pragma unroll
    for (int j = 0; j < 8; ++j) v[j] = Lg[t1i + 32 * j];
    bf8<false>(v);
#pragma unroll
    for (int j = 1; j < 8; ++j) v[j] = cmul(v[j], t2[j - 1]);
#pragma unroll
    for (int j = 0; j < 8; ++j) Lg[j * 36 + t1i] = v[j];
#pragma unroll
    for (int j = 0; j < 8; ++j) v[j] = Lg[k1 * 36 + q + 4 * j];
    bf8<false>(v);
#pragma unroll
    for (int j = 1; j < 8; ++j) v[j] = cmul(v[j], t3[j - 1]);
#pragma unroll
    for (int j = 0; j < 8; ++j) Lg[(j >> 1) * 65 + (j & 1) * 32 + t1i] = v[j];
#pragma unroll
    for (int j = 0; j < 8; ++j) v[j] = Lg[q * 65 + (j >> 2) * 32 + 4 * k1 + (j & 3)];
    bf4<false>(v[0], v[1], v[2], v[3]);
    bf4<false>(v[4], v[5], v[6], v[7]);
    const unsigned wv[8] = {w0.x, w0.y, w0.z, w0.w, w1.x, w1.y, w1.z, w1.w};
    ST(5);
    if (k != 0) {
#pragma unroll
      for (int j = 0; j < 8; ++j) v[j] = cmul(v[j], unpk(wv[j]));
    } else {
      auto ebase = [&](int gk) { return (gk & 3) == 0 ? L + gk * 288 : (volatile LAS f32x2*)(sm + L_E) + (gk - 1 - (gk > 4)) * 256; };
      volatile LAS f32x2* Ew = ebase(k0);
      volatile LAS f32x2* Er = ebase((8 - k0) & 7);
#pragma unroll
      for (int j = 0; j < 8; ++j) Ew[j * 32 + t1i] = v[j];
      f32x2 Bc[8];
#pragma unroll
      for (int j = 0; j < 8; ++j) {
        const f32x2 a0 = unpk(wv[j]), aa = unpk(a1[j]), bb = unpk(b0[j]), bc = unpk(b1[j]);
        const f32x2 s0 = f32x2{a0.x + aa.x, a0.y - aa.y}, sn = f32x2{bb.x + bc.x, bb.y - bc.y};
        Bc[j] = (s0 - sn) * 0.25f;
        v[j] = cmul(v[j], (s0 + sn) * 0.25f);
        PIN(v[j]); PIN(Bc[j]);
      }
      subbar<1>(sync + C_B, nb += 4, lane);
#pragma unroll
      for (int j = 0; j < 8; ++j) {
        const f32x2 cn = Er[(pn[j] & 7) * 32 + ((pn[j] >> 3) & 31)];
        v[j] += cmulc(Bc[j], cn);
      }
    }
    bf4<true>(v[0], v[1], v[2], v[3]);
    bf4<true>(v[4], v[5], v[6], v[7]);
#pragma unroll
    for (int j = 0; j < 8; ++j) Lg[q * 65 + (j >> 2) * 32 + 4 * k1 + (j & 3)] = v[j];
#pragma unroll
    for (int j = 0; j < 8; ++j) v[j] = Lg[(j >> 1) * 65 + (j & 1) * 32 + t1i];
#pragma unroll
    for (int j = 1; j < 8; ++j) v[j] = cmulc(v[j], t3[j - 1]);
    bf8<true>(v);
#pragma unroll
    for (int j = 0; j < 8; ++j) Lg[k1 * 36 + q + 4 * j] = v[j];
#pragma unroll
    for (int j = 0; j < 8; ++j) v[j] = Lg[j * 36 + t1i];
#pragma unroll
    for (int j = 1; j < 8; ++j) v[j] = cmulc(v[j], t2[j - 1]);
    bf8<true>(v);
#pragma unroll
    for (int j = 0; j < 8; ++j) Lg[t1i + 32 * j] = v[j];
    subbar<1>(sync + C_B, nb += 4, lane);
#pragma unroll
    for (int j = 0; j < 8; ++j) v[j] = L[j * 288 + bt];
#pragma unroll
    for (int j = 1; j < 8; ++j) v[j] = cmulc(v[j], t1[j - 1]);
    bf8<true>(v);
    {
      const int zo = WS_Z + k * 8192 + (bt >> 2) * 128 + (bt & 3) * 16;
      ST128((u32x4{pk2(v[0].x, v[1].x), pk2(v[2].x, v[3].x), pk2(v[4].x, v[5].x), pk2(v[6].x, v[7].x)}), zo);
      ST128((u32x4{pk2(v[0].y, v[1].y), pk2(v[2].y, v[3].y), pk2(v[4].y, v[5].y), pk2(v[6].y, v[7].y)}), zo + 64);
    }
    __builtin_amdgcn_s_setprio(0);
    ST(6);
    s16x8 ga[16];
    const float df = (float)(64 * cb + 16 * wb + c) * (1.f / 512.f);
    auto gen = [&](int f) {
      unsigned e[4];
#pragma unroll
      for (int i = 0; i < 4; ++i) {
        const float a = __builtin_amdgcn_fractf((float)(8 * g + 32 * (f & 7) + 2 * i + (f >> 3)) * df);
        float cs = __builtin_amdgcn_cosf(a), sn = -__builtin_amdgcn_sinf(a);
        if (f == 0 && i == 0 && g == 0) { cs = 0.5f; sn = (c & 1) ? -0.5f : 0.5f; }
        e[i] = pk2(cs, sn);
      }
      ga[f] = __builtin_bit_cast(s16x8, u32x4{e[0], e[1], e[2], e[3]});
      PIN(ga[f]);
    };
#pragma unroll
    for (int f = 0; f < 8; ++f) gen(f);
    SB();
    DRAIN();
    {
      unsigned old = 0;
      if (lane == 0) old = __hip_atomic_fetch_add(sync + C_ZD, 1u, RLX_WG);
      old = __builtin_amdgcn_readfirstlane(old);
      if (old == 3 && lane < REP)
        __hip_atomic_fetch_add((GAS u64*)(ws + (size_t)(LN_CZ + lane * 16 + 2 * ct + (mt >> 4)) * 128), 1ull, RLX_AGENT);
    }
    ST(7);
    {
      const u32x4 gw = *(const u32x4*)(gate_w + lane * 16);
      u32x4 xr[8];
#pragma unroll
      for (int i = 0; i < 8; ++i) xr[i] = *(const u32x4*)(x + (size_t)(4 * rb + wb + 256 * i) * 1024 + lane * 16);
      SB();
#pragma unroll
      for (int f = 8; f < 16; ++f) gen(f);
      SB();
      float gs = 0.f;
#pragma unroll
      for (int i = 0; i < 8; ++i) {
        float s = 0.f;
#pragma unroll
        for (int j = 0; j < 4; ++j) s += bflo(xr[i][j]) * bflo(gw[j]) + bfhi(xr[i][j]) * bfhi(gw[j]);
        s = dpp_add<0xB1>(s);
        s = dpp_add<0x4E>(s);
        s = dpp_add<0x141>(s);
        gs = (lane & 7) == i ? s : gs;
      }
      gs = dpp_add<0x128>(gs);
      gs = add_x32(add_x16(gs));
      if (lane < 8) ((LAS float*)(sm + L_GATE))[8 * wb + lane] = 1.f / (1.f + __expf(-gs));
    }
    if (wb == 0) {
      poll_all(ws, LN_CZ + (bid % REP) * 16 + c, tag + 16);
      if (lane == 0) __hip_atomic_store(sync + F_Z, 1u, RLX_WG);
      if (bid == 0 && lane == 0) *(u64*)ws = epoch + 1;
    } else lds_wait<1>(sync + F_Z, 1u);
    ST(8);
    {
      u32x4 z[8];
#pragma unroll
      for (int i = 0; i < 8; ++i) z[i] = LD128(WS_Z + (64 * wb + 8 * i + (lane >> 3)) * 8192 + rb * 128 + (lane & 7) * 16);
      lds_wait(sync + C_W, 4u);
#pragma unroll
      for (int i = 0; i < 8; ++i) {
        const int kk = 64 * wb + 8 * i + (lane >> 3);
        const int R = (((kk & 1) * 128 + (kk >> 1)) << 1) + ((lane >> 2) & 1);
        *(LAS u32x4*)(sm + L_XI + R * 64 + (((lane & 3) ^ ((R >> 2) & 2)) << 4)) = z[i];
      }
    }
    ST(9);
    subbar<1>(sync + C_B, nb += 4, lane);
    f32x4 acc[2][2] = {{{0, 0, 0, 0}, {0, 0, 0, 0}}, {{0, 0, 0, 0}, {0, 0, 0, 0}}};
    {
      const LAS char* zb = sm + L_XI + (8 * g + (c >> 2)) * 64 + 8 * (c & 3) + ((g & 1) << 5);
      const LAS char* zb1 = sm + L_XI + (8 * g + (c >> 2)) * 64 + 8 * (c & 3) + (((g & 1) ^ 1) << 5);
      s16x8 zf[16][2];
#pragma unroll
      for (int f = 0; f < 16; ++f) { zf[f][0] = trfrag(zb + f * 2048, 256); zf[f][1] = trfrag(zb1 + f * 2048, 256); }
#pragma unroll
      for (int f = 0; f < 16; ++f) {
        acc[f >> 3][0] = mfma32(ga[f], zf[f][0], acc[f >> 3][0]);
        acc[f >> 3][1] = mfma32(ga[f], zf[f][1], acc[f >> 3][1]);
      }
      __builtin_amdgcn_sched_group_barrier(0x100, 12, 0);
#pragma unroll
      for (int f = 0; f < 26; ++f) { __builtin_amdgcn_sched_group_barrier(0x8, 1, 0); __builtin_amdgcn_sched_group_barrier(0x100, 2, 0); }
      __builtin_amdgcn_sched_group_barrier(0x8, 6, 0);
    }
    {
      LAS f32x4* X = (LAS f32x4*)(sm + L_SCR) + wb * 256 + lane;
      X[0] = acc[0][0] + acc[1][0]; X[64] = acc[0][1] + acc[1][1];
      X[128] = acc[0][0] - acc[1][0]; X[192] = acc[0][1] - acc[1][1];
    }
    ST(10);
    BAR();
  } else {
    u32x4 wrv, wiv, wov = {0, 0, 0, 0};
    unsigned nyq = 0, nyq2 = 0, pf = 0;
    const u32x2 ev = *(const u32x2*)ws;
    if (w == 3) pf = *(const unsigned*)(wqkv + (64 * i8 + lane) * 3072 + ct * 128);
    {
      const int u = wt_row(64 * mt + (tid >> 2));
      const unsigned off = (unsigned)(u * 257 + 32 * ct + 8 * (tid & 3)) * 2u;
      wrv = *(const u32x4u*)(w_r + off);
      wiv = *(const u32x4u*)(w_i + off);
      if (ct < 4) wov = *(const u32x4*)(wo + (32 * (mt >> 1) + (tid >> 3)) * 1024 + (mt & 1) * 512 + ct * 128 + (tid & 7) * 16);
      if (ct == 0 && w == 0) {
        const int un = wt_row(64 * mt + lane);
        nyq = *(const unsigned short*)(w_r + (un * 257 + 256) * 2);
        nyq2 = *(const unsigned short*)(w_i + (un * 257 + 256) * 2);
      }
    }
    SB();
    const int rh = w & 1, kt = w >> 1;
    const int kf = 32 * ct + 16 * kt + c;
    s16x8 xf[4][4][2];
    unsigned tagl, tagh;
    {
      s16x8 fc[4], fs[4];
      {
        const float kk = (float)kf * (1.f / 512.f);
        const float a0 = __builtin_amdgcn_fractf(kk * (float)(8 * g + 128 * ((c0 + 1) & 3)));
#pragma unroll
        for (int ks = 0; ks < 4; ++ks) {
          unsigned ec[4], es[4];
          const float ak = __builtin_fmaf(kk, (float)(((ks & 2) ^ (flip << 1)) * 32), a0);
#pragma unroll
          for (int i = 0; i < 4; ++i) {
            const float p0 = __builtin_fmaf(kk, (float)(32 * (ks & 1) + 2 * i), ak), p1 = __builtin_fmaf(kk, (float)(32 * (ks & 1) + 2 * i + 1), ak);
            ec[i] = pk2(__builtin_amdgcn_cosf(p0), __builtin_amdgcn_cosf(p1));
            es[i] = pk2(__builtin_amdgcn_sinf(-p0), __builtin_amdgcn_sinf(-p1));
            if (kf == 0) es[i] = 0xBF803F80u;
          }
          fc[ks] = __builtin_bit_cast(s16x8, u32x4{ec[0], ec[1], ec[2], ec[3]});
          fs[ks] = __builtin_bit_cast(s16x8, u32x4{es[0], es[1], es[2], es[3]});
          PIN(fc[ks]); PIN(fs[ks]);
        }
      }
      PIN(pf); PIN(nyq); PIN(nyq2);
      tagl = __builtin_amdgcn_readfirstlane(ev.x); tagh = __builtin_amdgcn_readfirstlane(ev.y);
      const float ra = (c & 1) ? 0.f : ((c & 2) ? -1.f : 1.f), rbi = (c & 1) ? ((c & 2) ? 1.f : -1.f) : 0.f;
      {
        const int pos = tid >> 2, kq = tid & 3;
        LAS unsigned* sc = (LAS unsigned*)(sm + L_SCR) + kq * 512 + (pos ^ (kq << 3));
#pragma unroll
        for (int j = 0; j < 4; ++j) {
          sc[(2 * j) * 64] = __builtin_amdgcn_perm(wiv[j], wrv[j], 0x05040100u);
          sc[(2 * j + 1) * 64] = __builtin_amdgcn_perm(wiv[j], wrv[j], 0x07060302u);
        }
        const int r = tid >> 3;
        *(LAS u32x4*)(sm + L_SCR + 8192 + r * 128 + (((tid & 7) ^ (r & 6)) << 4)) = wov;
      }
      if (mt == 0 && w == 1 && lane < 4 * REP)
        put_flag(ws, LN_CZ + (lane >> 1) * 16 + 2 * ct + (lane & 1), TAG_OF(((u64)tagh << 32) | tagl));
      ST(1);
      const LAS char* xbase = sm + rh * 8192 + c * 256;
      f32x4 xr[2] = {{0, 0, 0, 0}, {0, 0, 0, 0}}, xi[2] = {{0, 0, 0, 0}, {0, 0, 0, 0}};
#pragma unroll
      for (int s = 0; s < 4; ++s) {
        if (s > 0) {
#pragma unroll
          for (int tt = 0; tt < 2; ++tt) {
            const f32x4 nr = xr[tt] * ra - xi[tt] * rbi, ni = xr[tt] * rbi + xi[tt] * ra;
            xr[tt] = nr; xi[tt] = ni;
          }
        }
        BAR();
#pragma unroll
        for (int ks = 0; ks < 4; ++ks)
#pragma unroll
          for (int tt = 0; tt < 2; ++tt)
            xf[s][ks][tt] = *(const LAS s16x8*)(xbase + (((4 * ks + g) ^ (flip << 3) ^ c) << 4) + s * 16384 + tt * 4096);
        SB();
#pragma unroll
        for (int ks = 0; ks < 4; ++ks)
#pragma unroll
          for (int tt = 0; tt < 2; ++tt) {
            xr[tt] = mfma32(xf[s][ks][tt], fc[ks], xr[tt]);
            xi[tt] = mfma32(xf[s][ks][tt], fs[ks], xi[tt]);
          }
        if (s == 0) {
#pragma unroll
          for (int i = 0; i < 2; ++i) {
            const int kk = (tid >> 4) + 16 * i, m4 = tid & 15;
            const u32x4 v = *(const LAS u32x4*)(sm + L_SCR + kk * 256 + ((m4 ^ ((kk >> 3) << 1)) << 4));
            ST128(v, WS_WT + (32 * ct + kk) * 8192 + (64 * mt + 4 * m4) * 4);
          }
          if (ct < 4) {
            const int r = 4 * g + (c >> 2);
            const s16x8 f = trfrag(sm + L_SCR + 8192 + r * 128 + ((w ^ ((r >> 1) & 3)) << 5) + 8 * (c & 3), 2048);
            ST128(__builtin_bit_cast(u32x4, f), WS_WOF + ((ct * 4 + w) * 32 + mt) * 1024 + lane * 16);
          }
          if (ct == 0 && w == 0) ST32(nyq | (nyq2 << 16), WS_WT + 256 * 8192 + (64 * mt + lane) * 4);
        }
      }
#pragma unroll
      for (int tt = 0; tt < 2; ++tt)
        ST128((u32x4{pk2(xr[tt][0], xi[tt][0]), pk2(xr[tt][1], xi[tt][1]), pk2(xr[tt][2], xi[tt][2]), pk2(xr[tt][3], xi[tt][3])}),
              WS_XT + kf * 8192 + (64 * mt + 32 * rh + 16 * tt + 4 * g) * 4);
    }
    const u64 tag = TAG_OF(((u64)tagh << 32) | tagl);
    ST(2);
    const unsigned fl = (unsigned)((c >> 2) | ((g & 1) << 2));
    const unsigned bu = (unsigned)((8 * g + (c >> 2)) * 256 + 8 * (c & 3)) + (((unsigned)(2 * kt) ^ fl) << 5);
    unsigned bb[2][4];
#pragma unroll
    for (int u = 0; u < 4; ++u) {
      bb[0][u] = lds0 + (bu ^ (unsigned)((u & 1) * 32 + (u >> 1) * 128));
      bb[1][u] = bb[0][u] + 65536u;
      PIN(bb[0][u]); PIN(bb[1][u]);
    }
    s16x8 fb[3][4];
    auto ldf = [&](int n, int slot0) {
#pragma unroll
      for (int u = 0; u < 4; ++u) {
        const int slot = (slot0 + (n >> 1)) & 7;
        fb[n % 3][u] = trfrag((const LAS char*)(size_t)bb[slot >> 2][u] + (slot & 3) * 16384 + (n & 1) * 8192, 1024);
      }
    };
    auto mix = [&]() {
#pragma unroll
      for (int i = 0; i < 8; ++i) { __builtin_amdgcn_sched_group_barrier(0x100, 1, 0); __builtin_amdgcn_sched_group_barrier(0x8, 1, 0); }
      SB();
    };
    {
      f32x4 acc[2][4];
#pragma unroll
      for (int t = 0; t < 2; ++t)
#pragma unroll
        for (int u = 0; u < 4; ++u) acc[t][u] = f32x4{0, 0, 0, 0};
      ldf(0, 4); ldf(1, 4);
#pragma unroll
      for (int n = 0; n < 16; ++n) {
        if (n == 4) DRAIN();
        if ((n & 3) == 0) BAR();
        if (n == 4) { if (tid == 0) put_flag(ws, LN_FB + ct * 32 + mt, tag); SB(); }
        if (n < 14) ldf(n + 2, 4);
#pragma unroll
        for (int u = 0; u < 4; ++u)
#pragma unroll
          for (int t = 0; t < 2; ++t) acc[t][u] = mfma32(xf[n >> 2][n & 3][t], fb[n % 3][u], acc[t][u]);
        mix();
      }
      ST(3);
      const s16x8 ones = {0x3f80, 0x3f80, 0x3f80, 0x3f80, 0x3f80, 0x3f80, 0x3f80, 0x3f80};
      f32x4 kv[2], zz[2];
#pragma unroll
      for (int n = 0; n < 2; ++n) {
        const f32x4 va = acc[0][2 + n], vb = acc[1][2 + n], zero = {0, 0, 0, 0};
        const u32x4 pk = phi8(acc[0][n], acc[1][n]);
        const u32x4 vv = {pk2(va[0], va[1]), pk2(va[2], va[3]), pk2(vb[0], vb[1]), pk2(vb[2], vb[3])};
        kv[n] = mfma32(__builtin_bit_cast(s16x8, pk), __builtin_bit_cast(s16x8, vv), zero);
        zz[n] = mfma32(__builtin_bit_cast(s16x8, pk), ones, zero);
      }
      LAS f32x4* ex = (LAS f32x4*)sm + lane;
      ex[(w ^ 1) * 128] = rh ? kv[0] : kv[1];
      ex[(w ^ 1) * 128 + 64] = rh ? zz[0] : zz[1];
      BAR();
      const f32x4 kvo = (rh ? kv[1] : kv[0]) + ex[w * 128], zo = (rh ? zz[1] : zz[0]) + ex[w * 128 + 64];
      const int rec = (4 * ct + w) * 32 + mt;
      ST128(__builtin_bit_cast(u32x4, kvo), WS_KVP + rec * 1024 + lane * 16);
      if (c == 0) ST128(__builtin_bit_cast(u32x4, zo), WS_ZP + rec * 64 + g * 16);
    }
    ST(4);
    {
      f32x4 acc[2][2] = {{{0, 0, 0, 0}, {0, 0, 0, 0}}, {{0, 0, 0, 0}, {0, 0, 0, 0}}};
      SB();
      ldf(0, 4); ldf(1, 4);
      SB();
#pragma unroll
      for (int n = 0; n < 8; ++n) {
        if (n < 6) ldf(n + 2, 4);
        if (n == 6) {
          CFENCE();
          if (lane == 0) __hip_atomic_fetch_add(sync + C_W, 1u, RLX_WG);
          DRAIN();
          if (lane == 0) put_flag(ws, LN_FA + (4 * ct + w) * 32 + mt, tag);
          SB();
        }
#pragma unroll
        for (int u = 0; u < 4; ++u)
#pragma unroll
          for (int t = 0; t < 2; ++t) acc[u & 1][t] = mfma32(fb[n % 3][u], xf[n >> 1][2 * (u >> 1) + (n & 1)][t], acc[u & 1][t]);
        mix();
      }
#pragma unroll
      for (int n = 0; n < 2; ++n)
        ST128(phi8(acc[n][0], acc[n][1]), WS_PQ + ((2 * mt + rh) * 32 + 4 * ct + 2 * kt + n) * 1024 + lane * 16);
    }
    ST(5);
    auto pq_arrive = [&]() {
      unsigned old = 0;
      if (lane == 0) old = __hip_atomic_fetch_add(sync + C_Q, 1u, RLX_WG);
      old = __builtin_amdgcn_readfirstlane(old);
      if (old == 3 && lane == 0) put_flag(ws, LN_FQ + mt * 8 + ct, tag);
    };
    if (w == 0) {
      const int h = 4 * ct + (mt >> 3), sub = mt & 7;
      poll_all(ws, LN_FA + h * 32 + (lane & 31), tag);
      ST(6);
      u32x4 pv[4], pz[2];
#pragma unroll
      for (int i = 0; i < 4; ++i) pv[i] = LD128(WS_KVP + (h * 32 + 8 * i + (lane >> 3)) * 1024 + sub * 128 + (lane & 7) * 16);
      if (sub == 0) {
#pragma unroll
        for (int i = 0; i < 2; ++i) pz[i] = LD128(WS_ZP + h * 2048 + i * 1024 + lane * 16);
      }
      SB();
      pq_arrive();
      f32x4 s = (__builtin_bit_cast(f32x4, pv[0]) + __builtin_bit_cast(f32x4, pv[1])) + (__builtin_bit_cast(f32x4, pv[2]) + __builtin_bit_cast(f32x4, pv[3]));
#pragma unroll
      for (int e = 0; e < 4; ++e) s[e] = add_x32(add_x16(dpp_add<0x128>(s[e])));
      if (lane < 8) ST64((u32x2{pk2(s[0], s[1]), pk2(s[2], s[3])}), WS_KVF + (h >> 1) * 1024 + (sub * 8 + lane) * 16 + (h & 1) * 8);
      if (sub == 0) {
        f32x4 z = __builtin_bit_cast(f32x4, pz[0]) + __builtin_bit_cast(f32x4, pz[1]);
#pragma unroll
        for (int e = 0; e < 4; ++e) z[e] = add_x32(add_x16(dpp_add<0x128>(dpp_add<0x124>(z[e]))));
        if (lane < 4) ST64((u32x2{pk2(z[0], z[1]), pk2(z[2], z[3])}), WS_ZF + (h >> 1) * 64 + lane * 16 + (h & 1) * 8);
      }
      DRAIN();
      if (lane < REP) __hip_atomic_fetch_add((GAS u64*)(ws + (size_t)(LN_CK + lane * 16 + 2 * ct + (mt >> 4)) * 128), 1ull, RLX_AGENT);
    }
    ST(7);
    poll_all(ws, LN_FB + cb * 32 + (lane & 31), tag);
    if (w != 0) pq_arrive();
    s16x8 wa[16][2];
#pragma unroll
    for (int st = 0; st < 16; ++st)
#pragma unroll
      for (int hf = 0; hf < 2; ++hf) wa[st][hf] = __builtin_bit_cast(s16x8, LD128(WS_WOF + ((cb * 4 + w) * 32 + 2 * st + hf) * 1024 + lane * 16));
    SB();
    ST(8);
    poll_all(ws, LN_FQ + (rb >> 1) * 8 + 2 * w + (lane & 1), tag);
    u32x4 pq[8];
#pragma unroll
    for (int i = 0; i < 8; ++i) pq[i] = LD128(WS_PQ + (rb * 32 + 8 * w + i) * 1024 + lane * 16);
    SB();
    ST(9);
    if (w == 0) {
      poll_all(ws, LN_CK + (bid % REP) * 16 + c, tag + 16);
      if (lane == 0) __hip_atomic_store(sync + F_K, 1u, RLX_WG);
    } else lds_wait<1>(sync + F_K, 1u);
    ST(10);
    {
      u32x4 kf4[4], zf4[4];
#pragma unroll
      for (int i = 0; i < 4; ++i) {
        kf4[i] = LD128(WS_KVF + (4 * w + i) * 1024 + lane * 16);
        zf4[i] = LD128(WS_ZF + (4 * w + i) * 64 + g * 16);
      }
      lds_wait(sync + C_W, 4u);
      LAS u32x4* O = (LAS u32x4*)(sm + L_RING) + w * 512 + lane;
#pragma unroll
      for (int hp = 0; hp < 4; ++hp)
#pragma unroll
        for (int tt = 0; tt < 2; ++tt) {
          unsigned o[4];
#pragma unroll
          for (int n = 0; n < 2; ++n) {
            const u32x4 pp = pq[2 * hp + n];
            const u32x2 b = tt ? u32x2{pp.z, pp.w} : u32x2{pp.x, pp.y};
            const u32x2 ka = n ? u32x2{kf4[hp].z, kf4[hp].w} : u32x2{kf4[hp].x, kf4[hp].y};
            const u32x2 za = n ? u32x2{zf4[hp].z, zf4[hp].w} : u32x2{zf4[hp].x, zf4[hp].y};
            const f32x4 zero = {0, 0, 0, 0};
            const f32x4 num = mfma16(ka, b, zero), den = mfma16(za, b, zero);
            const float r = __builtin_amdgcn_rcpf(den[0] + 1e-6f);
            o[2 * n] = pk2(num[0] * r, num[1] * r);
            o[2 * n + 1] = pk2(num[2] * r, num[3] * r);
          }
          O[(hp * 2 + tt) * 64] = u32x4{o[0], o[1], o[2], o[3]};
        }
    }
    ST(11);
    subbar<1>(sync + C_A, 4u, lane);
    f32x4 acc[2][2] = {{{0, 0, 0, 0}, {0, 0, 0, 0}}, {{0, 0, 0, 0}, {0, 0, 0, 0}}};
    {
      const LAS s16x8* O = (const LAS s16x8*)(sm + L_RING) + lane;
#pragma unroll
      for (int st = 0; st < 16; ++st)
#pragma unroll
        for (int tt = 0; tt < 2; ++tt) {
          const s16x8 ob = O[(st * 2 + tt) * 64];
          acc[0][tt] = mfma32(wa[st][0], ob, acc[0][tt]);
          acc[1][tt] = mfma32(wa[st][1], ob, acc[1][tt]);
        }
      __builtin_amdgcn_sched_group_barrier(0x100, 8, 0);
#pragma unroll
      for (int i = 0; i < 24; ++i) { __builtin_amdgcn_sched_group_barrier(0x8, 2, 0); __builtin_amdgcn_sched_group_barrier(0x100, 1, 0); }
      __builtin_amdgcn_sched_group_barrier(0x8, 16, 0);
    }
    ST(12);
    BAR();
    {
      const LAS f32x4* X = (const LAS f32x4*)(sm + L_SCR) + w * 256 + lane;
#pragma unroll
      for (int tt = 0; tt < 2; ++tt) {
        const int p = 32 * rb + 16 * tt + c, srow = (p >> 3) + 256 * (p & 7);
        const float gate = ((const LAS float*)(sm + L_GATE))[16 * tt + c];
        const float sc = (1.f - gate) * (2.f / (2048.f * 512.f));
#pragma unroll
        for (int hf = 0; hf < 2; ++hf) {
          const f32x4 r = acc[hf][tt] * gate + X[(hf * 2 + tt) * 64] * sc;
          *(u32x2*)(out + ((size_t)srow * 512 + 256 * hf + 64 * cb + 16 * w + 4 * g) * 2) = u32x2{pk2(r[0], r[1]), pk2(r[2], r[3])};
        }
      }
    }
    ST(13);
  }
}

extern "C" void kernel_launch(void* const* d_in, const int* in_sizes, int n_in, void* d_out, int out_size,
                              void* d_ws, size_t ws_size, hipStream_t stream) {
  (void)hipFuncSetAttribute((const void*)fused_kernel, hipFuncAttributeMaxDynamicSharedMemorySize, LDS_BYTES);
  fused_kernel<<<256, 512, LDS_BYTES, stream>>>((const char*)d_in[0], (const char*)d_in[1], (const char*)d_in[2],
                                                (const char*)d_in[4], (const char*)d_in[5], (const char*)d_in[6],
                                                (char*)d_ws, (char*)d_out);
}
```

```cpp
#include <hip/hip_runtime.h>
#include <hip/hip_bf16.h>
#include <stdint.h>

typedef short s16x4 __attribute__((ext_vector_type(4)));
typedef short s16x8 __attribute__((ext_vector_type(8)));
typedef float f32x2 __attribute__((ext_vector_type(2)));
typedef float f32x4 __attribute__((ext_vector_type(4)));
typedef unsigned u32x2 __attribute__((ext_vector_type(2)));
typedef unsigned u32x4 __attribute__((ext_vector_type(4)));
typedef unsigned u32x4u __attribute__((ext_vector_type(4), aligned(2)));
typedef __bf16 bf16x2 __attribute__((ext_vector_type(2)));
typedef unsigned long long u64;
#define DI __device__ __forceinline__
#define LAS __attribute__((address_space(3)))
#define GAS __attribute__((address_space(1)))
#define RLX_AGENT __ATOMIC_RELAXED, __HIP_MEMORY_SCOPE_AGENT
#define CFENCE() asm volatile("" ::: "memory")
#define PIN(v) asm volatile("" : "+v"(v))
#define DRAIN() asm volatile("s_waitcnt vmcnt(0)" ::: "memory")
#define LGKM0() __builtin_amdgcn_s_waitcnt(0xC07F)
#define SB() __builtin_amdgcn_sched_barrier(0)
#define VMW_(n) asm volatile("s_waitcnt vmcnt(" #n ")" ::: "memory")
#define VMW(n) VMW_(n)
#define LP(T, a) ((LAS T*)(size_t)(a))
#define WS_DBG (64u << 20)
#define BAR() do { CFENCE(); SB(); LGKM0(); __builtin_amdgcn_s_barrier(); SB(); CFENCE(); } while (0)
#define ST(i)

DI unsigned pk2(float a, float b) {
  f32x2 v = {a, b};
  return __builtin_bit_cast(unsigned, __builtin_convertvector(v, bf16x2));
}
DI float bflo(unsigned u) { return __uint_as_float(u << 16); }
DI float bfhi(unsigned u) { return __uint_as_float(u & 0xffff0000u); }
DI f32x4 lo4(u32x4 u) { return f32x4{bflo(u.x), bflo(u.y), bflo(u.z), bflo(u.w)}; }
DI f32x4 hi4(u32x4 u) { return f32x4{bfhi(u.x), bfhi(u.y), bfhi(u.z), bfhi(u.w)}; }
DI f32x4 mfma32(u32x4 a, u32x4 b, f32x4 c) {
  return __builtin_amdgcn_mfma_f32_16x16x32_bf16(__builtin_bit_cast(s16x8, a), __builtin_bit_cast(s16x8, b), c, 0, 0, 0);
}
DI f32x4 mfma16(u32x2 a, u32x2 b, f32x4 c) {
  return __builtin_amdgcn_mfma_f32_16x16x16bf16_1k(__builtin_bit_cast(s16x4, a), __builtin_bit_cast(s16x4, b), c, 0, 0, 0);
}
DI u32x4 pack8(f32x4 a, f32x4 b) { return u32x4{pk2(a[0], a[1]), pk2(a[2], a[3]), pk2(b[0], b[1]), pk2(b[2], b[3])}; }
DI u32x4 neg8(u32x4 a) {
  PIN(a);
  a ^= 0x80008000u;
  PIN(a);
  return a;
}
DI f32x4 phi4(f32x4 t) {
  const f32x4 m = t * 1.4426950408889634f, r = t + 1e-6f;
  f32x4 e, q;
#pragma unroll
  for (int i = 0; i < 4; ++i) { e[i] = __builtin_amdgcn_exp2f(fminf(m[i], 0.f)); q[i] = fmaxf(r[i], 1e-6f); }
  return e + q;
}
DI u32x4 trfrag(unsigned a, int off, int hi_off) {
  s16x4 l = __builtin_amdgcn_ds_read_tr16_b64_v4i16((LAS s16x4*)(LP(char, a) + off));
  s16x4 h = __builtin_amdgcn_ds_read_tr16_b64_v4i16((LAS s16x4*)(LP(char, a) + off + hi_off));
  return __builtin_bit_cast(u32x4, __builtin_shufflevector(l, h, 0, 1, 2, 3, 4, 5, 6, 7));
}
DI u32x4 ld16(unsigned a, int off) { return *(const LAS u32x4*)(LP(char, a) + off); }
template <int CTRL> DI float dpp_add(float v) {
  return v + __int_as_float(__builtin_amdgcn_update_dpp(0, __float_as_int(v), CTRL, 0xf, 0xf, false));
}
DI float row_sum16(float v) { return dpp_add<0x128>(dpp_add<0x124>(dpp_add<0x4E>(dpp_add<0xB1>(v)))); }

constexpr int REP = 8;
constexpr int LN_FB = 8, LN_FQ = LN_FB + 256, LN_CZ = LN_FQ + 256, LN_CK = LN_CZ + 16 * REP;
constexpr int WS_XT = 128 * 1024;
constexpr int WS_WT = WS_XT + 256 * 8192;
constexpr int WS_KVP = WS_WT + 257 * 8192;
constexpr int WS_ZP = WS_KVP + 16 * 64 * 1024;
constexpr int WS_KVF = WS_ZP + 16 * 64 * 64;
constexpr int WS_ZF = WS_KVF + 16 * 1024;
constexpr int WS_G = WS_ZF + 2048;
constexpr int WS_PQ = WS_G + 8192;
constexpr int WS_Z = WS_PQ + 2048 * 1024;
constexpr int WS_WOF = WS_Z;
constexpr int WS_END = WS_Z + 256 * 8192;
constexpr int L_SCR = 131072, L_ST = L_SCR + 30720, L_SYNC = L_SCR + 32768 - 512, LDS_BYTES = 163840;
enum { E_QL, E_FB, E_X1, E_X2, E_ZD, E_FZ, E_ZI, E_QR, E_PQ, E_WL, E_WR, E_KA, E_FQ, E_FK, E_O, E_G };
constexpr unsigned SPIN_CAP = 1u << 16;
#define POLL_SLEEP 3
#define WO_THR 4
#define CODE_LINES 7
#define TAG_OF(e) ((e) * 0x9E3779B97F4A7C15ull + 0x632BE59BD9B4E019ull)

#define SIG(ev) do { CFENCE(); *(volatile LAS unsigned*)(LP(char, sigp) + (ev) * 16) = 1u; CFENCE(); } while (0)
template <unsigned MASK> DI void ev_wait(int ev) {
  const volatile LAS u32x4* p = (const volatile LAS u32x4*)(size_t)(L_SYNC + ev * 16);
  for (;;) {
    const u32x4 v = *p;
    const unsigned a = ((MASK & 1) ? v.x : 1u) & ((MASK & 2) ? v.y : 1u) & ((MASK & 4) ? v.z : 1u) & ((MASK & 8) ? v.w : 1u);
    if (__builtin_amdgcn_readfirstlane(a)) break;
    __builtin_amdgcn_s_sleep(1);
  }
  CFENCE();
}
DI void poll_all(const char* ws, unsigned line, u64 want) {
  const GAS u64* p = (const GAS u64*)(ws + (size_t)line * 128);
  for (unsigned spins = 0; spins < SPIN_CAP; ++spins) {
    if (__all(__hip_atomic_load(p, RLX_AGENT) == want)) break;
    __builtin_amdgcn_s_sleep(POLL_SLEEP);
  }
  __builtin_amdgcn_fence(__ATOMIC_ACQUIRE, "wavefront");
  CFENCE();
}
DI void put_flag(char* ws, unsigned line, u64 v) { __hip_atomic_store((GAS u64*)(ws + (size_t)line * 128), v, RLX_AGENT); }

DI void dma4(const unsigned (&voff)[4], const char* sb, unsigned dst) {
  unsigned keep;
  asm volatile("s_mov_b32 %0, m0\n\ts_mov_b32 m0, %6\n\ts_nop 0\n\t"
               "global_load_lds_dwordx4 %1, %5\n\t"
               "global_load_lds_dwordx4 %2, %5 offset:1024\n\t"
               "global_load_lds_dwordx4 %3, %5 offset:2048\n\t"
               "global_load_lds_dwordx4 %4, %5 offset:3072\n\t"
               "s_mov_b32 m0, %0"
               : "=&s"(keep) : "v"(voff[0]), "v"(voff[1]), "v"(voff[2]), "v"(voff[3]), "s"(sb), "s"(dst) : "memory");
}
DI int wt_pos(int u) {
  const int k0 = u & 63, u1 = u >> 6;
  return (((k0 >> 4) * 64 + ((u1 >> 2) & 3) * 16 + (k0 & 15)) << 3) + (u1 >> 4) * 4 + (u1 & 3);
}
DI int wt_row(int pos) {
  const int bt = pos >> 3;
  return 16 * (bt >> 6) + (bt & 15) + 64 * (16 * ((pos >> 2) & 1) + 4 * ((bt >> 4) & 3) + (pos & 3));
}
DI void trig4(float f, float i0, f32x4& co, f32x4& si) {
#pragma unroll
  for (int e = 0; e < 4; ++e) {
    const float a = f * (i0 + (float)e);
    co[e] = __builtin_amdgcn_cosf(a); si[e] = __builtin_amdgcn_sinf(a);
  }
}
DI void trig_frag(float f, float i0, u32x4& co, u32x4& si) {
  f32x4 c0, s0, c1, s1;
  trig4(f, i0, c0, s0); trig4(f, i0 + 16.f, c1, s1);
  co = pack8(c0, c1); si = pack8(s0, s1);
  PIN(co); PIN(si);
}

#define LD128(off) __builtin_amdgcn_raw_buffer_load_b128(rs, (off), 0, 16)
#define LD32(off) __builtin_amdgcn_raw_buffer_load_b32(rs, (off), 0, 16)
#define ST128(v, off) __builtin_amdgcn_raw_buffer_store_b128((v), rs, (off), 0, 16)
#define ST64(v, off) __builtin_amdgcn_raw_buffer_store_b64((v), rs, (off), 0, 16)
#define ST32(v, off) __builtin_amdgcn_raw_buffer_store_b32((v), rs, (off), 0, 16)

__global__ __launch_bounds__(512, 2) void fused_kernel(const char* __restrict__ x, const char* __restrict__ wqkv,
                                                       const char* __restrict__ wo, const char* __restrict__ w_r,
                                                       const char* __restrict__ w_i, const char* __restrict__ gate_w,
                                                       char* __restrict__ ws, char* __restrict__ out) {
  extern __shared__ __attribute__((aligned(16))) char smem[];
  LAS char* sm = (LAS char*)smem;
  asm volatile("" :: "s"(x), "s"(wqkv), "s"(wo), "s"(w_r), "s"(w_i), "s"(gate_w), "s"(ws), "s"(out), "s"(sm));
  u64 pc0;
  asm volatile("s_getpc_b64 %0" : "=s"(pc0));
  pc0 &= ~127ull;
  const int tid = threadIdx.x, lane = tid & 63;
  const int w = __builtin_amdgcn_readfirstlane(tid >> 6);
  const int bid = blockIdx.x, xcd = bid & 7, idx = bid >> 3;
  const int mt = (xcd & 3) * 8 + (idx & 7), ct = (xcd >> 2) * 4 + (idx >> 3);
  const int rb = xcd * 8 + (idx & 7), cb = idx >> 3;
  const int c = lane & 15, g = lane >> 4;
  const __amdgpu_buffer_rsrc_t rs = __builtin_amdgcn_make_buffer_rsrc(ws, 0, WS_END, 0x00020000);
  const f32x4 zero = {0, 0, 0, 0};
  unsigned sigp = L_SYNC + (w & 3) * 4;
  PIN(sigp);
  ST(0);
  const int i8 = idx & 7, c0 = (idx + (idx >> 3)) & 3, flip = (idx >> 2) & 1;
  if (w >= 4) {
    const int wb = w - 4, bt = tid - 256;
    unsigned vx[4], vk[4], vq[4];
    const unsigned dst0 = wb * 4096;
#pragma unroll
    for (int i = 0; i < 4; ++i) {
      const int r = 16 * wb + 4 * i + g;
      vx[i] = (unsigned)((mt + 32 * r) * 1024 + ((c ^ (r & 15)) << 4) + 4096 - i * 1024);
    }
    auto issue_x = [&](int i) { dma4(vx, x - 4096 + ((c0 - i) & 3) * 256, dst0 + i * 16384); };
    issue_x(0);
#pragma unroll
    for (int i = 0; i < 4; ++i) {
      const int r = 16 * wb + 4 * i + g, sl = c ^ ((g | ((i >> 1) << 2)) << 1);
      vk[i] = (unsigned)(r * 3072 + (sl >> 3) * 1024 + ((sl & 7) << 4) + 4096 - i * 1024);
    }
    auto issue_kv = [&](int j) {
      const int blk = 2 * ((c0 - (j >> 1)) & 3) + ((j & 1) ^ flip);
      dma4(vk, wqkv - 4096 + 1024 + ct * 128 + (unsigned)blk * (64 * 3072), dst0 + ((j + 4) & 7) * 16384);
    };
    u32x4 gw[4];
#pragma unroll
    for (int i = 0; i < 4; ++i)
      asm volatile("global_load_dwordx4 %0, %1, %2" : "=v"(gw[i]) : "v"(c * 16), "s"(gate_w + ((c0 - i) & 3) * 256) : "memory");
    issue_kv(0);
    issue_x(1);
    if (wb == 0) *(LAS u32x2*)(sm + L_SYNC + lane * 8) = u32x2{0u, 0u};
#pragma unroll
    for (int i = 0; i < 4; ++i) {
      const int r = 16 * wb + 4 * i + g, sl = c ^ ((g | ((i >> 1) << 2)) << 1);
      vq[i] = (unsigned)(r * 3072 + ((sl >> 3) ^ flip) * (64 * 3072) + ((sl & 7) << 4) + 4096 - i * 1024);
      PIN(vq[i]);
    }
    auto issue_q = [&](int j) { dma4(vq, wqkv - 4096 + ct * 128 + (unsigned)((c0 - j) & 3) * (128 * 3072), dst0 + (j + 4) * 16384); };
    const float g4 = (float)(4 * g);
    u32x4 m0c[2], m0s[2], m0n[2], mac[2], mas[2], mcc[2][2], mcs[2][2];
    f32x4 w1c[2], w1s[2], w2c[2], w2s[2];
#pragma unroll
    for (int i = 0; i < 2; ++i) {
      trig_frag((float)(16 * wb + c) * (1.f / 64.f), g4 + 32.f * i, m0c[i], m0s[i]);
      m0n[i] = neg8(m0s[i]);
    }
    const int gr = 8 * ct + 2 * wb + (g & 1);
    unsigned gxp = gr * 256 + ((c ^ (gr & 15)) << 4);
    PIN(gxp);
    u32x4 gx[4];
    u64 epoch;
    asm volatile("s_load_dwordx2 %0, %1, 0x0\n\ts_waitcnt lgkmcnt(0)" : "=s"(epoch) : "s"(ws) : "memory");
    const u64 tag = TAG_OF(epoch);
    VMW(12); BAR();
    gx[0] = ld16(gxp, 0);
    issue_x(2); issue_x(3);
    asm volatile("s_waitcnt vmcnt(8)" : "+v"(gw[0]), "+v"(gw[1]), "+v"(gw[2]), "+v"(gw[3]) :: "memory");
    BAR();
    gx[1] = ld16(gxp, 16384);
    issue_kv(1); issue_kv(2);
    VMW(12); BAR();
    gx[2] = ld16(gxp, 32768);
    issue_kv(3); issue_kv(4);
    VMW(16); BAR();
    gx[3] = ld16(gxp, 49152);
    issue_kv(5); issue_kv(6);
#pragma unroll
    for (int i = 0; i < 2; ++i) trig_frag((float)(16 * i + c) * (1.f / 32.f), g4, mac[i], mas[i]);
    VMW(16); BAR();
    issue_kv(7); issue_q(0);
    unsigned pfw;
    asm volatile("global_load_dword %0, %1, %2" : "=v"(pfw) : "v"((lane & 31) * 128), "s"(wo + idx * 16384 + wb * 4096) : "memory");
#pragma unroll
    for (int tt = 0; tt < 2; ++tt) {
      trig4((float)(16 * wb + c) * (1.f / 2048.f), g4 + 16.f * tt, w1c[tt], w1s[tt]);
      PIN(w1c[tt]); PIN(w1s[tt]);
    }
    VMW(17); BAR();
    issue_q(1); issue_q(2);
#pragma unroll
    for (int tt = 0; tt < 2; ++tt) {
      trig4((float)(16 * tt + c) * (1.f / 2048.f), g4 + 16.f * wb, w2c[tt], w2s[tt]);
      PIN(w2c[tt]); PIN(w2s[tt]);
    }
    VMW(13); BAR();
    issue_q(3);
    ST(1);
    {
      float s = 0.f;
#pragma unroll
      for (int i = 0; i < 4; ++i)
#pragma unroll
        for (int j = 0; j < 4; ++j) s += bflo(gx[i][j]) * bflo(gw[i][j]) + bfhi(gx[i][j]) * bfhi(gw[i][j]);
      s = row_sum16(s);
      if ((lane & 47) == 0) ST32(__float_as_uint(__builtin_amdgcn_rcpf(1.f + __expf(-s))), WS_G + (64 * mt + gr) * 4);
    }
#pragma unroll
    for (int i = 0; i < 4; ++i)
      trig_frag((float)(32 * (wb >> 1) + 8 * (c >> 2) + 4 * (i >> 1) + (c & 3)) * (1.f / 64.f), g4 + 32.f * (i & 1), mcc[i >> 1][i & 1], mcs[i >> 1][i & 1]);
    asm volatile("s_waitcnt vmcnt(0)" : "+v"(pfw) :: "memory");
    SIG(E_QL);
    const int k = 32 * ct + mt;
    unsigned o_x = (unsigned)(WS_XT + k * 8192 + bt * 16), o_w = (unsigned)(WS_WT + k * 8192 + bt * 32);
    unsigned l_xw = L_SCR + (bt >> 4) * 256 + ((c ^ (bt >> 4)) << 4);
    unsigned l_ca[4];
#pragma unroll
    for (int i = 0; i < 4; ++i) { l_ca[i] = L_SCR + c * 256 + (((4 * i + g) ^ c) << 4); PIN(l_ca[i]); }
    unsigned l_ew = L_SCR + 8192 + (wb >> 1) * 4096 + lane * 16 + (wb & 1) * 8, l_er = L_SCR + 8192 + (wb & 1) * 2048 + lane * 16;
    unsigned o_z = (unsigned)(WS_Z + k * 8192 + (2 * (16 * (wb & 1) + c) + (wb >> 1)) * 128 + g * 16);
    PIN(o_x); PIN(o_w); PIN(l_xw); PIN(l_ew); PIN(l_er); PIN(o_z);
    ST(2);
    if (wb == 0) { poll_all(ws, LN_FB + ct * 32 + (lane & 31), tag); SIG(E_FB); }
    else ev_wait<1>(E_FB);
    __builtin_amdgcn_s_setprio(2);
    ST(3);
    {
      const u32x4 x0 = LD128(o_x), x1 = LD128(o_x + 4096);
      const u32x4 wv[2] = {LD128(o_w), LD128(o_w + 16)};
      u32x4 ar, ai, an;
      u32x4 ca[2][2][2];
      auto twiddle_dft32 = [&](const f32x4 (&yr)[2], const f32x4 (&yi)[2], f32x4 (&xr)[2], f32x4 (&xi)[2]) {
        const f32x4 r0 = yr[0] * w1c[0] + yi[0] * w1s[0], r1 = yr[1] * w1c[1] + yi[1] * w1s[1];
        const u32x4 br = pack8(r0, r1), bn = pack8(-r0, -r1);
        const u32x4 bi = pack8(yi[0] * w1c[0] - yr[0] * w1s[0], yi[1] * w1c[1] - yr[1] * w1s[1]);
#pragma unroll
        for (int ut = 0; ut < 2; ++ut) {
          xr[ut] = mfma32(mas[ut], bi, mfma32(mac[ut], br, zero));
          xi[ut] = mfma32(mas[ut], bn, mfma32(mac[ut], bi, zero));
        }
      };
      if (k != 0) {
        *LP(u32x4, l_xw) = x0; *(LAS u32x4*)(LP(char, l_xw) + 4096) = x1;
        SIG(E_X1);
        f32x4 fr[2] = {lo4(wv[0]), lo4(wv[1])}, fi[2] = {hi4(wv[0]), hi4(wv[1])};
        PIN(fr[0]); PIN(fr[1]); PIN(fi[0]); PIN(fi[1]);
        ST(4);
        ev_wait<15>(E_X1);
#pragma unroll
        for (int i = 0; i < 8; ++i) ca[i >> 2][(i >> 1) & 1][i & 1] = ld16(l_ca[i >> 1], (i & 1) * 4096);
        f32x4 yr[2] = {zero, zero}, yi[2] = {zero, zero}, xr[2], xi[2];
#pragma unroll
        for (int ks = 0; ks < 2; ++ks)
#pragma unroll
          for (int tt = 0; tt < 2; ++tt) { yr[tt] = mfma32(ca[0][ks][tt], m0c[ks], yr[tt]); yi[tt] = mfma32(ca[0][ks][tt], m0n[ks], yi[tt]); }
#pragma unroll
        for (int ks = 0; ks < 2; ++ks)
#pragma unroll
          for (int tt = 0; tt < 2; ++tt) { yr[tt] = mfma32(ca[1][ks][tt], m0s[ks], yr[tt]); yi[tt] = mfma32(ca[1][ks][tt], m0c[ks], yi[tt]); }
        twiddle_dft32(yr, yi, xr, xi);
        const f32x4 i0 = xr[0] * fi[0] + xi[0] * fr[0], i1 = xr[1] * fi[1] + xi[1] * fr[1];
        ar = pack8(xr[0] * fr[0] - xi[0] * fi[0], xr[1] * fr[1] - xi[1] * fi[1]);
        ai = pack8(i0, i1); an = pack8(-i0, -i1);
      } else {
        const u32x4 wn[2] = {LD128(o_w + 256 * 8192), LD128(o_w + 256 * 8192 + 16)};
        unsigned mp[8];
#pragma unroll
        for (int e = 0; e < 8; ++e) { mp[e] = 4 * wt_pos((2048 - (16 * wb + c + 64 * (16 * (e >> 2) + 4 * g + (e & 3)))) & 2047); PIN(mp[e]); }
        *LP(u32x4, l_xw) = x0; *(LAS u32x4*)(LP(char, l_xw) + 4096) = x1;
        LAS u32x4* d = (LAS u32x4*)sm + bt * 2;
        d[0] = wv[0]; d[1] = wv[1]; d[512] = wn[0]; d[513] = wn[1];
        SIG(E_X1);
        ev_wait<15>(E_X1);
#pragma unroll
        for (int i = 0; i < 8; ++i) ca[i >> 2][(i >> 1) & 1][i & 1] = ld16(l_ca[i >> 1], (i & 1) * 4096);
        u32x4 m0[2], m1[2];
#pragma unroll
        for (int e = 0; e < 8; ++e) { m0[e >> 2][e & 3] = *LP(unsigned, mp[e]); m1[e >> 2][e & 3] = *(LAS unsigned*)(LP(char, mp[e]) + 8192); }
        f32x4 yr[2] = {zero, zero}, yi[2] = {zero, zero}, xr[2], xi[2], dr[2], di[2];
#pragma unroll
        for (int ks = 0; ks < 2; ++ks)
#pragma unroll
          for (int tt = 0; tt < 2; ++tt) { yr[tt] = mfma32(ca[0][ks][tt], m0c[ks], yr[tt]); yi[tt] = mfma32(ca[0][ks][tt], m0n[ks], yi[tt]); }
        twiddle_dft32(yr, yi, xr, xi);
#pragma unroll
        for (int ut = 0; ut < 2; ++ut) {
          const f32x4 fr = lo4(wv[ut]) + lo4(m0[ut]), fi = hi4(wv[ut]) - hi4(m0[ut]);
          dr[ut] = xr[ut] * fr - xi[ut] * fi; di[ut] = xr[ut] * fi + xi[ut] * fr;
          yr[ut] = zero; yi[ut] = zero;
        }
#pragma unroll
        for (int ks = 0; ks < 2; ++ks)
#pragma unroll
          for (int tt = 0; tt < 2; ++tt) { yr[tt] = mfma32(ca[1][ks][tt], m0s[ks], yr[tt]); yi[tt] = mfma32(ca[1][ks][tt], m0c[ks], yi[tt]); }
        twiddle_dft32(yr, yi, xr, xi);
#pragma unroll
        for (int ut = 0; ut < 2; ++ut) {
          const f32x4 fr = lo4(wn[ut]) + lo4(m1[ut]), fi = hi4(wn[ut]) - hi4(m1[ut]);
          dr[ut] += xr[ut] * fr - xi[ut] * fi; di[ut] += xr[ut] * fi + xi[ut] * fr;
        }
        ar = pack8(dr[0], dr[1]); ai = pack8(di[0], di[1]); an = pack8(-di[0], -di[1]);
      }
      ST(5);
#pragma unroll
      for (int tt = 0; tt < 2; ++tt) {
        const f32x4 vr = mfma32(an, mas[tt], mfma32(ar, mac[tt], zero));
        const f32x4 vi = mfma32(ar, mas[tt], mfma32(ai, mac[tt], zero));
        const f32x4 pr = vr * w2c[tt] - vi * w2s[tt], pi = vi * w2c[tt] + vr * w2s[tt];
        *(LAS u32x2*)(LP(char, l_ew) + tt * 2048) = u32x2{pk2(pr[0], pr[1]), pk2(pr[2], pr[3])};
        *(LAS u32x2*)(LP(char, l_ew) + tt * 2048 + 1024) = u32x2{pk2(pi[0], pi[1]), pk2(pi[2], pi[3])};
      }
      SIG(E_X2);
      ev_wait<15>(E_X2);
      f32x4 zr[2] = {zero, zero}, zi[2] = {zero, zero}, zs[2] = {zero, zero};
#pragma unroll
      for (int ks = 0; ks < 2; ++ks) {
        const u32x4 vr = ld16(l_er, ks * 4096), vi = ld16(l_er, ks * 4096 + 1024);
#pragma unroll
        for (int jt = 0; jt < 2; ++jt) {
          zr[jt] = mfma32(mcc[jt][ks], vr, zr[jt]); zi[jt] = mfma32(mcs[jt][ks], vr, zi[jt]);
          zs[jt] = mfma32(mcs[jt][ks], vi, zs[jt]); zi[jt] = mfma32(mcc[jt][ks], vi, zi[jt]);
        }
      }
      ST128(pack8(zr[0] - zs[0], zr[1] - zs[1]), o_z);
      ST128(pack8(zi[0], zi[1]), o_z + 64);
    }
    __builtin_amdgcn_s_setprio(0);
    ST(6);
    u32x4 ga[16];
    const float df = (float)(64 * cb + 16 * wb + c) * (1.f / 512.f);
    auto gen = [&](int f) {
#pragma unroll
      for (int i = 0; i < 4; ++i) {
        const float a = (2.f * g4 + (float)(32 * (f & 7) + 2 * i + (f >> 3))) * df;
        ga[f][i] = pk2(__builtin_amdgcn_cosf(a), __builtin_amdgcn_sinf(-a));
      }
      if (f == 0 && g == 0) ga[0][0] = (c & 1) ? 0xBE803E80u : 0x3E803E80u;
      PIN(ga[f]);
    };
#pragma unroll
    for (int f = 0; f < 6; ++f) gen(f);
    SB();
    DRAIN();
    SIG(E_ZD);
    if (wb == 0) {
      ev_wait<15>(E_ZD);
      if (lane < REP) __hip_atomic_fetch_add((GAS u64*)(ws + (size_t)(LN_CZ + lane * 16 + 2 * ct + (mt >> 4)) * 128), 1ull, RLX_AGENT);
    }
    ST(7);
#pragma unroll
    for (int f = 6; f < 16; ++f) gen(f);
    unsigned o_zl = (unsigned)(WS_Z + (64 * wb + (lane >> 3)) * 8192 + rb * 128 + (lane & 7) * 16);
    unsigned l_zw, l_z0, l_z1;
    {
      const int kk = 64 * wb + (lane >> 3), R = (((kk & 1) * 128 + (kk >> 1)) << 1) + ((lane >> 2) & 1);
      l_zw = R * 64 + ((lane & 3) << 4);
      l_z0 = (8 * g + (c >> 2)) * 64 + 8 * (c & 3) + ((g & 1) << 5);
      l_z1 = l_z0 ^ 32u;
    }
    unsigned l_zw1 = l_zw ^ 32u;
    PIN(o_zl); PIN(l_zw); PIN(l_zw1); PIN(l_z0); PIN(l_z1);
    ev_wait<15>(E_WR);
    ST(11);
    if (wb == 0) { poll_all(ws, LN_CZ + (bid % REP) * 16 + c, tag + 16); SIG(E_FZ); }
    else ev_wait<1>(E_FZ);
    ST(8);
    {
      u32x4 z[8];
#pragma unroll
      for (int i = 0; i < 8; ++i) z[i] = __builtin_amdgcn_raw_buffer_load_b128(rs, o_zl, i * 65536, 16);
#pragma unroll
      for (int i = 0; i < 8; ++i) *(LAS u32x4*)(LP(char, (i & 1) ? l_zw1 : l_zw) + i * 512) = z[i];
    }
    SIG(E_ZI);
    ST(9);
    ev_wait<15>(E_ZI);
    f32x4 acc[2][2] = {{zero, zero}, {zero, zero}};
    {
      u32x4 zf[3][2][2];
      auto ldz = [&](int n) {
#pragma unroll
        for (int i = 0; i < 2; ++i) { zf[n % 3][i][0] = trfrag(l_z0, (2 * n + i) * 2048, 256); zf[n % 3][i][1] = trfrag(l_z1, (2 * n + i) * 2048, 256); }
      };
      ldz(0); ldz(1);
      SB();
#pragma unroll
      for (int n = 0; n < 8; ++n) {
        if (n < 6) ldz(n + 2);
#pragma unroll
        for (int i = 0; i < 2; ++i) {
          acc[n >> 2][0] = mfma32(ga[2 * n + i], zf[n % 3][i][0], acc[n >> 2][0]);
          acc[n >> 2][1] = mfma32(ga[2 * n + i], zf[n % 3][i][1], acc[n >> 2][1]);
        }
#pragma unroll
        for (int i = 0; i < 4; ++i) { __builtin_amdgcn_sched_group_barrier(0x100, 2, 0); __builtin_amdgcn_sched_group_barrier(0x8, 1, 0); }
        SB();
      }
    }
    {
      LAS f32x4* X = (LAS f32x4*)(sm + L_SCR) + wb * 256 + lane;
      X[0] = acc[0][0] + acc[1][0]; X[64] = acc[0][1] + acc[1][1];
      X[128] = acc[0][0] - acc[1][0]; X[192] = acc[0][1] - acc[1][1];
    }
    ST(10);
    BAR();
  } else {
    __builtin_amdgcn_s_setprio(1);
    u32x4 wrv, wiv;
    unsigned nyq = 0, nyq2 = 0, pf = 0;
    {
      const unsigned o_w = (unsigned)(wt_row(64 * mt + (tid >> 2)) * 257 + 32 * ct + 8 * (tid & 3)) * 2u;
      wrv = *(const u32x4u*)(w_r + o_w);
      wiv = *(const u32x4u*)(w_i + o_w);
      if (ct == 0 && w == 0) {
        const unsigned o_ny = (unsigned)(wt_row(64 * mt + lane) * 257 + 256) * 2u;
        nyq = *(const unsigned short*)(w_r + o_ny); nyq2 = *(const unsigned short*)(w_i + o_ny);
      }
    }
    if (w == 2 && lane < CODE_LINES) pf = *(const GAS unsigned*)(pc0 + (idx * CODE_LINES + lane) * 128);
    const u32x2 ev = *(const u32x2*)ws;
    const int rec = (2 * ct + (w >> 1)) * 64 + 2 * mt + (w & 1);
    unsigned o_kp = (unsigned)(WS_KVP + rec * 1024 + lane * 16), o_zp = (unsigned)(WS_ZP + rec * 64 + g * 16);
    ST128((u32x4{~0u, ~0u, ~0u, ~0u}), o_kp);
    if (c == 0) ST128((u32x4{~0u, ~0u, ~0u, ~0u}), o_zp);
    SB();
    const int rh = w & 1, kt = w >> 1;
    const int kf = 32 * ct + 16 * kt + c;
    u32x4 xf[4][4][2];
    unsigned bb[2][4];
    {
      const unsigned fl = (unsigned)((c >> 2) | ((g & 1) << 2));
      const unsigned bu = (unsigned)((8 * g + (c >> 2)) * 256 + 8 * (c & 3)) + (((unsigned)(2 * kt) ^ fl) << 5);
#pragma unroll
      for (int u = 0; u < 4; ++u) {
        bb[0][u] = bu ^ (unsigned)((u & 1) * 32 + (u >> 1) * 128);
        bb[1][u] = bb[0][u] + 65536u;
      }
    }
    u32x4 fb[3][4];
    auto ldf = [&](int n) {
#pragma unroll
      for (int u = 0; u < 4; ++u) {
        const int slot = (4 + (n >> 1)) & 7;
        fb[n % 3][u] = trfrag(bb[slot >> 2][u], (slot & 3) * 16384 + (n & 1) * 8192, 1024);
      }
    };
    u64 tag;
    {
      u32x4 fc[4], fs[4];
      {
        const float kk = (float)kf * (1.f / 512.f);
        const float a0 = __builtin_amdgcn_fractf(kk * (float)(8 * g + 128 * ((c0 + 1) & 3)));
#pragma unroll
        for (int ks = 0; ks < 4; ++ks) {
          const float ak = __builtin_fmaf(kk, (float)(((ks & 2) ^ (flip << 1)) * 32), a0);
#pragma unroll
          for (int i = 0; i < 4; ++i) {
            const float p0 = __builtin_fmaf(kk, (float)(32 * (ks & 1) + 2 * i), ak), p1 = __builtin_fmaf(kk, (float)(32 * (ks & 1) + 2 * i + 1), ak);
            fc[ks][i] = pk2(__builtin_amdgcn_cosf(p0), __builtin_amdgcn_cosf(p1));
            fs[ks][i] = kf == 0 ? 0xBF803F80u
                                : pk2(__builtin_amdgcn_sinf(-p0), __builtin_amdgcn_sinf(-p1));
          }
          PIN(fc[ks]); PIN(fs[ks]);
        }
      }
      float ra = (c & 1) ? 0.f : ((c & 2) ? -1.f : 1.f), rbi = (c & 1) ? ((c & 2) ? 1.f : -1.f) : 0.f;
      unsigned l_sc = L_SCR + ((tid & 3) * 512 + ((tid >> 2) ^ ((tid & 3) << 3))) * 4;
      unsigned l_rd = L_SCR + (tid >> 4) * 256 + (((tid & 15) ^ ((tid >> 7) << 1)) << 4);
      unsigned o_wt = (unsigned)(WS_WT + (32 * ct + (tid >> 4)) * 8192 + (64 * mt + 4 * (tid & 15)) * 4);
      unsigned o_xt = (unsigned)(WS_XT + kf * 8192 + mt * 256 + rh * 64 + g * 16);
      unsigned xa[4];
#pragma unroll
      for (int ks = 0; ks < 4; ++ks) { xa[ks] = rh * 8192 + c * 256 + (((4 * ks + g) ^ (flip << 3) ^ c) << 4); PIN(xa[ks]); }
#pragma unroll
      for (int u = 0; u < 4; ++u) { PIN(bb[0][u]); PIN(bb[1][u]); }
      PIN(l_sc); PIN(l_rd); PIN(o_wt); PIN(o_xt); PIN(ra); PIN(rbi); PIN(o_kp); PIN(o_zp);
      ST(1);
      f32x4 xr[4][2], xi[4][2];
      auto ldx = [&](int s) {
#pragma unroll
        for (int ks = 0; ks < 4; ++ks)
#pragma unroll
          for (int tt = 0; tt < 2; ++tt) xf[s][ks][tt] = ld16(xa[ks], s * 16384 + tt * 4096);
      };
      auto mmf = [&](int s) {
#pragma unroll
        for (int tt = 0; tt < 2; ++tt) { xr[s][tt] = zero; xi[s][tt] = zero; }
#pragma unroll
        for (int ks = 0; ks < 4; ++ks)
#pragma unroll
          for (int tt = 0; tt < 2; ++tt) {
            xr[s][tt] = mfma32(xf[s][ks][tt], fc[ks], xr[s][tt]);
            xi[s][tt] = mfma32(xf[s][ks][tt], fs[ks], xi[s][tt]);
          }
      };
      auto horner = [&](int s) {
#pragma unroll
        for (int tt = 0; tt < 2; ++tt) {
          xr[s][tt] = (xr[s][tt] + xr[s - 1][tt] * ra) - xi[s - 1][tt] * rbi;
          xi[s][tt] = (xi[s][tt] + xr[s - 1][tt] * rbi) + xi[s - 1][tt] * ra;
        }
      };
      auto mixv = [&]() {
#pragma unroll
        for (int i = 0; i < 16; ++i) { __builtin_amdgcn_sched_group_barrier(0x8, 1, 0); __builtin_amdgcn_sched_group_barrier(0x2 | 0x80 | 0x40, 2, 0); }
        SB();
      };
      BAR();
      ldx(0);
      {
        PIN(pf); PIN(nyq); PIN(nyq2);
        tag = TAG_OF(((u64)(unsigned)__builtin_amdgcn_readfirstlane(ev.y) << 32) | (unsigned)__builtin_amdgcn_readfirstlane(ev.x));
        LAS unsigned* sc = LP(unsigned, l_sc);
#pragma unroll
        for (int j = 0; j < 4; ++j) {
          sc[(2 * j) * 64] = __builtin_amdgcn_perm(wiv[j], wrv[j], 0x05040100u);
          sc[(2 * j + 1) * 64] = __builtin_amdgcn_perm(wiv[j], wrv[j], 0x07060302u);
        }
        if (ct == 0 && w == 0) ST32(nyq | (nyq2 << 16), WS_WT + 256 * 8192 + (64 * mt + lane) * 4);
        if (mt == 0 && w == 1 && lane < 4 * REP)
          put_flag(ws, LN_CZ + (lane >> 1) * 16 + 2 * ct + (lane & 1), tag);
      }
      BAR();
      const u32x4 wt0 = ld16(l_rd, 0), wt1 = ld16(l_rd ^ 64u, 4096);
      ldx(1); SB(); mmf(0);
      BAR();
      ldx(2); SB(); mmf(1);
      BAR();
      ldx(3); SB();
      mmf(2); horner(1);
      ST128(wt0, o_wt); ST128(wt1, o_wt + 16 * 8192);
      mixv();
      mmf(3); horner(2);
      mixv();
      ldf(0); ldf(1);
      horner(3);
      ST128(pack8(xr[3][0], xr[3][1]), o_xt);
      ST128(pack8(xi[3][0], xi[3][1]), o_xt + 128);
    }
    ST(2);
    auto mix = [&]() {
#pragma unroll
      for (int i = 0; i < 8; ++i) { __builtin_amdgcn_sched_group_barrier(0x100, 1, 0); __builtin_amdgcn_sched_group_barrier(0x8, 1, 0); }
      SB();
    };
    {
      f32x4 acc[2][4];
#pragma unroll
      for (int t = 0; t < 2; ++t)
#pragma unroll
        for (int u = 0; u < 4; ++u) acc[t][u] = zero;
#pragma unroll
      for (int n = 0; n < 16; ++n) {
        if (n == 8) DRAIN();
        if (n == 0 || n == 4 || n == 8) BAR();
        if (n == 8) { if (tid == 0) put_flag(ws, LN_FB + ct * 32 + mt, tag); SB(); }
        if (n < 14) ldf(n + 2);
#pragma unroll
        for (int u = 0; u < 4; ++u)
#pragma unroll
          for (int t = 0; t < 2; ++t) acc[t][u] = mfma32(xf[n >> 2][n & 3][t], fb[n % 3][u], acc[t][u]);
        mix();
      }
      ST(3);
      const u32x4 ones = {0x3f803f80u, 0x3f803f80u, 0x3f803f80u, 0x3f803f80u};
      f32x4 kv[2], zz[2];
#pragma unroll
      for (int n = 0; n < 2; ++n) {
        const u32x4 pk = pack8(phi4(acc[0][n]), phi4(acc[1][n]));
        kv[n] = mfma32(pk, pack8(acc[0][2 + n], acc[1][2 + n]), zero);
        zz[n] = mfma32(pk, ones, zero);
      }
      u32x4 kvo = pack8(kv[0], kv[1]), zo = pack8(zz[0], zz[1]);
#pragma unroll
      for (int e = 0; e < 4; ++e) {
        kvo[e] = min(kvo[e], 0xfffffffeu);
        zo[e] = min(zo[e], 0xfffffffeu);
      }
      ST128(kvo, o_kp);
      if (c == 0) ST128(zo, o_zp);
    }
    ST(4);
    {
      const int hp = 2 * ct + (mt >> 4), sub = mt & 15;
      u32x4 pv[4];
      unsigned pz;
      auto ldp = [&]() {
#pragma unroll
        for (int i = 0; i < 4; ++i)
          pv[i] = __builtin_amdgcn_raw_buffer_load_b128(rs, WS_KVP + (hp * 64 + (lane >> 2)) * 1024 + sub * 64 + (lane & 3) * 16, i * 16384, 16);
        pz = LD32(WS_ZP + (hp * 64 + lane) * 64 + sub * 4);
      };
      f32x4 acc[2][2] = {{zero, zero}, {zero, zero}};
      ev_wait<15>(E_QL);
      SB();
      ldf(0); ldf(1);
      SB();
#pragma unroll
      for (int n = 0; n < 8; ++n) {
        if (n < 6) ldf(n + 2);
        if (n == 6) {
          SIG(E_QR);
          if (w == 0) { ev_wait<1>(E_FB); ldp(); }
          SB();
        }
#pragma unroll
        for (int u = 0; u < 4; ++u)
#pragma unroll
          for (int t = 0; t < 2; ++t) acc[u & 1][t] = mfma32(fb[n % 3][u], xf[n >> 1][2 * (u >> 1) + (n & 1)][t], acc[u & 1][t]);
        mix();
      }
#pragma unroll
      for (int n = 0; n < 2; ++n)
        ST128(pack8(phi4(acc[n][0]), phi4(acc[n][1])), WS_PQ + ((2 * mt + rh) * 32 + 4 * ct + 2 * kt + n) * 1024 + lane * 16);
      SB();
      ST(5);
      if (w == 0) {
        for (unsigned spins = 0; spins < SPIN_CAP; ++spins) {
          unsigned m = pz;
#pragma unroll
          for (int i = 0; i < 4; ++i) m = max(max(m, max(pv[i][0], pv[i][1])), max(pv[i][2], pv[i][3]));
          if (__all(m != ~0u)) break;
          CFENCE();
          ldp();
        }
        ST(6);
        const unsigned one = (c & 1) ? 0x3f800000u : 0x00003f80u;
        const u32x4 sel = {(c >> 1) == 0 ? one : 0u, (c >> 1) == 1 ? one : 0u, (c >> 1) == 2 ? one : 0u, (c >> 1) == 3 ? one : 0u};
        f32x4 s = zero;
#pragma unroll
        for (int i = 0; i < 4; ++i) s = mfma32(sel, pv[i], s);
        const f32x4 zq = mfma32(sel, u32x4{pz, 0u, 0u, 0u}, zero);
#pragma unroll
        for (int e = 0; e < 4; ++e) s[e] = dpp_add<0x128>(dpp_add<0x124>(s[e]));
        const float z0 = row_sum16(zq[0]), z1 = row_sum16(zq[1]);
        if ((lane & 44) == 0) ST64((u32x2{pk2(s[0], s[1]), pk2(s[2], s[3])}), WS_KVF + hp * 1024 + (sub * 4 + c) * 16 + g * 8);
        if (lane == 0) ST32(pk2(z0, z1), WS_ZF + hp * 64 + sub * 4);
        VMW(2);
        ev_wait<14>(E_PQ);
        if (lane == 0) put_flag(ws, LN_FQ + mt * 8 + ct, tag);
        VMW(1);
        if (lane < REP) __hip_atomic_fetch_add((GAS u64*)(ws + (size_t)(LN_CK + lane * 16 + 2 * ct + (mt >> 4)) * 128), 1ull, RLX_AGENT);
        SIG(E_KA);
      } else {
        unsigned voff[4];
#pragma unroll
        for (int i = 0; i < 4; ++i) {
          const int r = 4 * i + g, sl = c ^ ((r & 7) << 1);
          voff[i] = (unsigned)(r * 1024 + (sl >> 3) * 512 + ((sl & 7) << 4) + 4096 - i * 1024);
        }
        ev_wait<15>(E_QR);
#pragma unroll 1
        for (int gj = 3 + w; gj < 36; gj += 3) {
          const int gi = gj & 31;
          if (gj >= 32) ev_wait<15>(E_X2);
          dma4(voff, wo - 4096 + cb * 128 + ((gi + 4 * i8) & 31) * 16384, gi * 4096);
          VMW(WO_THR);
          if (gj < 7) { VMW(4); SIG(E_PQ); }
        }
        DRAIN();
        SIG(E_WL);
      }
    }
    __builtin_amdgcn_s_setprio(0);
    ST(7);
    u32x4 wa[16][2];
    {
      const unsigned f7 = (unsigned)((4 * g + (c >> 2)) & 7);
      unsigned pw[2][2];
#pragma unroll
      for (int hf = 0; hf < 2; ++hf) {
        pw[hf][0] = (4 * g + (c >> 2)) * 256 + 8 * (c & 3) + ((((unsigned)w + 4u * hf) ^ f7) << 5);
        pw[hf][1] = pw[hf][0] + 65536u;
        PIN(pw[hf][0]); PIN(pw[hf][1]);
      }
      ev_wait<14>(E_WL);
#pragma unroll
      for (int st = 0; st < 16; ++st)
#pragma unroll
        for (int hf = 0; hf < 2; ++hf) wa[st][hf] = trfrag(pw[hf][st >> 3], (st & 7) * 8192, 4096);
      SIG(E_WR);
    }
    SB();
    ST(8);
    if (w == 1 || w == 2) ev_wait<1>(E_KA);
    if (w == 1) { poll_all(ws, LN_FQ + (rb >> 1) * 8 + (lane & 7), tag); SIG(E_FQ); }
    else if (w == 2) {
      poll_all(ws, LN_CK + (bid % REP) * 16 + c, tag + 16);
      SIG(E_FK);
      if (bid == 0 && lane == 0) *(u32x2*)ws = u32x2{ev.x + 1u, ev.y};
    }
    ev_wait<2>(E_FQ);
    u32x4 pq[8];
#pragma unroll
    for (int i = 0; i < 8; ++i) pq[i] = LD128(WS_PQ + (rb * 32 + 8 * w + i) * 1024 + lane * 16);
    float gate[2];
#pragma unroll
    for (int tt = 0; tt < 2; ++tt) gate[tt] = __uint_as_float(LD32(WS_G + (32 * rb + 16 * tt + c) * 4));
    unsigned l_o[4];
#pragma unroll
    for (int hp = 0; hp < 4; ++hp) { l_o[hp] = 65536u + ((4 * w + hp - 2 * i8) & 15) * 2048 + lane * 16; PIN(l_o[hp]); }
    unsigned o_kf = (unsigned)(WS_KVF + 4 * w * 1024 + lane * 16), o_zf = (unsigned)(WS_ZF + 4 * w * 64 + g * 16), l_or = 65536u + lane * 16;
    PIN(o_kf); PIN(o_zf); PIN(l_or);
    ev_wait<15>(E_WR);
    SB();
    ST(9);
    ev_wait<4>(E_FK);
    ST(10);
    {
      u32x4 kf4[4], zf4[4];
#pragma unroll
      for (int i = 0; i < 4; ++i) { kf4[i] = LD128(o_kf + i * 1024); zf4[i] = LD128(o_zf + i * 64); }
#pragma unroll
      for (int hp = 0; hp < 4; ++hp)
#pragma unroll
        for (int tt = 0; tt < 2; ++tt) {
          f32x4 o[2];
#pragma unroll
          for (int n = 0; n < 2; ++n) {
            const u32x4 pp = pq[2 * hp + n];
            const u32x2 b = tt ? u32x2{pp.z, pp.w} : u32x2{pp.x, pp.y};
            const u32x2 ka = n ? u32x2{kf4[hp].z, kf4[hp].w} : u32x2{kf4[hp].x, kf4[hp].y};
            const u32x2 za = n ? u32x2{zf4[hp].z, zf4[hp].w} : u32x2{zf4[hp].x, zf4[hp].y};
            const f32x4 num = mfma16(ka, b, zero), den = mfma16(za, b, f32x4{1e-6f, 1e-6f, 1e-6f, 1e-6f});
            o[n] = num * __builtin_amdgcn_rcpf(den[0]);
          }
          *(LAS u32x4*)(LP(char, l_o[hp]) + tt * 1024) = pack8(o[0], o[1]);
        }
    }
    SIG(E_O);
    ST(11);
    ev_wait<15>(E_O);
    f32x4 acc[2][2] = {{zero, zero}, {zero, zero}};
    {
#pragma unroll
      for (int st = 0; st < 16; ++st)
#pragma unroll
        for (int tt = 0; tt < 2; ++tt) {
          const u32x4 ob = ld16(l_or, (st * 2 + tt) * 1024);
          acc[0][tt] = mfma32(wa[st][0], ob, acc[0][tt]);
          acc[1][tt] = mfma32(wa[st][1], ob, acc[1][tt]);
        }
      __builtin_amdgcn_sched_group_barrier(0x100, 8, 0);
#pragma unroll
      for (int i = 0; i < 24; ++i) { __builtin_amdgcn_sched_group_barrier(0x8, 2, 0); __builtin_amdgcn_sched_group_barrier(0x100, 1, 0); }
      __builtin_amdgcn_sched_group_barrier(0x8, 16, 0);
    }
    ST(12);
    BAR();
    {
      const LAS f32x4* X = (const LAS f32x4*)(sm + L_SCR) + w * 256 + lane;
#pragma unroll
      for (int tt = 0; tt < 2; ++tt) {
        const int srow = (rb >> 1) + 32 * (32 * (rb & 1) + 16 * tt + c);
        const float sc = (1.f - gate[tt]) * (2.f / (2048.f * 512.f));
#pragma unroll
        for (int hf = 0; hf < 2; ++hf) {
          const f32x4 r = acc[hf][tt] * gate[tt] + X[(hf * 2 + tt) * 64] * sc;
          *(u32x2*)(out + ((size_t)srow * 512 + 256 * hf + 64 * cb + 16 * w + 4 * g) * 2) = u32x2{pk2(r[0], r[1]), pk2(r[2], r[3])};
        }
      }
    }
    ST(13);
  }
}

extern "C" void kernel_launch(void* const* d_in, const int* in_sizes, int n_in, void* d_out, int out_size,
                              void* d_ws, size_t ws_size, hipStream_t stream) {
  (void)hipFuncSetAttribute((const void*)fused_kernel, hipFuncAttributeMaxDynamicSharedMemorySize, LDS_BYTES);
  fused_kernel<<<256, 512, LDS_BYTES, stream>>>((const char*)d_in[0], (const char*)d_in[1], (const char*)d_in[2],
                                                (const char*)d_in[4], (const char*)d_in[5], (const char*)d_in[6],
                                                (char*)d_ws, (char*)d_out);
}
```

```cpp
#include <hip/hip_runtime.h>
#include <hip/hip_bf16.h>
#include <stdint.h>

typedef short s16x4 __attribute__((ext_vector_type(4)));
typedef short s16x8 __attribute__((ext_vector_type(8)));
typedef float f32x2 __attribute__((ext_vector_type(2)));
typedef float f32x4 __attribute__((ext_vector_type(4)));
typedef unsigned u32x2 __attribute__((ext_vector_type(2)));
typedef unsigned u32x4 __attribute__((ext_vector_type(4)));
typedef unsigned u32x4u __attribute__((ext_vector_type(4), aligned(2)));
typedef __bf16 bf16x2 __attribute__((ext_vector_type(2)));
typedef unsigned long long u64;
#define DI __device__ __forceinline__
#define LAS __attribute__((address_space(3)))
#define GAS __attribute__((address_space(1)))
#define RLX_AGENT __ATOMIC_RELAXED, __HIP_MEMORY_SCOPE_AGENT
#define CFENCE() asm volatile("" ::: "memory")
#define PIN(v) asm volatile("" : "+v"(v))
#define DRAIN() asm volatile("s_waitcnt vmcnt(0)" ::: "memory")
#define LGKM0() __builtin_amdgcn_s_waitcnt(0xC07F)
#define SB() __builtin_amdgcn_sched_barrier(0)
#define VMW_(n) asm volatile("s_waitcnt vmcnt(" #n ")" ::: "memory")
#define VMW(n) VMW_(n)
#define LP(T, a) ((LAS T*)(size_t)(a))
#define WS_DBG (64u << 20)
#define BAR() do { CFENCE(); SB(); LGKM0(); __builtin_amdgcn_s_barrier(); SB(); CFENCE(); } while (0)
#define ST(i)

DI unsigned pk2(float a, float b) {
  f32x2 v = {a, b};
  return __builtin_bit_cast(unsigned, __builtin_convertvector(v, bf16x2));
}
DI float bflo(unsigned u) { return __uint_as_float(u << 16); }
DI float bfhi(unsigned u) { return __uint_as_float(u & 0xffff0000u); }
DI f32x4 lo4(u32x4 u) { return f32x4{bflo(u.x), bflo(u.y), bflo(u.z), bflo(u.w)}; }
DI f32x4 hi4(u32x4 u) { return f32x4{bfhi(u.x), bfhi(u.y), bfhi(u.z), bfhi(u.w)}; }
DI f32x4 mfma32(u32x4 a, u32x4 b, f32x4 c) {
  return __builtin_amdgcn_mfma_f32_16x16x32_bf16(__builtin_bit_cast(s16x8, a), __builtin_bit_cast(s16x8, b), c, 0, 0, 0);
}
DI f32x4 mfma16(u32x2 a, u32x2 b, f32x4 c) {
  return __builtin_amdgcn_mfma_f32_16x16x16bf16_1k(__builtin_bit_cast(s16x4, a), __builtin_bit_cast(s16x4, b), c, 0, 0, 0);
}
DI u32x4 pack8(f32x4 a, f32x4 b) { return u32x4{pk2(a[0], a[1]), pk2(a[2], a[3]), pk2(b[0], b[1]), pk2(b[2], b[3])}; }
DI u32x4 neg8(u32x4 a) {
  PIN(a);
  a ^= 0x80008000u;
  PIN(a);
  return a;
}
DI f32x4 phi4(f32x4 t) {
  const f32x4 m = t * 1.4426950408889634f, r = t + 1e-6f;
  f32x4 e, q;
#pragma unroll
  for (int i = 0; i < 4; ++i) { e[i] = __builtin_amdgcn_exp2f(fminf(m[i], 0.f)); q[i] = fmaxf(r[i], 1e-6f); }
  return e + q;
}
DI u32x4 trfrag(unsigned a, int off, int hi_off) {
  s16x4 l = __builtin_amdgcn_ds_read_tr16_b64_v4i16((LAS s16x4*)(LP(char, a) + off));
  s16x4 h = __builtin_amdgcn_ds_read_tr16_b64_v4i16((LAS s16x4*)(LP(char, a) + off + hi_off));
  return __builtin_bit_cast(u32x4, __builtin_shufflevector(l, h, 0, 1, 2, 3, 4, 5, 6, 7));
}
DI u32x4 ld16(unsigned a, int off) { return *(const LAS u32x4*)(LP(char, a) + off); }
template <int CTRL> DI float dpp_add(float v) {
  return v + __int_as_float(__builtin_amdgcn_update_dpp(0, __float_as_int(v), CTRL, 0xf, 0xf, false));
}
DI float row_sum16(float v) { return dpp_add<0x128>(dpp_add<0x124>(dpp_add<0x4E>(dpp_add<0xB1>(v)))); }

constexpr int REP = 8;
constexpr int LN_FB = 8, LN_FQ = LN_FB + 256, LN_CZ = LN_FQ + 256, LN_CK = LN_CZ + 16 * REP;
constexpr int WS_XT = 128 * 1024;
constexpr int WS_WT = WS_XT + 256 * 8192;
constexpr int WS_KVP = WS_WT + 257 * 8192;
constexpr int WS_ZP = WS_KVP + 16 * 64 * 1024;
constexpr int WS_KVF = WS_ZP + 16 * 64 * 64;
constexpr int WS_ZF = WS_KVF + 16 * 1024;
constexpr int WS_G = WS_ZF + 2048;
constexpr int WS_PQ = WS_G + 8192;
constexpr int WS_Z = WS_PQ + 2048 * 1024;
constexpr int WS_WOF = WS_Z;
constexpr int WS_END = WS_Z + 256 * 8192;
constexpr int L_SCR = 131072, L_ST = L_SCR + 30720, L_SYNC = L_SCR + 32768 - 512, LDS_BYTES = 163840;
enum { E_QL, E_FB, E_X1, E_X2, E_ZD, E_FZ, E_ZI, E_QR, E_PQ, E_WL, E_WR, E_KA, E_FQ, E_FK, E_O, E_G };
constexpr unsigned SPIN_CAP = 1u << 16;
#define POLL_SLEEP 3
#define WO_THR 4
#define CODE_LINES 7
#define TAG_OF(e) ((e) * 0x9E3779B97F4A7C15ull + 0x632BE59BD9B4E019ull)

#define SIG(ev) do { CFENCE(); *(volatile LAS unsigned*)(LP(char, sigp) + (ev) * 16) = 1u; CFENCE(); } while (0)
template <unsigned MASK> DI void ev_wait(int ev) {
  const volatile LAS u32x4* p = (const volatile LAS u32x4*)(size_t)(L_SYNC + ev * 16);
  for (;;) {
    const u32x4 v = *p;
    const unsigned a = ((MASK & 1) ? v.x : 1u) & ((MASK & 2) ? v.y : 1u) & ((MASK & 4) ? v.z : 1u) & ((MASK & 8) ? v.w : 1u);
    if (__builtin_amdgcn_readfirstlane(a)) break;
    __builtin_amdgcn_s_sleep(1);
  }
  CFENCE();
}
DI void poll_all(const char* ws, unsigned line, u64 want) {
  const GAS u64* p = (const GAS u64*)(ws + (size_t)line * 128);
  for (unsigned spins = 0; spins < SPIN_CAP; ++spins) {
    if (__all(__hip_atomic_load(p, RLX_AGENT) == want)) break;
    __builtin_amdgcn_s_sleep(POLL_SLEEP);
  }
  __builtin_amdgcn_fence(__ATOMIC_ACQUIRE, "wavefront");
  CFENCE();
}
DI void put_flag(char* ws, unsigned line, u64 v) { __hip_atomic_store((GAS u64*)(ws + (size_t)line * 128), v, RLX_AGENT); }

DI void dma4(const unsigned (&voff)[4], const char* sb, unsigned dst) {
  unsigned keep;
  asm volatile("s_mov_b32 %0, m0\n\ts_mov_b32 m0, %6\n\ts_nop 0\n\t"
               "global_load_lds_dwordx4 %1, %5\n\t"
               "global_load_lds_dwordx4 %2, %5 offset:1024\n\t"
               "global_load_lds_dwordx4 %3, %5 offset:2048\n\t"
               "global_load_lds_dwordx4 %4, %5 offset:3072\n\t"
               "s_mov_b32 m0, %0"
               : "=&s"(keep) : "v"(voff[0]), "v"(voff[1]), "v"(voff[2]), "v"(voff[3]), "s"(sb), "s"(dst) : "memory");
}
DI int wt_pos(int u) {
  const int k0 = u & 63, u1 = u >> 6;
  return (((k0 >> 4) * 64 + ((u1 >> 2) & 3) * 16 + (k0 & 15)) << 3) + (u1 >> 4) * 4 + (u1 & 3);
}
DI int wt_row(int pos) {
  const int bt = pos >> 3;
  return 16 * (bt >> 6) + (bt & 15) + 64 * (16 * ((pos >> 2) & 1) + 4 * ((bt >> 4) & 3) + (pos & 3));
}
DI void trig4(float f, float i0, f32x4& co, f32x4& si) {
#pragma unroll
  for (int e = 0; e < 4; ++e) {
    const float a = f * (i0 + (float)e);
    co[e] = __builtin_amdgcn_cosf(a); si[e] = __builtin_amdgcn_sinf(a);
  }
}
DI void trig_frag(float f, float i0, u32x4& co, u32x4& si) {
  f32x4 c0, s0, c1, s1;
  trig4(f, i0, c0, s0); trig4(f, i0 + 16.f, c1, s1);
  co = pack8(c0, c1); si = pack8(s0, s1);
  PIN(co); PIN(si);
}

#define LD128(off) __builtin_amdgcn_raw_buffer_load_b128(rs, (off), 0, 16)
#define LD32(off) __builtin_amdgcn_raw_buffer_load_b32(rs, (off), 0, 16)
#define ST128(v, off) __builtin_amdgcn_raw_buffer_store_b128((v), rs, (off), 0, 16)
#define ST64(v, off) __builtin_amdgcn_raw_buffer_store_b64((v), rs, (off), 0, 16)
#define ST32(v, off) __builtin_amdgcn_raw_buffer_store_b32((v), rs, (off), 0, 16)

__global__ __launch_bounds__(512, 2) void fused_kernel(const char* __restrict__ x, const char* __restrict__ wqkv,
                                                       const char* __restrict__ wo, const char* __restrict__ w_r,
                                                       const char* __restrict__ w_i, const char* __restrict__ gate_w,
                                                       char* __restrict__ ws, char* __restrict__ out) {
  extern __shared__ __attribute__((aligned(16))) char smem[];
  LAS char* sm = (LAS char*)smem;
  asm volatile("" :: "s"(x), "s"(wqkv), "s"(wo), "s"(w_r), "s"(w_i), "s"(gate_w), "s"(ws), "s"(out), "s"(sm));
  u64 pc0;
  asm volatile("s_getpc_b64 %0" : "=s"(pc0));
  pc0 &= ~127ull;
  const int tid = threadIdx.x, lane = tid & 63;
  const int w = __builtin_amdgcn_readfirstlane(tid >> 6);
  const int bid = blockIdx.x, xcd = bid & 7, idx = bid >> 3;
  const int mt = (xcd & 3) * 8 + (idx & 7), ct = (xcd >> 2) * 4 + (idx >> 3);
  const int rb = xcd * 8 + (idx & 7), cb = idx >> 3;
  const int c = lane & 15, g = lane >> 4;
  const __amdgpu_buffer_rsrc_t rs = __builtin_amdgcn_make_buffer_rsrc(ws, 0, WS_END, 0x00020000);
  const f32x4 zero = {0, 0, 0, 0};
  unsigned sigp = L_SYNC + (w & 3) * 4;
  PIN(sigp);
  ST(0);
  const int i8 = idx & 7, c0 = (idx + (idx >> 3)) & 3, flip = (idx >> 2) & 1;
  if (w >= 4) {
    const int wb = w - 4, bt = tid - 256;
    unsigned vx[4], vk[4], vq[4];
    const unsigned dst0 = wb * 4096;
#pragma unroll
    for (int i = 0; i < 4; ++i) {
      const int r = 16 * wb + 4 * i + g;
      vx[i] = (unsigned)((mt + 32 * r) * 1024 + ((c ^ (r & 15)) << 4) + 4096 - i * 1024);
    }
    auto issue_x = [&](int i) { dma4(vx, x - 4096 + ((c0 - i) & 3) * 256, dst0 + i * 16384); };
    issue_x(0);
#pragma unroll
    for (int i = 0; i < 4; ++i) {
      const int r = 16 * wb + 4 * i + g, sl = c ^ ((g | ((i >> 1) << 2)) << 1);
      vk[i] = (unsigned)(r * 3072 + (sl >> 3) * 1024 + ((sl & 7) << 4) + 4096 - i * 1024);
    }
    auto issue_kv = [&](int j) {
      const int blk = 2 * ((c0 - (j >> 1)) & 3) + ((j & 1) ^ flip);
      dma4(vk, wqkv - 4096 + 1024 + ct * 128 + (unsigned)blk * (64 * 3072), dst0 + ((j + 4) & 7) * 16384);
    };
    u32x4 gw[4];
#pragma unroll
    for (int i = 0; i < 4; ++i)
      asm volatile("global_load_dwordx4 %0, %1, %2" : "=v"(gw[i]) : "v"(c * 16), "s"(gate_w + ((c0 - i) & 3) * 256) : "memory");
    issue_kv(0);
    issue_x(1);
    if (wb == 0) *(LAS u32x2*)(sm + L_SYNC + lane * 8) = u32x2{0u, 0u};
#pragma unroll
    for (int i = 0; i < 4; ++i) {
      const int r = 16 * wb + 4 * i + g, sl = c ^ ((g | ((i >> 1) << 2)) << 1);
      vq[i] = (unsigned)(r * 3072 + ((sl >> 3) ^ flip) * (64 * 3072) + ((sl & 7) << 4) + 4096 - i * 1024);
      PIN(vq[i]);
    }
    auto issue_q = [&](int j) { dma4(vq, wqkv - 4096 + ct * 128 + (unsigned)((c0 - j) & 3) * (128 * 3072), dst0 + (j + 4) * 16384); };
    const float g4 = (float)(4 * g);
    u32x4 m0c[2], m0s[2], m0n[2], mac[2], mas[2], mcc[2][2], mcs[2][2];
    f32x4 w1c[2], w1s[2], w2c[2], w2s[2];
#pragma unroll
    for (int i = 0; i < 2; ++i) {
      trig_frag((float)(16 * wb + c) * (1.f / 64.f), g4 + 32.f * i, m0c[i], m0s[i]);
      m0n[i] = neg8(m0s[i]);
    }
    const int gr = 8 * ct + 2 * wb + (g & 1);
    unsigned gxp = gr * 256 + ((c ^ (gr & 15)) << 4);
    PIN(gxp);
    u32x4 gx[4];
    u64 epoch;
    asm volatile("s_load_dwordx2 %0, %1, 0x0\n\ts_waitcnt lgkmcnt(0)" : "=s"(epoch) : "s"(ws) : "memory");
    const u64 tag = TAG_OF(epoch);
    VMW(12); BAR();
    gx[0] = ld16(gxp, 0);
    issue_x(2); issue_x(3);
    asm volatile("s_waitcnt vmcnt(8)" : "+v"(gw[0]), "+v"(gw[1]), "+v"(gw[2]), "+v"(gw[3]) :: "memory");
    BAR();
    gx[1] = ld16(gxp, 16384);
    issue_kv(1); issue_kv(2);
    VMW(12); BAR();
    gx[2] = ld16(gxp, 32768);
    issue_kv(3); issue_kv(4);
    VMW(16); BAR();
    gx[3] = ld16(gxp, 49152);
    issue_kv(5); issue_kv(6);
#pragma unroll
    for (int i = 0; i < 2; ++i) trig_frag((float)(16 * i + c) * (1.f / 32.f), g4, mac[i], mas[i]);
    VMW(16); BAR();
    issue_kv(7); issue_q(0);
    unsigned pfw;
    asm volatile("global_load_dword %0, %1, %2" : "=v"(pfw) : "v"((lane & 31) * 128), "s"(wo + idx * 16384 + wb * 4096) : "memory");
#pragma unroll
    for (int tt = 0; tt < 2; ++tt) {
      trig4((float)(16 * wb + c) * (1.f / 2048.f), g4 + 16.f * tt, w1c[tt], w1s[tt]);
      PIN(w1c[tt]); PIN(w1s[tt]);
    }
    VMW(17); BAR();
    issue_q(1); issue_q(2);
#pragma unroll
    for (int tt = 0; tt < 2; ++tt) {
      trig4((float)(16 * tt + c) * (1.f / 2048.f), g4 + 16.f * wb, w2c[tt], w2s[tt]);
      PIN(w2c[tt]); PIN(w2s[tt]);
    }
    VMW(13); BAR();
    issue_q(3);
    ST(1);
    {
      float s = 0.f;
#pragma unroll
      for (int i = 0; i < 4; ++i)
#pragma unroll
        for (int j = 0; j < 4; ++j) s += bflo(gx[i][j]) * bflo(gw[i][j]) + bfhi(gx[i][j]) * bfhi(gw[i][j]);
      s = row_sum16(s);
      if ((lane & 47) == 0) ST32(__float_as_uint(__builtin_amdgcn_rcpf(1.f + __expf(-s))), WS_G + (64 * mt + gr) * 4);
    }
#pragma unroll
    for (int i = 0; i < 4; ++i)
      trig_frag((float)(32 * (wb >> 1) + 8 * (c >> 2) + 4 * (i >> 1) + (c & 3)) * (1.f / 64.f), g4 + 32.f * (i & 1), mcc[i >> 1][i & 1], mcs[i >> 1][i & 1]);
    asm volatile("s_waitcnt vmcnt(0)" : "+v"(pfw) :: "memory");
    SIG(E_QL);
    const int k = 32 * ct + mt;
    unsigned o_x = (unsigned)(WS_XT + k * 8192 + bt * 16), o_w = (unsigned)(WS_WT + k * 8192 + bt * 32);
    unsigned l_xw = L_SCR + (bt >> 4) * 256 + ((c ^ (bt >> 4)) << 4);
    unsigned l_ca[4];
#pragma unroll
    for (int i = 0; i < 4; ++i) { l_ca[i] = L_SCR + c * 256 + (((4 * i + g) ^ c) << 4); PIN(l_ca[i]); }
    unsigned l_ew = L_SCR + 8192 + (wb >> 1) * 4096 + lane * 16 + (wb & 1) * 8, l_er = L_SCR + 8192 + (wb & 1) * 2048 + lane * 16;
    unsigned o_z = (unsigned)(WS_Z + k * 8192 + (2 * (16 * (wb & 1) + c) + (wb >> 1)) * 128 + g * 16);
    PIN(o_x); PIN(o_w); PIN(l_xw); PIN(l_ew); PIN(l_er); PIN(o_z);
    ST(2);
    if (wb == 0) { poll_all(ws, LN_FB + ct * 32 + (lane & 31), tag); SIG(E_FB); }
    else ev_wait<1>(E_FB);
    __builtin_amdgcn_s_setprio(2);
    ST(3);
    {
      const u32x4 x0 = LD128(o_x), x1 = LD128(o_x + 4096);
      const u32x4 wv[2] = {LD128(o_w), LD128(o_w + 16)};
      u32x4 ar, ai, an;
      u32x4 ca[2][2][2];
      auto twiddle_dft32 = [&](const f32x4 (&yr)[2], const f32x4 (&yi)[2], f32x4 (&xr)[2], f32x4 (&xi)[2]) {
        const f32x4 r0 = yr[0] * w1c[0] + yi[0] * w1s[0], r1 = yr[1] * w1c[1] + yi[1] * w1s[1];
        const u32x4 br = pack8(r0, r1), bn = pack8(-r0, -r1);
        const u32x4 bi = pack8(yi[0] * w1c[0] - yr[0] * w1s[0], yi[1] * w1c[1] - yr[1] * w1s[1]);
#pragma unroll
        for (int ut = 0; ut < 2; ++ut) {
          xr[ut] = mfma32(mas[ut], bi, mfma32(mac[ut], br, zero));
          xi[ut] = mfma32(mas[ut], bn, mfma32(mac[ut], bi, zero));
        }
      };
      if (k != 0) {
        *LP(u32x4, l_xw) = x0; *(LAS u32x4*)(LP(char, l_xw) + 4096) = x1;
        SIG(E_X1);
        f32x4 fr[2] = {lo4(wv[0]), lo4(wv[1])}, fi[2] = {hi4(wv[0]), hi4(wv[1])};
        PIN(fr[0]); PIN(fr[1]); PIN(fi[0]); PIN(fi[1]);
        ST(4);
        ev_wait<15>(E_X1);
#pragma unroll
        for (int i = 0; i < 8; ++i) ca[i >> 2][(i >> 1) & 1][i & 1] = ld16(l_ca[i >> 1], (i & 1) * 4096);
        f32x4 yr[2] = {zero, zero}, yi[2] = {zero, zero}, xr[2], xi[2];
#pragma unroll
        for (int ks = 0; ks < 2; ++ks)
#pragma unroll
          for (int tt = 0; tt < 2; ++tt) { yr[tt] = mfma32(ca[0][ks][tt], m0c[ks], yr[tt]); yi[tt] = mfma32(ca[0][ks][tt], m0n[ks], yi[tt]); }
#pragma unroll
        for (int ks = 0; ks < 2; ++ks)
#pragma unroll
          for (int tt = 0; tt < 2; ++tt) { yr[tt] = mfma32(ca[1][ks][tt], m0s[ks], yr[tt]); yi[tt] = mfma32(ca[1][ks][tt], m0c[ks], yi[tt]); }
        twiddle_dft32(yr, yi, xr, xi);
        const f32x4 i0 = xr[0] * fi[0] + xi[0] * fr[0], i1 = xr[1] * fi[1] + xi[1] * fr[1];
        ar = pack8(xr[0] * fr[0] - xi[0] * fi[0], xr[1] * fr[1] - xi[1] * fi[1]);
        ai = pack8(i0, i1); an = pack8(-i0, -i1);
      } else {
        const u32x4 wn[2] = {LD128(o_w + 256 * 8192), LD128(o_w + 256 * 8192 + 16)};
        unsigned mp[8];
#pragma unroll
        for (int e = 0; e < 8; ++e) { mp[e] = 4 * wt_pos((2048 - (16 * wb + c + 64 * (16 * (e >> 2) + 4 * g + (e & 3)))) & 2047); PIN(mp[e]); }
        *LP(u32x4, l_xw) = x0; *(LAS u32x4*)(LP(char, l_xw) + 4096) = x1;
        LAS u32x4* d = (LAS u32x4*)sm + bt * 2;
        d[0] = wv[0]; d[1] = wv[1]; d[512] = wn[0]; d[513] = wn[1];
        SIG(E_X1);
        ev_wait<15>(E_X1);
#pragma unroll
        for (int i = 0; i < 8; ++i) ca[i >> 2][(i >> 1) & 1][i & 1] = ld16(l_ca[i >> 1], (i & 1) * 4096);
        u32x4 m0[2], m1[2];
#pragma unroll
        for (int e = 0; e < 8; ++e) { m0[e >> 2][e & 3] = *LP(unsigned, mp[e]); m1[e >> 2][e & 3] = *(LAS unsigned*)(LP(char, mp[e]) + 8192); }
        f32x4 yr[2] = {zero, zero}, yi[2] = {zero, zero}, xr[2], xi[2], dr[2], di[2];
#pragma unroll
        for (int ks = 0; ks < 2; ++ks)
#pragma unroll
          for (int tt = 0; tt < 2; ++tt) { yr[tt] = mfma32(ca[0][ks][tt], m0c[ks], yr[tt]); yi[tt] = mfma32(ca[0][ks][tt], m0n[ks], yi[tt]); }
        twiddle_dft32(yr, yi, xr, xi);
#pragma unroll
        for (int ut = 0; ut < 2; ++ut) {
          const f32x4 fr = lo4(wv[ut]) + lo4(m0[ut]), fi = hi4(wv[ut]) - hi4(m0[ut]);
          dr[ut] = xr[ut] * fr - xi[ut] * fi; di[ut] = xr[ut] * fi + xi[ut] * fr;
          yr[ut] = zero; yi[ut] = zero;
        }
#pragma unroll
        for (int ks = 0; ks < 2; ++ks)
#pragma unroll
          for (int tt = 0; tt < 2; ++tt) { yr[tt] = mfma32(ca[1][ks][tt], m0s[ks], yr[tt]); yi[tt] = mfma32(ca[1][ks][tt], m0c[ks], yi[tt]); }
        twiddle_dft32(yr, yi, xr, xi);
#pragma unroll
        for (int ut = 0; ut < 2; ++ut) {
          const f32x4 fr = lo4(wn[ut]) + lo4(m1[ut]), fi = hi4(wn[ut]) - hi4(m1[ut]);
          dr[ut] += xr[ut] * fr - xi[ut] * fi; di[ut] += xr[ut] * fi + xi[ut] * fr;
        }
        ar = pack8(dr[0], dr[1]); ai = pack8(di[0], di[1]); an = pack8(-di[0], -di[1]);
      }
      ST(5);
#pragma unroll
      for (int tt = 0; tt < 2; ++tt) {
        const f32x4 vr = mfma32(an, mas[tt], mfma32(ar, mac[tt], zero));
        const f32x4 vi = mfma32(ar, mas[tt], mfma32(ai, mac[tt], zero));
        const f32x4 pr = vr * w2c[tt] - vi * w2s[tt], pi = vi * w2c[tt] + vr * w2s[tt];
        *(LAS u32x2*)(LP(char, l_ew) + tt * 2048) = u32x2{pk2(pr[0], pr[1]), pk2(pr[2], pr[3])};
        *(LAS u32x2*)(LP(char, l_ew) + tt * 2048 + 1024) = u32x2{pk2(pi[0], pi[1]), pk2(pi[2], pi[3])};
      }
      SIG(E_X2);
      ev_wait<15>(E_X2);
      f32x4 zr[2] = {zero, zero}, zi[2] = {zero, zero}, zs[2] = {zero, zero};
#pragma unroll
      for (int ks = 0; ks < 2; ++ks) {
        const u32x4 vr = ld16(l_er, ks * 4096), vi = ld16(l_er, ks * 4096 + 1024);
#pragma unroll
        for (int jt = 0; jt < 2; ++jt) {
          zr[jt] = mfma32(mcc[jt][ks], vr, zr[jt]); zi[jt] = mfma32(mcs[jt][ks], vr, zi[jt]);
          zs[jt] = mfma32(mcs[jt][ks], vi, zs[jt]); zi[jt] = mfma32(mcc[jt][ks], vi, zi[jt]);
        }
      }
      ST128(pack8(zr[0] - zs[0], zr[1] - zs[1]), o_z);
      ST128(pack8(zi[0], zi[1]), o_z + 64);
    }
    __builtin_amdgcn_s_setprio(0);
    ST(6);
    u32x4 ga[16];
    const float df = (float)(64 * cb + 16 * wb + c) * (1.f / 512.f);
    auto gen = [&](int f) {
#pragma unroll
      for (int i = 0; i < 4; ++i) {
        const float a = (2.f * g4 + (float)(32 * (f & 7) + 2 * i + (f >> 3))) * df;
        ga[f][i] = pk2(__builtin_amdgcn_cosf(a), __builtin_amdgcn_sinf(-a));
      }
      if (f == 0 && g == 0) ga[0][0] = (c & 1) ? 0xBE803E80u : 0x3E803E80u;
      PIN(ga[f]);
    };
#pragma unroll
    for (int f = 0; f < 6; ++f) gen(f);
    SB();
    DRAIN();
    SIG(E_ZD);
    if (wb == 0) {
      ev_wait<15>(E_ZD);
      if (lane < REP) __hip_atomic_fetch_add((GAS u64*)(ws + (size_t)(LN_CZ + lane * 16 + 2 * ct + (mt >> 4)) * 128), 1ull, RLX_AGENT);
    }
    ST(7);
#pragma unroll
    for (int f = 6; f < 16; ++f) gen(f);
    unsigned o_zl = (unsigned)(WS_Z + (64 * wb + (lane >> 3)) * 8192 + rb * 128 + (lane & 7) * 16);
    unsigned l_zw, l_z0, l_z1;
    {
      const int kk = 64 * wb + (lane >> 3), R = (((kk & 1) * 128 + (kk >> 1)) << 1) + ((lane >> 2) & 1);
      l_zw = R * 64 + ((lane & 3) << 4);
      l_z0 = (8 * g + (c >> 2)) * 64 + 8 * (c & 3) + ((g & 1) << 5);
      l_z1 = l_z0 ^ 32u;
    }
    unsigned l_zw1 = l_zw ^ 32u;
    PIN(o_zl); PIN(l_zw); PIN(l_zw1); PIN(l_z0); PIN(l_z1);
    ev_wait<15>(E_WR);
    ST(11);
    if (wb == 0) { poll_all(ws, LN_CZ + (bid % REP) * 16 + c, tag + 16); SIG(E_FZ); }
    else ev_wait<1>(E_FZ);
    ST(8);
    {
      u32x4 z[8];
#pragma unroll
      for (int i = 0; i < 8; ++i) z[i] = __builtin_amdgcn_raw_buffer_load_b128(rs, o_zl, i * 65536, 16);
#pragma unroll
      for (int i = 0; i < 8; ++i) *(LAS u32x4*)(LP(char, (i & 1) ? l_zw1 : l_zw) + i * 512) = z[i];
    }
    SIG(E_ZI);
    ST(9);
    ev_wait<15>(E_ZI);
    f32x4 acc[2][2] = {{zero, zero}, {zero, zero}};
    {
      u32x4 zf[3][2][2];
      auto ldz = [&](int n) {
#pragma unroll
        for (int i = 0; i < 2; ++i) { zf[n % 3][i][0] = trfrag(l_z0, (2 * n + i) * 2048, 256); zf[n % 3][i][1] = trfrag(l_z1, (2 * n + i) * 2048, 256); }
      };
      ldz(0); ldz(1);
      SB();
#pragma unroll
      for (int n = 0; n < 8; ++n) {
        if (n < 6) ldz(n + 2);
#pragma unroll
        for (int i = 0; i < 2; ++i) {
          acc[n >> 2][0] = mfma32(ga[2 * n + i], zf[n % 3][i][0], acc[n >> 2][0]);
          acc[n >> 2][1] = mfma32(ga[2 * n + i], zf[n % 3][i][1], acc[n >> 2][1]);
        }
#pragma unroll
        for (int i = 0; i < 4; ++i) { __builtin_amdgcn_sched_group_barrier(0x100, 2, 0); __builtin_amdgcn_sched_group_barrier(0x8, 1, 0); }
        SB();
      }
    }
    {
      LAS f32x4* X = (LAS f32x4*)(sm + L_SCR) + wb * 256 + lane;
      X[0] = acc[0][0] + acc[1][0]; X[64] = acc[0][1] + acc[1][1];
      X[128] = acc[0][0] - acc[1][0]; X[192] = acc[0][1] - acc[1][1];
    }
    ST(10);
    BAR();
  } else {
    __builtin_amdgcn_s_setprio(1);
    u32x4 wrv, wiv;
    unsigned nyq = 0, nyq2 = 0, pf = 0;
    {
      const unsigned o_w = (unsigned)(wt_row(64 * mt + (tid >> 2)) * 257 + 32 * ct + 8 * (tid & 3)) * 2u;
      wrv = *(const u32x4u*)(w_r + o_w);
      wiv = *(const u32x4u*)(w_i + o_w);
      if (ct == 0 && w == 0) {
        const unsigned o_ny = (unsigned)(wt_row(64 * mt + lane) * 257 + 256) * 2u;
        nyq = *(const unsigned short*)(w_r + o_ny); nyq2 = *(const unsigned short*)(w_i + o_ny);
      }
    }
    if (w == 2 && lane < CODE_LINES) pf = *(const GAS unsigned*)(pc0 + (idx * CODE_LINES + lane) * 128);
    const u32x2 ev = *(const u32x2*)ws;
    unsigned tp = 0;
    {
      constexpr int G = 1 << 18;
      constexpr int NWS = (WS_END + G - 1) / G, NOUT = (2 * 1024 * 1024) / G;
      for (int i = tid; i < NWS + NOUT; i += 256)
        tp ^= i < NWS ? *(const unsigned*)(ws + (size_t)i * G + 64) : *(const unsigned*)(out + (size_t)(i - NWS) * G);
    }
    const int rec = (2 * ct + (w >> 1)) * 64 + 2 * mt + (w & 1);
    unsigned o_kp = (unsigned)(WS_KVP + rec * 1024 + lane * 16), o_zp = (unsigned)(WS_ZP + rec * 64 + g * 16);
    ST128((u32x4{~0u, ~0u, ~0u, ~0u}), o_kp);
    if (c == 0) ST128((u32x4{~0u, ~0u, ~0u, ~0u}), o_zp);
    SB();
    const int rh = w & 1, kt = w >> 1;
    const int kf = 32 * ct + 16 * kt + c;
    u32x4 xf[4][4][2];
    unsigned bb[2][4];
    {
      const unsigned fl = (unsigned)((c >> 2) | ((g & 1) << 2));
      const unsigned bu = (unsigned)((8 * g + (c >> 2)) * 256 + 8 * (c & 3)) + (((unsigned)(2 * kt) ^ fl) << 5);
#pragma unroll
      for (int u = 0; u < 4; ++u) {
        bb[0][u] = bu ^ (unsigned)((u & 1) * 32 + (u >> 1) * 128);
        bb[1][u] = bb[0][u] + 65536u;
      }
    }
    u32x4 fb[3][4];
    auto ldf = [&](int n) {
#pragma unroll
      for (int u = 0; u < 4; ++u) {
        const int slot = (4 + (n >> 1)) & 7;
        fb[n % 3][u] = trfrag(bb[slot >> 2][u], (slot & 3) * 16384 + (n & 1) * 8192, 1024);
      }
    };
    u64 tag;
    {
      u32x4 fc[4], fs[4];
      {
        const float kk = (float)kf * (1.f / 512.f);
        const float a0 = __builtin_amdgcn_fractf(kk * (float)(8 * g + 128 * ((c0 + 1) & 3)));
#pragma unroll
        for (int ks = 0; ks < 4; ++ks) {
          const float ak = __builtin_fmaf(kk, (float)(((ks & 2) ^ (flip << 1)) * 32), a0);
#pragma unroll
          for (int i = 0; i < 4; ++i) {
            const float p0 = __builtin_fmaf(kk, (float)(32 * (ks & 1) + 2 * i), ak), p1 = __builtin_fmaf(kk, (float)(32 * (ks & 1) + 2 * i + 1), ak);
            fc[ks][i] = pk2(__builtin_amdgcn_cosf(p0), __builtin_amdgcn_cosf(p1));
            fs[ks][i] = kf == 0 ? 0xBF803F80u
                                : pk2(__builtin_amdgcn_sinf(-p0), __builtin_amdgcn_sinf(-p1));
          }
          PIN(fc[ks]); PIN(fs[ks]);
        }
      }
      float ra = (c & 1) ? 0.f : ((c & 2) ? -1.f : 1.f), rbi = (c & 1) ? ((c & 2) ? 1.f : -1.f) : 0.f;
      unsigned l_sc = L_SCR + ((tid & 3) * 512 + ((tid >> 2) ^ ((tid & 3) << 3))) * 4;
      unsigned l_rd = L_SCR + (tid >> 4) * 256 + (((tid & 15) ^ ((tid >> 7) << 1)) << 4);
      unsigned o_wt = (unsigned)(WS_WT + (32 * ct + (tid >> 4)) * 8192 + (64 * mt + 4 * (tid & 15)) * 4);
      unsigned o_xt = (unsigned)(WS_XT + kf * 8192 + mt * 256 + rh * 64 + g * 16);
      unsigned xa[4];
#pragma unroll
      for (int ks = 0; ks < 4; ++ks) { xa[ks] = rh * 8192 + c * 256 + (((4 * ks + g) ^ (flip << 3) ^ c) << 4); PIN(xa[ks]); }
#pragma unroll
      for (int u = 0; u < 4; ++u) { PIN(bb[0][u]); PIN(bb[1][u]); }
      PIN(l_sc); PIN(l_rd); PIN(o_wt); PIN(o_xt); PIN(ra); PIN(rbi); PIN(o_kp); PIN(o_zp);
      ST(1);
      f32x4 xr[4][2], xi[4][2];
      auto ldx = [&](int s) {
#pragma unroll
        for (int ks = 0; ks < 4; ++ks)
#pragma unroll
          for (int tt = 0; tt < 2; ++tt) xf[s][ks][tt] = ld16(xa[ks], s * 16384 + tt * 4096);
      };
      auto mmf = [&](int s) {
#pragma unroll
        for (int tt = 0; tt < 2; ++tt) { xr[s][tt] = zero; xi[s][tt] = zero; }
#pragma unroll
        for (int ks = 0; ks < 4; ++ks)
#pragma unroll
          for (int tt = 0; tt < 2; ++tt) {
            xr[s][tt] = mfma32(xf[s][ks][tt], fc[ks], xr[s][tt]);
            xi[s][tt] = mfma32(xf[s][ks][tt], fs[ks], xi[s][tt]);
          }
      };
      auto horner = [&](int s) {
#pragma unroll
        for (int tt = 0; tt < 2; ++tt) {
          xr[s][tt] = (xr[s][tt] + xr[s - 1][tt] * ra) - xi[s - 1][tt] * rbi;
          xi[s][tt] = (xi[s][tt] + xr[s - 1][tt] * rbi) + xi[s - 1][tt] * ra;
        }
      };
      auto mixv = [&]() {
#pragma unroll
        for (int i = 0; i < 16; ++i) { __builtin_amdgcn_sched_group_barrier(0x8, 1, 0); __builtin_amdgcn_sched_group_barrier(0x2 | 0x80 | 0x40, 2, 0); }
        SB();
      };
      BAR();
      ldx(0);
      {
        PIN(pf); PIN(nyq); PIN(nyq2); PIN(tp);
        tag = TAG_OF(((u64)(unsigned)__builtin_amdgcn_readfirstlane(ev.y) << 32) | (unsigned)__builtin_amdgcn_readfirstlane(ev.x));
        LAS unsigned* sc = LP(unsigned, l_sc);
#pragma unroll
        for (int j = 0; j < 4; ++j) {
          sc[(2 * j) * 64] = __builtin_amdgcn_perm(wiv[j], wrv[j], 0x05040100u);
          sc[(2 * j + 1) * 64] = __builtin_amdgcn_perm(wiv[j], wrv[j], 0x07060302u);
        }
        if (ct == 0 && w == 0) ST32(nyq | (nyq2 << 16), WS_WT + 256 * 8192 + (64 * mt + lane) * 4);
        if (mt == 0 && w == 1 && lane < 4 * REP)
          put_flag(ws, LN_CZ + (lane >> 1) * 16 + 2 * ct + (lane & 1), tag);
      }
      BAR();
      const u32x4 wt0 = ld16(l_rd, 0), wt1 = ld16(l_rd ^ 64u, 4096);
      ldx(1); SB(); mmf(0);
      BAR();
      ldx(2); SB(); mmf(1);
      BAR();
      ldx(3); SB();
      mmf(2); horner(1);
      ST128(wt0, o_wt); ST128(wt1, o_wt + 16 * 8192);
      mixv();
      mmf(3); horner(2);
      mixv();
      ldf(0); ldf(1);
      horner(3);
      ST128(pack8(xr[3][0], xr[3][1]), o_xt);
      ST128(pack8(xi[3][0], xi[3][1]), o_xt + 128);
    }
    ST(2);
    auto mix = [&]() {
#pragma unroll
      for (int i = 0; i < 8; ++i) { __builtin_amdgcn_sched_group_barrier(0x100, 1, 0); __builtin_amdgcn_sched_group_barrier(0x8, 1, 0); }
      SB();
    };
    {
      f32x4 acc[2][4];
#pragma unroll
      for (int t = 0; t < 2; ++t)
#pragma unroll
        for (int u = 0; u < 4; ++u) acc[t][u] = zero;
#pragma unroll
      for (int n = 0; n < 16; ++n) {
        if (n == 8) DRAIN();
        if (n == 0 || n == 4 || n == 8) BAR();
        if (n == 8) { if (tid == 0) put_flag(ws, LN_FB + ct * 32 + mt, tag); SB(); }
        if (n < 14) ldf(n + 2);
#pragma unroll
        for (int u = 0; u < 4; ++u)
#pragma unroll
          for (int t = 0; t < 2; ++t) acc[t][u] = mfma32(xf[n >> 2][n & 3][t], fb[n % 3][u], acc[t][u]);
        mix();
      }
      ST(3);
      const u32x4 ones = {0x3f803f80u, 0x3f803f80u, 0x3f803f80u, 0x3f803f80u};
      f32x4 kv[2], zz[2];
#pragma unroll
      for (int n = 0; n < 2; ++n) {
        const u32x4 pk = pack8(phi4(acc[0][n]), phi4(acc[1][n]));
        kv[n] = mfma32(pk, pack8(acc[0][2 + n], acc[1][2 + n]), zero);
        zz[n] = mfma32(pk, ones, zero);
      }
      u32x4 kvo = pack8(kv[0], kv[1]), zo = pack8(zz[0], zz[1]);
#pragma unroll
      for (int e = 0; e < 4; ++e) {
        kvo[e] = min(kvo[e], 0xfffffffeu);
        zo[e] = min(zo[e], 0xfffffffeu);
      }
      ST128(kvo, o_kp);
      if (c == 0) ST128(zo, o_zp);
    }
    ST(4);
    {
      const int hp = 2 * ct + (mt >> 4), sub = mt & 15;
      u32x4 pv[4];
      unsigned pz;
      auto ldp = [&]() {
#pragma unroll
        for (int i = 0; i < 4; ++i)
          pv[i] = __builtin_amdgcn_raw_buffer_load_b128(rs, WS_KVP + (hp * 64 + (lane >> 2)) * 1024 + sub * 64 + (lane & 3) * 16, i * 16384, 16);
        pz = LD32(WS_ZP + (hp * 64 + lane) * 64 + sub * 4);
      };
      f32x4 acc[2][2] = {{zero, zero}, {zero, zero}};
      ev_wait<15>(E_QL);
      SB();
      ldf(0); ldf(1);
      SB();
#pragma unroll
      for (int n = 0; n < 8; ++n) {
        if (n < 6) ldf(n + 2);
        if (n == 6) {
          SIG(E_QR);
          if (w == 0) { ev_wait<1>(E_FB); ldp(); }
          SB();
        }
#pragma unroll
        for (int u = 0; u < 4; ++u)
#pragma unroll
          for (int t = 0; t < 2; ++t) acc[u & 1][t] = mfma32(fb[n % 3][u], xf[n >> 1][2 * (u >> 1) + (n & 1)][t], acc[u & 1][t]);
        mix();
      }
#pragma unroll
      for (int n = 0; n < 2; ++n)
        ST128(pack8(phi4(acc[n][0]), phi4(acc[n][1])), WS_PQ + ((2 * mt + rh) * 32 + 4 * ct + 2 * kt + n) * 1024 + lane * 16);
      SB();
      ST(5);
      if (w == 0) {
        for (unsigned spins = 0; spins < SPIN_CAP; ++spins) {
          unsigned m = pz;
#pragma unroll
          for (int i = 0; i < 4; ++i) m = max(max(m, max(pv[i][0], pv[i][1])), max(pv[i][2], pv[i][3]));
          if (__all(m != ~0u)) break;
          CFENCE();
          ldp();
        }
        ST(6);
        const unsigned one = (c & 1) ? 0x3f800000u : 0x00003f80u;
        const u32x4 sel = {(c >> 1) == 0 ? one : 0u, (c >> 1) == 1 ? one : 0u, (c >> 1) == 2 ? one : 0u, (c >> 1) == 3 ? one : 0u};
        f32x4 s = zero;
#pragma unroll
        for (int i = 0; i < 4; ++i) s = mfma32(sel, pv[i], s);
        const f32x4 zq = mfma32(sel, u32x4{pz, 0u, 0u, 0u}, zero);
#pragma unroll
        for (int e = 0; e < 4; ++e) s[e] = dpp_add<0x128>(dpp_add<0x124>(s[e]));
        const float z0 = row_sum16(zq[0]), z1 = row_sum16(zq[1]);
        if ((lane & 44) == 0) ST64((u32x2{pk2(s[0], s[1]), pk2(s[2], s[3])}), WS_KVF + hp * 1024 + (sub * 4 + c) * 16 + g * 8);
        if (lane == 0) ST32(pk2(z0, z1), WS_ZF + hp * 64 + sub * 4);
        VMW(2);
        ev_wait<14>(E_PQ);
        if (lane == 0) put_flag(ws, LN_FQ + mt * 8 + ct, tag);
        VMW(1);
        if (lane < REP) __hip_atomic_fetch_add((GAS u64*)(ws + (size_t)(LN_CK + lane * 16 + 2 * ct + (mt >> 4)) * 128), 1ull, RLX_AGENT);
        SIG(E_KA);
      } else {
        unsigned voff[4];
#pragma unroll
        for (int i = 0; i < 4; ++i) {
          const int r = 4 * i + g, sl = c ^ ((r & 7) << 1);
          voff[i] = (unsigned)(r * 1024 + (sl >> 3) * 512 + ((sl & 7) << 4) + 4096 - i * 1024);
        }
        ev_wait<15>(E_QR);
#pragma unroll 1
        for (int gj = 3 + w; gj < 36; gj += 3) {
          const int gi = gj & 31;
          if (gj >= 32) ev_wait<15>(E_X2);
          dma4(voff, wo - 4096 + cb * 128 + ((gi + 4 * i8) & 31) * 16384, gi * 4096);
          VMW(WO_THR);
          if (gj < 7) { VMW(4); SIG(E_PQ); }
        }
        DRAIN();
        SIG(E_WL);
      }
    }
    __builtin_amdgcn_s_setprio(0);
    ST(7);
    u32x4 wa[16][2];
    {
      const unsigned f7 = (unsigned)((4 * g + (c >> 2)) & 7);
      unsigned pw[2][2];
#pragma unroll
      for (int hf = 0; hf < 2; ++hf) {
        pw[hf][0] = (4 * g + (c >> 2)) * 256 + 8 * (c & 3) + ((((unsigned)w + 4u * hf) ^ f7) << 5);
        pw[hf][1] = pw[hf][0] + 65536u;
        PIN(pw[hf][0]); PIN(pw[hf][1]);
      }
      ev_wait<14>(E_WL);
#pragma unroll
      for (int st = 0; st < 16; ++st)
#pragma unroll
        for (int hf = 0; hf < 2; ++hf) wa[st][hf] = trfrag(pw[hf][st >> 3], (st & 7) * 8192, 4096);
      SIG(E_WR);
    }
    SB();
    ST(8);
    if (w == 1 || w == 2) ev_wait<1>(E_KA);
    if (w == 1) { poll_all(ws, LN_FQ + (rb >> 1) * 8 + (lane & 7), tag); SIG(E_FQ); }
    else if (w == 2) {
      poll_all(ws, LN_CK + (bid % REP) * 16 + c, tag + 16);
      SIG(E_FK);
      if (bid == 0 && lane == 0) *(u32x2*)ws = u32x2{ev.x + 1u, ev.y};
    }
    ev_wait<2>(E_FQ);
    u32x4 pq[8];
#pragma unroll
    for (int i = 0; i < 8; ++i) pq[i] = LD128(WS_PQ + (rb * 32 + 8 * w + i) * 1024 + lane * 16);
    float gate[2];
#pragma unroll
    for (int tt = 0; tt < 2; ++tt) gate[tt] = __uint_as_float(LD32(WS_G + (32 * rb + 16 * tt + c) * 4));
    unsigned l_o[4];
#pragma unroll
    for (int hp = 0; hp < 4; ++hp) { l_o[hp] = 65536u + ((4 * w + hp - 2 * i8) & 15) * 2048 + lane * 16; PIN(l_o[hp]); }
    unsigned o_kf = (unsigned)(WS_KVF + 4 * w * 1024 + lane * 16), o_zf = (unsigned)(WS_ZF + 4 * w * 64 + g * 16), l_or = 65536u + lane * 16;
    PIN(o_kf); PIN(o_zf); PIN(l_or);
    ev_wait<15>(E_WR);
    SB();
    ST(9);
    ev_wait<4>(E_FK);
    ST(10);
    {
      u32x4 kf4[4], zf4[4];
#pragma unroll
      for (int i = 0; i < 4; ++i) { kf4[i] = LD128(o_kf + i * 1024); zf4[i] = LD128(o_zf + i * 64); }
#pragma unroll
      for (int hp = 0; hp < 4; ++hp)
#pragma unroll
        for (int tt = 0; tt < 2; ++tt) {
          f32x4 o[2];
#pragma unroll
          for (int n = 0; n < 2; ++n) {
            const u32x4 pp = pq[2 * hp + n];
            const u32x2 b = tt ? u32x2{pp.z, pp.w} : u32x2{pp.x, pp.y};
            const u32x2 ka = n ? u32x2{kf4[hp].z, kf4[hp].w} : u32x2{kf4[hp].x, kf4[hp].y};
            const u32x2 za = n ? u32x2{zf4[hp].z, zf4[hp].w} : u32x2{zf4[hp].x, zf4[hp].y};
            const f32x4 num = mfma16(ka, b, zero), den = mfma16(za, b, f32x4{1e-6f, 1e-6f, 1e-6f, 1e-6f});
            o[n] = num * __builtin_amdgcn_rcpf(den[0]);
          }
          *(LAS u32x4*)(LP(char, l_o[hp]) + tt * 1024) = pack8(o[0], o[1]);
        }
    }
    SIG(E_O);
    ST(11);
    ev_wait<15>(E_O);
    f32x4 acc[2][2] = {{zero, zero}, {zero, zero}};
    {
#pragma unroll
      for (int st = 0; st < 16; ++st)
#pragma unroll
        for (int tt = 0; tt < 2; ++tt) {
          const u32x4 ob = ld16(l_or, (st * 2 + tt) * 1024);
          acc[0][tt] = mfma32(wa[st][0], ob, acc[0][tt]);
          acc[1][tt] = mfma32(wa[st][1], ob, acc[1][tt]);
        }
      __builtin_amdgcn_sched_group_barrier(0x100, 8, 0);
#pragma unroll
      for (int i = 0; i < 24; ++i) { __builtin_amdgcn_sched_group_barrier(0x8, 2, 0); __builtin_amdgcn_sched_group_barrier(0x100, 1, 0); }
      __builtin_amdgcn_sched_group_barrier(0x8, 16, 0);
    }
    ST(12);
    BAR();
    {
      const LAS f32x4* X = (const LAS f32x4*)(sm + L_SCR) + w * 256 + lane;
#pragma unroll
      for (int tt = 0; tt < 2; ++tt) {
        const int srow = (rb >> 1) + 32 * (32 * (rb & 1) + 16 * tt + c);
        const float sc = (1.f - gate[tt]) * (2.f / (2048.f * 512.f));
#pragma unroll
        for (int hf = 0; hf < 2; ++hf) {
          const f32x4 r = acc[hf][tt] * gate[tt] + X[(hf * 2 + tt) * 64] * sc;
          *(u32x2*)(out + ((size_t)srow * 512 + 256 * hf + 64 * cb + 16 * w + 4 * g) * 2) = u32x2{pk2(r[0], r[1]), pk2(r[2], r[3])};
        }
      }
    }
    ST(13);
  }
}

extern "C" void kernel_launch(void* const* d_in, const int* in_sizes, int n_in, void* d_out, int out_size,
                              void* d_ws, size_t ws_size, hipStream_t stream) {
  (void)hipFuncSetAttribute((const void*)fused_kernel, hipFuncAttributeMaxDynamicSharedMemorySize, LDS_BYTES);
  fused_kernel<<<256, 512, LDS_BYTES, stream>>>((const char*)d_in[0], (const char*)d_in[1], (const char*)d_in[2],
                                                (const char*)d_in[4], (const char*)d_in[5], (const char*)d_in[6],
                                                (char*)d_ws, (char*)d_out);
}
```

```cpp
#define OPT_TAB
#define OPT_BP
#define OPT_S0
#define CODE_LINES 8
#define CODE_MAX 30464
#define KS 8320
#define RS 1152
#include <hip/hip_runtime.h>
#include <hip/hip_bf16.h>
#include <stdint.h>

typedef short s16x4 __attribute__((ext_vector_type(4)));
typedef short s16x8 __attribute__((ext_vector_type(8)));
typedef float f32x2 __attribute__((ext_vector_type(2)));
typedef float f32x4 __attribute__((ext_vector_type(4)));
typedef unsigned u32x2 __attribute__((ext_vector_type(2)));
typedef unsigned u32x4 __attribute__((ext_vector_type(4)));
typedef unsigned u32x4u __attribute__((ext_vector_type(4), aligned(2)));
typedef __bf16 bf16x2 __attribute__((ext_vector_type(2)));
typedef unsigned long long u64;
#define DI __device__ __forceinline__
#define LAS __attribute__((address_space(3)))
#define GAS __attribute__((address_space(1)))
#define RLX_AGENT __ATOMIC_RELAXED, __HIP_MEMORY_SCOPE_AGENT
#define CFENCE() asm volatile("" ::: "memory")
#define PIN(v) asm volatile("" : "+v"(v))
#define DRAIN() asm volatile("s_waitcnt vmcnt(0)" ::: "memory")
#define LGKM0() __builtin_amdgcn_s_waitcnt(0xC07F)
#define SB() __builtin_amdgcn_sched_barrier(0)
#define VMW_(n) asm volatile("s_waitcnt vmcnt(" #n ")" ::: "memory")
#define VMW(n) VMW_(n)
#define LP(T, a) ((LAS T*)(size_t)(a))
#define WS_DBG (64u << 20)
#define BAR() do { CFENCE(); SB(); LGKM0(); __builtin_amdgcn_s_barrier(); SB(); CFENCE(); } while (0)
#define ST(i)

DI unsigned pk2(float a, float b) {
  f32x2 v = {a, b};
  return __builtin_bit_cast(unsigned, __builtin_convertvector(v, bf16x2));
}
DI float bflo(unsigned u) { return __uint_as_float(u << 16); }
DI float bfhi(unsigned u) { return __uint_as_float(u & 0xffff0000u); }
DI f32x4 lo4(u32x4 u) { return f32x4{bflo(u.x), bflo(u.y), bflo(u.z), bflo(u.w)}; }
DI f32x4 hi4(u32x4 u) { return f32x4{bfhi(u.x), bfhi(u.y), bfhi(u.z), bfhi(u.w)}; }
DI f32x4 mfma32(u32x4 a, u32x4 b, f32x4 c) {
  return __builtin_amdgcn_mfma_f32_16x16x32_bf16(__builtin_bit_cast(s16x8, a), __builtin_bit_cast(s16x8, b), c, 0, 0, 0);
}
DI f32x4 mfma16(u32x2 a, u32x2 b, f32x4 c) {
  return __builtin_amdgcn_mfma_f32_16x16x16bf16_1k(__builtin_bit_cast(s16x4, a), __builtin_bit_cast(s16x4, b), c, 0, 0, 0);
}
DI u32x4 pack8(f32x4 a, f32x4 b) { return u32x4{pk2(a[0], a[1]), pk2(a[2], a[3]), pk2(b[0], b[1]), pk2(b[2], b[3])}; }
DI u32x4 neg8(u32x4 a) {
  PIN(a);
  a ^= 0x80008000u;
  PIN(a);
  return a;
}
DI f32x4 phi4(f32x4 t) {
  const f32x4 m = t * 1.4426950408889634f, r = t + 1e-6f;
  f32x4 e, q;
#pragma unroll
  for (int i = 0; i < 4; ++i) { e[i] = __builtin_amdgcn_exp2f(fminf(m[i], 0.f)); q[i] = fmaxf(r[i], 1e-6f); }
  return e + q;
}
DI u32x4 trfrag(unsigned a, int off, int hi_off) {
  s16x4 l = __builtin_amdgcn_ds_read_tr16_b64_v4i16((LAS s16x4*)(LP(char, a) + off));
  s16x4 h = __builtin_amdgcn_ds_read_tr16_b64_v4i16((LAS s16x4*)(LP(char, a) + off + hi_off));
  return __builtin_bit_cast(u32x4, __builtin_shufflevector(l, h, 0, 1, 2, 3, 4, 5, 6, 7));
}
DI u32x4 ld16(unsigned a, int off) { return *(const LAS u32x4*)(LP(char, a) + off); }
template <int CTRL> DI float dpp_add(float v) {
  return v + __int_as_float(__builtin_amdgcn_update_dpp(0, __float_as_int(v), CTRL, 0xf, 0xf, false));
}
DI float row_sum16(float v) { return dpp_add<0x128>(dpp_add<0x124>(dpp_add<0x4E>(dpp_add<0xB1>(v)))); }

constexpr int REP = 8;
constexpr int LN_FB = 8, LN_FQ = LN_FB + 256, LN_CZ = LN_FQ + 256, LN_CK = LN_CZ + 16 * REP;
constexpr int WS_XT = 128 * 1024;
constexpr int WS_WT = WS_XT + 256 * KS;
constexpr int WS_KVP = WS_WT + 257 * KS;
constexpr int WS_ZP = WS_KVP + 16 * 64 * RS;
constexpr int WS_KVF = WS_ZP + 16 * 64 * 64;
constexpr int WS_ZF = WS_KVF + 16 * 1024;
constexpr int WS_G = WS_ZF + 2048;
constexpr int WS_PQ = WS_G + 8192;
constexpr int WS_Z = WS_PQ + 2048 * 1024;
constexpr int WS_WOF = WS_Z;
constexpr int WS_END = WS_Z + 256 * KS;
constexpr int L_SCR = 131072, L_ST = L_SCR + 30720, L_SYNC = L_SCR + 32768 - 512, LDS_BYTES = 163840;
enum { E_QL, E_FB, E_X1, E_X2, E_ZD, E_FZ, E_ZI, E_QR, E_PQ, E_WL, E_WR, E_KA, E_FQ, E_FK, E_O, E_G };
constexpr unsigned SPIN_CAP = 1u << 16;
#define POLL_SLEEP 3
#define WO_THR 4
#define FBN 8
#define TAG_OF(e) ((e) * 0x9E3779B97F4A7C15ull + 0x632BE59BD9B4E019ull)

#define SIG(ev) do { CFENCE(); *(volatile LAS unsigned*)(LP(char, sigp) + (ev) * 16) = 1u; CFENCE(); } while (0)
template <unsigned MASK> DI void ev_wait(int ev) {
  const volatile LAS u32x4* p = (const volatile LAS u32x4*)(size_t)(L_SYNC + ev * 16);
  for (;;) {
    const u32x4 v = *p;
    const unsigned a = ((MASK & 1) ? v.x : 1u) & ((MASK & 2) ? v.y : 1u) & ((MASK & 4) ? v.z : 1u) & ((MASK & 8) ? v.w : 1u);
    if (__builtin_amdgcn_readfirstlane(a)) break;
    __builtin_amdgcn_s_sleep(1);
  }
  CFENCE();
}
DI void poll_all(const char* ws, unsigned line, u64 want) {
  const GAS u64* p = (const GAS u64*)(ws + (size_t)line * 128);
  for (unsigned spins = 0; spins < SPIN_CAP; ++spins) {
    if (__all(__hip_atomic_load(p, RLX_AGENT) == want)) break;
    __builtin_amdgcn_s_sleep(POLL_SLEEP);
  }
  __builtin_amdgcn_fence(__ATOMIC_ACQUIRE, "wavefront");
  CFENCE();
}
DI void put_flag(char* ws, unsigned line, u64 v) { __hip_atomic_store((GAS u64*)(ws + (size_t)line * 128), v, RLX_AGENT); }

DI void dma4(const unsigned (&voff)[4], const char* sb, unsigned dst) {
  unsigned keep;
  asm volatile("s_mov_b32 %0, m0\n\ts_mov_b32 m0, %6\n\ts_nop 0\n\t"
               "global_load_lds_dwordx4 %1, %5\n\t"
               "global_load_lds_dwordx4 %2, %5 offset:1024\n\t"
               "global_load_lds_dwordx4 %3, %5 offset:2048\n\t"
               "global_load_lds_dwordx4 %4, %5 offset:3072\n\t"
               "s_mov_b32 m0, %0"
               : "=&s"(keep) : "v"(voff[0]), "v"(voff[1]), "v"(voff[2]), "v"(voff[3]), "s"(sb), "s"(dst) : "memory");
}
DI int wt_pos(int u) {
  const int k0 = u & 63, u1 = u >> 6;
  return (((k0 >> 4) * 64 + ((u1 >> 2) & 3) * 16 + (k0 & 15)) << 3) + (u1 >> 4) * 4 + (u1 & 3);
}
DI int wt_row(int pos) {
  const int bt = pos >> 3;
  return 16 * (bt >> 6) + (bt & 15) + 64 * (16 * ((pos >> 2) & 1) + 4 * ((bt >> 4) & 3) + (pos & 3));
}
DI void trig4(float f, float i0, f32x4& co, f32x4& si) {
#pragma unroll
  for (int e = 0; e < 4; ++e) {
    const float a = f * (i0 + (float)e);
    co[e] = __builtin_amdgcn_cosf(a); si[e] = __builtin_amdgcn_sinf(a);
  }
}
DI void trig_frag(float f, float i0, u32x4& co, u32x4& si) {
  f32x4 c0, s0, c1, s1;
  trig4(f, i0, c0, s0); trig4(f, i0 + 16.f, c1, s1);
  co = pack8(c0, c1); si = pack8(s0, s1);
  PIN(co); PIN(si);
}

#define LD128(off) __builtin_amdgcn_raw_buffer_load_b128(rs, (off), 0, 16)
#define LD32(off) __builtin_amdgcn_raw_buffer_load_b32(rs, (off), 0, 16)
#define ST128(v, off) __builtin_amdgcn_raw_buffer_store_b128((v), rs, (off), 0, 16)
#define ST64(v, off) __builtin_amdgcn_raw_buffer_store_b64((v), rs, (off), 0, 16)
#define ST32(v, off) __builtin_amdgcn_raw_buffer_store_b32((v), rs, (off), 0, 16)

__global__ __launch_bounds__(512, 2) void fused_kernel(const char* __restrict__ x, const char* __restrict__ wqkv,
                                                       const char* __restrict__ wo, const char* __restrict__ w_r,
                                                       const char* __restrict__ w_i, const char* __restrict__ gate_w,
                                                       char* __restrict__ ws, char* __restrict__ out) {
  extern __shared__ __attribute__((aligned(16))) char smem[];
  LAS char* sm = (LAS char*)smem;
  asm volatile("" :: "s"(x), "s"(wqkv), "s"(wo), "s"(w_r), "s"(w_i), "s"(gate_w), "s"(ws), "s"(out), "s"(sm));
  u64 pc0;
  asm volatile("s_getpc_b64 %0" : "=s"(pc0));
  pc0 &= ~127ull;
  const int tid = threadIdx.x, lane = tid & 63;
  const int w = __builtin_amdgcn_readfirstlane(tid >> 6);
  const int bid = blockIdx.x, xcd = bid & 7, idx = bid >> 3;
  const int mt = (xcd & 3) * 8 + (idx & 7), ct = (xcd >> 2) * 4 + (idx >> 3);
  const int rb = xcd * 8 + (idx & 7), cb = idx >> 3;
  const int c = lane & 15, g = lane >> 4;
  const __amdgpu_buffer_rsrc_t rs = __builtin_amdgcn_make_buffer_rsrc(ws, 0, WS_END, 0x00020000);
  const f32x4 zero = {0, 0, 0, 0};
  unsigned sigp = L_SYNC + (w & 3) * 4;
  PIN(sigp);
  ST(0);
  const int i8 = idx & 7, c0 = (idx + (idx >> 3)) & 3, flip = (idx >> 2) & 1;
  if (w >= 4) {
    const int wb = w - 4, bt = tid - 256;
    unsigned vx[4], vk[4], vq[4];
    const unsigned dst0 = wb * 4096;
#pragma unroll
    for (int i = 0; i < 4; ++i) {
      const int r = 16 * wb + 4 * i + g;
      vx[i] = (unsigned)((mt + 32 * r) * 1024 + ((c ^ (r & 15)) << 4) + 4096 - i * 1024);
    }
    auto issue_x = [&](int i) { dma4(vx, x - 4096 + ((c0 - i) & 3) * 256, dst0 + i * 16384); };
    issue_x(0);
#pragma unroll
    for (int i = 0; i < 4; ++i) {
      const int r = 16 * wb + 4 * i + g, sl = c ^ ((g | ((i >> 1) << 2)) << 1);
      vk[i] = (unsigned)(r * 3072 + (sl >> 3) * 1024 + ((sl & 7) << 4) + 4096 - i * 1024);
    }
    auto issue_kv = [&](int j) {
      const int blk = 2 * ((c0 - (j >> 1)) & 3) + ((j & 1) ^ flip);
      dma4(vk, wqkv - 4096 + 1024 + ct * 128 + (unsigned)blk * (64 * 3072), dst0 + ((j + 4) & 7) * 16384);
    };
    u32x4 gw[4];
#pragma unroll
    for (int i = 0; i < 4; ++i)
      asm volatile("global_load_dwordx4 %0, %1, %2" : "=v"(gw[i]) : "v"(c * 16), "s"(gate_w + ((c0 - i) & 3) * 256) : "memory");
    issue_kv(0);
    issue_x(1);
    if (wb == 0) *(LAS u32x2*)(sm + L_SYNC + lane * 8) = u32x2{0u, 0u};
#pragma unroll
    for (int i = 0; i < 4; ++i) {
      const int r = 16 * wb + 4 * i + g, sl = c ^ ((g | ((i >> 1) << 2)) << 1);
      vq[i] = (unsigned)(r * 3072 + ((sl >> 3) ^ flip) * (64 * 3072) + ((sl & 7) << 4) + 4096 - i * 1024);
      PIN(vq[i]);
    }
    auto issue_q = [&](int j) { dma4(vq, wqkv - 4096 + ct * 128 + (unsigned)((c0 - j) & 3) * (128 * 3072), dst0 + (j + 4) * 16384); };
    const float g4 = (float)(4 * g);
    u32x4 m0c[2], m0s[2], m0n[2], mac[2], mas[2], mcc[2][2], mcs[2][2];
    f32x4 w1c[2], w1s[2], w2c[2], w2s[2];
    auto tab = [&](int row) { return (unsigned)(7 * 16384 + row * 256 + ((g ^ (row & 15)) << 4)); };
#pragma unroll
    for (int i = 0; i < 2; ++i) {
      trig_frag((float)(16 * wb + c) * (1.f / 64.f), g4 + 32.f * i, m0c[i], m0s[i]);
      m0n[i] = neg8(m0s[i]);
      *LP(u32x4, tab(16 * wb + c) ^ (64u * i)) = m0c[i];
      *LP(u32x4, tab(16 * wb + c) ^ (64u * i + 128u)) = m0s[i];
    }
    const int gr = 8 * ct + 2 * wb + (g & 1);
    unsigned gxp = gr * 256 + ((c ^ (gr & 15)) << 4);
    PIN(gxp);
    u32x4 gx[4];
    u64 epoch;
    asm volatile("s_load_dwordx2 %0, %1, 0x0\n\ts_waitcnt lgkmcnt(0)" : "=s"(epoch) : "s"(ws) : "memory");
    const u64 tag = TAG_OF(epoch);
    VMW(12); BAR();
    __builtin_amdgcn_s_setprio(3);
    gx[0] = ld16(gxp, 0);
    issue_x(2); issue_x(3);
#pragma unroll
    for (int i = 0; i < 2; ++i) {
      const unsigned ta = tab(2 * (16 * i + c)), tc = tab(32 * (wb >> 1) + 8 * (c >> 2) + 4 * i + (c & 3));
      mac[i] = *LP(u32x4, ta); mas[i] = *LP(u32x4, ta ^ 128u);
      mcc[i][0] = *LP(u32x4, tc); mcc[i][1] = *LP(u32x4, tc ^ 64u); mcs[i][0] = *LP(u32x4, tc ^ 128u); mcs[i][1] = *LP(u32x4, tc ^ 192u);
    }
    asm volatile("s_waitcnt vmcnt(8)" : "+v"(gw[0]), "+v"(gw[1]), "+v"(gw[2]), "+v"(gw[3]) :: "memory");
    BAR();
    gx[1] = ld16(gxp, 16384);
    issue_kv(1); issue_kv(2);
    VMW(12); BAR();
    gx[2] = ld16(gxp, 32768);
    issue_kv(3); issue_kv(4);
    VMW(16); BAR();
    gx[3] = ld16(gxp, 49152);
    issue_kv(5); issue_kv(6);
    VMW(16); BAR();
    issue_kv(7); issue_q(0);
    unsigned pfw;
    asm volatile("global_load_dword %0, %1, %2" : "=v"(pfw) : "v"((lane & 31) * 128), "s"(wo + idx * 16384 + wb * 4096) : "memory");
#pragma unroll
    for (int tt = 0; tt < 2; ++tt) {
      trig4((float)(16 * wb + c) * (1.f / 2048.f), g4 + 16.f * tt, w1c[tt], w1s[tt]);
      PIN(w1c[tt]); PIN(w1s[tt]);
    }
    VMW(17); BAR();
    issue_q(1); issue_q(2);
#pragma unroll
    for (int tt = 0; tt < 2; ++tt) {
      trig4((float)(16 * tt + c) * (1.f / 2048.f), g4 + 16.f * wb, w2c[tt], w2s[tt]);
      PIN(w2c[tt]); PIN(w2s[tt]);
    }
    VMW(13); BAR();
    issue_q(3);
    ST(1);
    {
      float s = 0.f;
#pragma unroll
      for (int i = 0; i < 4; ++i)
#pragma unroll
        for (int j = 0; j < 4; ++j) s += bflo(gx[i][j]) * bflo(gw[i][j]) + bfhi(gx[i][j]) * bfhi(gw[i][j]);
      s = row_sum16(s);
      if ((lane & 47) == 0) ST32(__float_as_uint(__builtin_amdgcn_rcpf(1.f + __expf(-s))), WS_G + (64 * mt + gr) * 4);
    }
    asm volatile("s_waitcnt vmcnt(0)" : "+v"(pfw) :: "memory");
    SIG(E_QL);
    const int k = 32 * ct + mt;
    unsigned o_x = (unsigned)(WS_XT + k * KS + bt * 16), o_w = (unsigned)(WS_WT + k * KS + bt * 32);
    unsigned l_xw = L_SCR + (bt >> 4) * 256 + ((c ^ (bt >> 4)) << 4);
    unsigned l_ca[4];
#pragma unroll
    for (int i = 0; i < 4; ++i) { l_ca[i] = L_SCR + c * 256 + (((4 * i + g) ^ c) << 4); PIN(l_ca[i]); }
    unsigned l_ew = L_SCR + 8192 + (wb >> 1) * 4096 + lane * 16 + (wb & 1) * 8, l_er = L_SCR + 8192 + (wb & 1) * 2048 + lane * 16;
    unsigned o_z = (unsigned)(WS_Z + k * KS + (2 * (16 * (wb & 1) + c) + (wb >> 1)) * 128 + g * 16);
    PIN(o_x); PIN(o_w); PIN(l_xw); PIN(l_ew); PIN(l_er); PIN(o_z);
    ST(2);
    if (wb == 0) { poll_all(ws, LN_FB + ct * 32 + (lane & 31), tag); SIG(E_FB); }
    else ev_wait<1>(E_FB);
    __builtin_amdgcn_s_setprio(2);
    ST(3);
    {
      const u32x4 x0 = LD128(o_x), x1 = LD128(o_x + 4096);
      const u32x4 wv[2] = {LD128(o_w), LD128(o_w + 16)};
      u32x4 ar, ai, an;
      u32x4 ca[2][2][2];
      auto twiddle_dft32 = [&](const f32x4 (&yr)[2], const f32x4 (&yi)[2], f32x4 (&xr)[2], f32x4 (&xi)[2]) {
        const f32x4 r0 = yr[0] * w1c[0] + yi[0] * w1s[0], r1 = yr[1] * w1c[1] + yi[1] * w1s[1];
        const u32x4 br = pack8(r0, r1), bn = pack8(-r0, -r1);
        const u32x4 bi = pack8(yi[0] * w1c[0] - yr[0] * w1s[0], yi[1] * w1c[1] - yr[1] * w1s[1]);
#pragma unroll
        for (int ut = 0; ut < 2; ++ut) {
          xr[ut] = mfma32(mas[ut], bi, mfma32(mac[ut], br, zero));
          xi[ut] = mfma32(mas[ut], bn, mfma32(mac[ut], bi, zero));
        }
      };
      if (k != 0) {
        *LP(u32x4, l_xw) = x0; *(LAS u32x4*)(LP(char, l_xw) + 4096) = x1;
        SIG(E_X1);
        f32x4 fr[2] = {lo4(wv[0]), lo4(wv[1])}, fi[2] = {hi4(wv[0]), hi4(wv[1])};
        PIN(fr[0]); PIN(fr[1]); PIN(fi[0]); PIN(fi[1]);
        ST(4);
        ev_wait<15>(E_X1);
#pragma unroll
        for (int i = 0; i < 8; ++i) ca[i >> 2][(i >> 1) & 1][i & 1] = ld16(l_ca[i >> 1], (i & 1) * 4096);
        f32x4 yr[2] = {zero, zero}, yi[2] = {zero, zero}, xr[2], xi[2];
#pragma unroll
        for (int ks = 0; ks < 2; ++ks)
#pragma unroll
          for (int tt = 0; tt < 2; ++tt) { yr[tt] = mfma32(ca[0][ks][tt], m0c[ks], yr[tt]); yi[tt] = mfma32(ca[0][ks][tt], m0n[ks], yi[tt]); }
#pragma unroll
        for (int ks = 0; ks < 2; ++ks)
#pragma unroll
          for (int tt = 0; tt < 2; ++tt) { yr[tt] = mfma32(ca[1][ks][tt], m0s[ks], yr[tt]); yi[tt] = mfma32(ca[1][ks][tt], m0c[ks], yi[tt]); }
        twiddle_dft32(yr, yi, xr, xi);
        const f32x4 i0 = xr[0] * fi[0] + xi[0] * fr[0], i1 = xr[1] * fi[1] + xi[1] * fr[1];
        ar = pack8(xr[0] * fr[0] - xi[0] * fi[0], xr[1] * fr[1] - xi[1] * fi[1]);
        ai = pack8(i0, i1); an = pack8(-i0, -i1);
      } else {
        const u32x4 wn[2] = {LD128(o_w + 256 * KS), LD128(o_w + 256 * KS + 16)};
        unsigned mp[8];
#pragma unroll
        for (int e = 0; e < 8; ++e) { mp[e] = 4 * wt_pos((2048 - (16 * wb + c + 64 * (16 * (e >> 2) + 4 * g + (e & 3)))) & 2047); PIN(mp[e]); }
        *LP(u32x4, l_xw) = x0; *(LAS u32x4*)(LP(char, l_xw) + 4096) = x1;
        LAS u32x4* d = (LAS u32x4*)sm + bt * 2;
        d[0] = wv[0]; d[1] = wv[1]; d[512] = wn[0]; d[513] = wn[1];
        SIG(E_X1);
        ev_wait<15>(E_X1);
#pragma unroll
        for (int i = 0; i < 8; ++i) ca[i >> 2][(i >> 1) & 1][i & 1] = ld16(l_ca[i >> 1], (i & 1) * 4096);
        u32x4 m0[2], m1[2];
#pragma unroll
        for (int e = 0; e < 8; ++e) { m0[e >> 2][e & 3] = *LP(unsigned, mp[e]); m1[e >> 2][e & 3] = *(LAS unsigned*)(LP(char, mp[e]) + 8192); }
        f32x4 yr[2] = {zero, zero}, yi[2] = {zero, zero}, xr[2], xi[2], dr[2], di[2];
#pragma unroll
        for (int ks = 0; ks < 2; ++ks)
#pragma unroll
          for (int tt = 0; tt < 2; ++tt) { yr[tt] = mfma32(ca[0][ks][tt], m0c[ks], yr[tt]); yi[tt] = mfma32(ca[0][ks][tt], m0n[ks], yi[tt]); }
        twiddle_dft32(yr, yi, xr, xi);
#pragma unroll
        for (int ut = 0; ut < 2; ++ut) {
          const f32x4 fr = lo4(wv[ut]) + lo4(m0[ut]), fi = hi4(wv[ut]) - hi4(m0[ut]);
          dr[ut] = xr[ut] * fr - xi[ut] * fi; di[ut] = xr[ut] * fi + xi[ut] * fr;
          yr[ut] = zero; yi[ut] = zero;
        }
#pragma unroll
        for (int ks = 0; ks < 2; ++ks)
#pragma unroll
          for (int tt = 0; tt < 2; ++tt) { yr[tt] = mfma32(ca[1][ks][tt], m0s[ks], yr[tt]); yi[tt] = mfma32(ca[1][ks][tt], m0c[ks], yi[tt]); }
        twiddle_dft32(yr, yi, xr, xi);
#pragma unroll
        for (int ut = 0; ut < 2; ++ut) {
          const f32x4 fr = lo4(wn[ut]) + lo4(m1[ut]), fi = hi4(wn[ut]) - hi4(m1[ut]);
          dr[ut] += xr[ut] * fr - xi[ut] * fi; di[ut] += xr[ut] * fi + xi[ut] * fr;
        }
        ar = pack8(dr[0], dr[1]); ai = pack8(di[0], di[1]); an = pack8(-di[0], -di[1]);
      }
      ST(5);
#pragma unroll
      for (int tt = 0; tt < 2; ++tt) {
        const f32x4 vr = mfma32(an, mas[tt], mfma32(ar, mac[tt], zero));
        const f32x4 vi = mfma32(ar, mas[tt], mfma32(ai, mac[tt], zero));
        const f32x4 pr = vr * w2c[tt] - vi * w2s[tt], pi = vi * w2c[tt] + vr * w2s[tt];
        *(LAS u32x2*)(LP(char, l_ew) + tt * 2048) = u32x2{pk2(pr[0], pr[1]), pk2(pr[2], pr[3])};
        *(LAS u32x2*)(LP(char, l_ew) + tt * 2048 + 1024) = u32x2{pk2(pi[0], pi[1]), pk2(pi[2], pi[3])};
      }
      SIG(E_X2);
      ev_wait<15>(E_X2);
      f32x4 zr[2] = {zero, zero}, zi[2] = {zero, zero}, zs[2] = {zero, zero};
#pragma unroll
      for (int ks = 0; ks < 2; ++ks) {
        const u32x4 vr = ld16(l_er, ks * 4096), vi = ld16(l_er, ks * 4096 + 1024);
#pragma unroll
        for (int jt = 0; jt < 2; ++jt) {
          zr[jt] = mfma32(mcc[jt][ks], vr, zr[jt]); zi[jt] = mfma32(mcs[jt][ks], vr, zi[jt]);
          zs[jt] = mfma32(mcs[jt][ks], vi, zs[jt]); zi[jt] = mfma32(mcc[jt][ks], vi, zi[jt]);
        }
      }
      ST128(pack8(zr[0] - zs[0], zr[1] - zs[1]), o_z);
      ST128(pack8(zi[0], zi[1]), o_z + 64);
    }
    __builtin_amdgcn_s_setprio(0);
    ST(6);
    u32x4 ga[16];
    const float df = (float)(64 * cb + 16 * wb + c) * (1.f / 512.f);
    auto gen = [&](int f) {
#pragma unroll
      for (int i = 0; i < 4; ++i) {
        const float a = (2.f * g4 + (float)(32 * (f & 7) + 2 * i + (f >> 3))) * df;
        ga[f][i] = pk2(__builtin_amdgcn_cosf(a), __builtin_amdgcn_sinf(-a));
      }
      if (f == 0 && g == 0) ga[0][0] = (c & 1) ? 0xBE803E80u : 0x3E803E80u;
      PIN(ga[f]);
    };
#pragma unroll
    for (int f = 0; f < 6; ++f) gen(f);
    SB();
    DRAIN();
    SIG(E_ZD);
    if (wb == 0) {
      ev_wait<15>(E_ZD);
      if (lane < REP) __hip_atomic_fetch_add((GAS u64*)(ws + (size_t)(LN_CZ + lane * 16 + 2 * ct + (mt >> 4)) * 128), 1ull, RLX_AGENT);
    }
    ST(7);
#pragma unroll
    for (int f = 6; f < 16; ++f) gen(f);
    unsigned o_zl = (unsigned)(WS_Z + (64 * wb + (lane >> 3)) * KS + rb * 128 + (lane & 7) * 16);
    unsigned l_zw, l_z0, l_z1;
    {
      const int kk = 64 * wb + (lane >> 3), R = (((kk & 1) * 128 + (kk >> 1)) << 1) + ((lane >> 2) & 1);
      l_zw = R * 64 + ((lane & 3) << 4);
      l_z0 = (8 * g + (c >> 2)) * 64 + 8 * (c & 3) + ((g & 1) << 5);
      l_z1 = l_z0 ^ 32u;
    }
    unsigned l_zw1 = l_zw ^ 32u;
    PIN(o_zl); PIN(l_zw); PIN(l_zw1); PIN(l_z0); PIN(l_z1);
    ev_wait<15>(E_WR);
    ST(11);
    if (wb == 0) { poll_all(ws, LN_CZ + (bid % REP) * 16 + c, tag + 16); SIG(E_FZ); }
    else ev_wait<1>(E_FZ);
    ST(8);
    {
      u32x4 z[8];
#pragma unroll
      for (int i = 0; i < 8; ++i) z[i] = __builtin_amdgcn_raw_buffer_load_b128(rs, o_zl, i * 8 * KS, 16);
#pragma unroll
      for (int i = 0; i < 8; ++i) *(LAS u32x4*)(LP(char, (i & 1) ? l_zw1 : l_zw) + i * 512) = z[i];
    }
    SIG(E_ZI);
    ST(9);
    ev_wait<15>(E_ZI);
    f32x4 acc[2][2] = {{zero, zero}, {zero, zero}};
    {
      u32x4 zf[3][2][2];
      auto ldz = [&](int n) {
#pragma unroll
        for (int i = 0; i < 2; ++i) { zf[n % 3][i][0] = trfrag(l_z0, (2 * n + i) * 2048, 256); zf[n % 3][i][1] = trfrag(l_z1, (2 * n + i) * 2048, 256); }
      };
      ldz(0); ldz(1);
      SB();
#pragma unroll
      for (int n = 0; n < 8; ++n) {
        if (n < 6) ldz(n + 2);
#pragma unroll
        for (int i = 0; i < 2; ++i) {
          acc[n >> 2][0] = mfma32(ga[2 * n + i], zf[n % 3][i][0], acc[n >> 2][0]);
          acc[n >> 2][1] = mfma32(ga[2 * n + i], zf[n % 3][i][1], acc[n >> 2][1]);
        }
#pragma unroll
        for (int i = 0; i < 4; ++i) { __builtin_amdgcn_sched_group_barrier(0x100, 2, 0); __builtin_amdgcn_sched_group_barrier(0x8, 1, 0); }
        SB();
      }
    }
    {
      LAS f32x4* X = (LAS f32x4*)(sm + L_SCR) + wb * 256 + lane;
      X[0] = acc[0][0] + acc[1][0]; X[64] = acc[0][1] + acc[1][1];
      X[128] = acc[0][0] - acc[1][0]; X[192] = acc[0][1] - acc[1][1];
    }
    ST(10);
    BAR();
  } else {
    __builtin_amdgcn_s_setprio(1);
    u32x4 wrv, wiv;
    unsigned nyq = 0, nyq2 = 0, pf = 0;
    {
      const unsigned o_w = (unsigned)(wt_row(64 * mt + (tid >> 2)) * 257 + 32 * ct + 8 * (tid & 3)) * 2u;
      wrv = *(const u32x4u*)(w_r + o_w);
      wiv = *(const u32x4u*)(w_i + o_w);
      if (ct == 0 && w == 0) {
        const unsigned o_ny = (unsigned)(wt_row(64 * mt + lane) * 257 + 256) * 2u;
        nyq = *(const unsigned short*)(w_r + o_ny); nyq2 = *(const unsigned short*)(w_i + o_ny);
      }
    }
    if (w == 2 && lane < CODE_LINES) pf = *(const GAS unsigned*)(pc0 + min((idx * CODE_LINES + lane) * 128, CODE_MAX));
    const u32x2 ev = *(const u32x2*)ws;
    const int rec = (2 * ct + (w >> 1)) * 64 + 2 * mt + (w & 1);
    unsigned o_kp = (unsigned)(WS_KVP + rec * RS + lane * 16), o_zp = (unsigned)(WS_ZP + rec * 64 + g * 16);
    ST128((u32x4{~0u, ~0u, ~0u, ~0u}), o_kp);
    if (c == 0) ST128((u32x4{~0u, ~0u, ~0u, ~0u}), o_zp);
    SB();
    const int rh = w & 1, kt = w >> 1;
    const int kf = 32 * ct + 16 * kt + c;
    u32x4 xf[4][4][2];
    unsigned bb[2][4];
    {
      const unsigned fl = (unsigned)((c >> 2) | ((g & 1) << 2));
      const unsigned bu = (unsigned)((8 * g + (c >> 2)) * 256 + 8 * (c & 3)) + (((unsigned)(2 * kt) ^ fl) << 5);
#pragma unroll
      for (int u = 0; u < 4; ++u) {
        bb[0][u] = bu ^ (unsigned)((u & 1) * 32 + (u >> 1) * 128);
        bb[1][u] = bb[0][u] + 65536u;
      }
    }
    u32x4 fb[3][4];
    auto ldf = [&](int n) {
#pragma unroll
      for (int u = 0; u < 4; ++u) {
        const int slot = (4 + (n >> 1)) & 7;
        fb[n % 3][u] = trfrag(bb[slot >> 2][u], (slot & 3) * 16384 + (n & 1) * 8192, 1024);
      }
    };
    u64 tag;
    {
      u32x4 fc[4], fs[4];
      {
        const float kk = (float)kf * (1.f / 512.f);
        const float a0 = __builtin_amdgcn_fractf(kk * (float)(8 * g + 128 * ((c0 + 1) & 3)));
#pragma unroll
        for (int ks = 0; ks < 4; ++ks) {
          const float ak = __builtin_fmaf(kk, (float)(((ks & 2) ^ (flip << 1)) * 32), a0);
#pragma unroll
          for (int i = 0; i < 4; ++i) {
            const float p0 = __builtin_fmaf(kk, (float)(32 * (ks & 1) + 2 * i), ak), p1 = __builtin_fmaf(kk, (float)(32 * (ks & 1) + 2 * i + 1), ak);
            fc[ks][i] = pk2(__builtin_amdgcn_cosf(p0), __builtin_amdgcn_cosf(p1));
            fs[ks][i] = kf == 0 ? 0xBF803F80u
                                : pk2(__builtin_amdgcn_sinf(-p0), __builtin_amdgcn_sinf(-p1));
          }
          PIN(fc[ks]); PIN(fs[ks]);
        }
      }
      float ra = (c & 1) ? 0.f : ((c & 2) ? -1.f : 1.f), rbi = (c & 1) ? ((c & 2) ? 1.f : -1.f) : 0.f;
      unsigned l_sc = L_SCR + ((tid & 3) * 512 + ((tid >> 2) ^ ((tid & 3) << 3))) * 4;
      unsigned l_rd = L_SCR + (tid >> 4) * 256 + (((tid & 15) ^ ((tid >> 7) << 1)) << 4);
      unsigned o_wt = (unsigned)(WS_WT + (32 * ct + (tid >> 4)) * KS + (64 * mt + 4 * (tid & 15)) * 4);
      unsigned o_xt = (unsigned)(WS_XT + kf * KS + mt * 256 + rh * 64 + g * 16);
      unsigned xa[4];
#pragma unroll
      for (int ks = 0; ks < 4; ++ks) { xa[ks] = rh * 8192 + c * 256 + (((4 * ks + g) ^ (flip << 3) ^ c) << 4); PIN(xa[ks]); }
#pragma unroll
      for (int u = 0; u < 4; ++u) { PIN(bb[0][u]); PIN(bb[1][u]); }
      PIN(l_sc); PIN(l_rd); PIN(o_wt); PIN(o_xt); PIN(ra); PIN(rbi); PIN(o_kp); PIN(o_zp);
      ST(1);
      f32x4 xr[4][2], xi[4][2];
      auto ldx = [&](int s) {
#pragma unroll
        for (int ks = 0; ks < 4; ++ks)
#pragma unroll
          for (int tt = 0; tt < 2; ++tt) xf[s][ks][tt] = ld16(xa[ks], s * 16384 + tt * 4096);
      };
      auto mmf = [&](int s) {
#pragma unroll
        for (int tt = 0; tt < 2; ++tt) { xr[s][tt] = zero; xi[s][tt] = zero; }
#pragma unroll
        for (int ks = 0; ks < 4; ++ks)
#pragma unroll
          for (int tt = 0; tt < 2; ++tt) {
            xr[s][tt] = mfma32(xf[s][ks][tt], fc[ks], xr[s][tt]);
            xi[s][tt] = mfma32(xf[s][ks][tt], fs[ks], xi[s][tt]);
          }
      };
      auto horner = [&](int s) {
#pragma unroll
        for (int tt = 0; tt < 2; ++tt) {
          xr[s][tt] = (xr[s][tt] + xr[s - 1][tt] * ra) - xi[s - 1][tt] * rbi;
          xi[s][tt] = (xi[s][tt] + xr[s - 1][tt] * rbi) + xi[s - 1][tt] * ra;
        }
      };
      auto mixv = [&]() {
#pragma unroll
        for (int i = 0; i < 16; ++i) { __builtin_amdgcn_sched_group_barrier(0x8, 1, 0); __builtin_amdgcn_sched_group_barrier(0x2 | 0x80 | 0x40, 2, 0); }
        SB();
      };
      BAR();
      ldx(0);
      {
        PIN(pf); PIN(nyq); PIN(nyq2);
        tag = TAG_OF(((u64)(unsigned)__builtin_amdgcn_readfirstlane(ev.y) << 32) | (unsigned)__builtin_amdgcn_readfirstlane(ev.x));
        LAS unsigned* sc = LP(unsigned, l_sc);
#pragma unroll
        for (int j = 0; j < 4; ++j) {
          sc[(2 * j) * 64] = __builtin_amdgcn_perm(wiv[j], wrv[j], 0x05040100u);
          sc[(2 * j + 1) * 64] = __builtin_amdgcn_perm(wiv[j], wrv[j], 0x07060302u);
        }
        if (ct == 0 && w == 0) ST32(nyq | (nyq2 << 16), WS_WT + 256 * KS + (64 * mt + lane) * 4);
        if (mt == 0 && w == 1 && lane < 4 * REP)
          put_flag(ws, LN_CZ + (lane >> 1) * 16 + 2 * ct + (lane & 1), tag);
      }
      BAR();
      const u32x4 wt0 = ld16(l_rd, 0), wt1 = ld16(l_rd ^ 64u, 4096);
      ldx(1); SB(); mmf(0);
      BAR();
      ldx(2); SB(); mmf(1);
      BAR();
      ldx(3); SB();
      mmf(2); horner(1);
      ST128(wt0, o_wt); ST128(wt1, o_wt + 16 * KS);
      mixv();
      mmf(3); horner(2);
      mixv();
      ldf(0); ldf(1);
      horner(3);
      ST128(pack8(xr[3][0], xr[3][1]), o_xt);
      ST128(pack8(xi[3][0], xi[3][1]), o_xt + 128);
    }
    ST(2);
    auto mix = [&]() {
#pragma unroll
      for (int i = 0; i < 8; ++i) { __builtin_amdgcn_sched_group_barrier(0x100, 1, 0); __builtin_amdgcn_sched_group_barrier(0x8, 1, 0); }
      SB();
    };
    {
      f32x4 acc[2][4];
#pragma unroll
      for (int t = 0; t < 2; ++t)
#pragma unroll
        for (int u = 0; u < 4; ++u) acc[t][u] = zero;
#pragma unroll
      for (int n = 0; n < 16; ++n) {
        if (n == FBN) DRAIN();
        if (n == 0 || n == 4 || n == 8) BAR();
        if (n == FBN) { if (tid == 0) put_flag(ws, LN_FB + ct * 32 + mt, tag); SB(); }
        if (n < 14) ldf(n + 2);
#pragma unroll
        for (int u = 0; u < 4; ++u)
#pragma unroll
          for (int t = 0; t < 2; ++t) acc[t][u] = mfma32(xf[n >> 2][n & 3][t], fb[n % 3][u], acc[t][u]);
        mix();
      }
      ST(3);
      const u32x4 ones = {0x3f803f80u, 0x3f803f80u, 0x3f803f80u, 0x3f803f80u};
      f32x4 kv[2], zz[2];
#pragma unroll
      for (int n = 0; n < 2; ++n) {
        const u32x4 pk = pack8(phi4(acc[0][n]), phi4(acc[1][n]));
        kv[n] = mfma32(pk, pack8(acc[0][2 + n], acc[1][2 + n]), zero);
        zz[n] = mfma32(pk, ones, zero);
      }
      u32x4 kvo = pack8(kv[0], kv[1]), zo = pack8(zz[0], zz[1]);
#pragma unroll
      for (int e = 0; e < 4; ++e) {
        kvo[e] = min(kvo[e], 0xfffffffeu);
        zo[e] = min(zo[e], 0xfffffffeu);
      }
      ST128(kvo, o_kp);
      if (c == 0) ST128(zo, o_zp);
    }
    ST(4);
    {
      const int hp = 2 * ct + (mt >> 4), sub = mt & 15;
      u32x4 pv[4];
      unsigned pz;
      auto ldp = [&]() {
#pragma unroll
        for (int i = 0; i < 4; ++i)
          pv[i] = __builtin_amdgcn_raw_buffer_load_b128(rs, WS_KVP + (hp * 64 + (lane >> 2)) * RS + sub * 64 + (lane & 3) * 16, i * 16 * RS, 16);
        pz = LD32(WS_ZP + (hp * 64 + lane) * 64 + sub * 4);
      };
      f32x4 acc[2][2] = {{zero, zero}, {zero, zero}};
      ev_wait<15>(E_QL);
      SB();
      ldf(0); ldf(1);
      SB();
#pragma unroll
      for (int n = 0; n < 8; ++n) {
        if (n < 6) ldf(n + 2);
        if (n == 6) {
          SIG(E_QR);
          if (w == 0) { ev_wait<1>(E_FB); ldp(); }
          SB();
        }
#pragma unroll
        for (int u = 0; u < 4; ++u)
#pragma unroll
          for (int t = 0; t < 2; ++t) acc[u & 1][t] = mfma32(fb[n % 3][u], xf[n >> 1][2 * (u >> 1) + (n & 1)][t], acc[u & 1][t]);
        mix();
      }
#pragma unroll
      for (int n = 0; n < 2; ++n)
        ST128(pack8(phi4(acc[n][0]), phi4(acc[n][1])), WS_PQ + ((2 * mt + rh) * 32 + 4 * ct + 2 * kt + n) * 1024 + lane * 16);
      SB();
      ST(5);
      if (w == 0) {
        for (unsigned spins = 0; spins < SPIN_CAP; ++spins) {
          unsigned m = pz;
#pragma unroll
          for (int i = 0; i < 4; ++i) m = max(max(m, max(pv[i][0], pv[i][1])), max(pv[i][2], pv[i][3]));
          if (__all(m != ~0u)) break;
          CFENCE();
          ldp();
        }
        ST(6);
        const unsigned one = (c & 1) ? 0x3f800000u : 0x00003f80u;
        const u32x4 sel = {(c >> 1) == 0 ? one : 0u, (c >> 1) == 1 ? one : 0u, (c >> 1) == 2 ? one : 0u, (c >> 1) == 3 ? one : 0u};
        f32x4 s = zero;
#pragma unroll
        for (int i = 0; i < 4; ++i) s = mfma32(sel, pv[i], s);
        const f32x4 zq = mfma32(sel, u32x4{pz, 0u, 0u, 0u}, zero);
#pragma unroll
        for (int e = 0; e < 4; ++e) s[e] = dpp_add<0x128>(dpp_add<0x124>(s[e]));
        const float z0 = row_sum16(zq[0]), z1 = row_sum16(zq[1]);
        if ((lane & 44) == 0) ST64((u32x2{pk2(s[0], s[1]), pk2(s[2], s[3])}), WS_KVF + hp * 1024 + (sub * 4 + c) * 16 + g * 8);
        if (lane == 0) ST32(pk2(z0, z1), WS_ZF + hp * 64 + sub * 4);
        VMW(2);
        ev_wait<14>(E_PQ);
        if (lane == 0) put_flag(ws, LN_FQ + mt * 8 + ct, tag);
        VMW(1);
        if (lane < REP) __hip_atomic_fetch_add((GAS u64*)(ws + (size_t)(LN_CK + lane * 16 + 2 * ct + (mt >> 4)) * 128), 1ull, RLX_AGENT);
        SIG(E_KA);
      } else {
        unsigned voff[4];
#pragma unroll
        for (int i = 0; i < 4; ++i) {
          const int r = 4 * i + g, sl = c ^ ((r & 7) << 1);
          voff[i] = (unsigned)(r * 1024 + (sl >> 3) * 512 + ((sl & 7) << 4) + 4096 - i * 1024);
        }
        ev_wait<15>(E_QR);
#pragma unroll 1
        for (int gj = 3 + w; gj < 36; gj += 3) {
          const int gi = gj & 31;
          if (gj >= 32 && (ct | mt) == 0) ev_wait<15>(E_X2);
          dma4(voff, wo - 4096 + cb * 128 + ((gi + 4 * i8) & 31) * 16384, gi * 4096);
          VMW(WO_THR);
          if (gj < 7) { VMW(4); SIG(E_PQ); }
        }
        DRAIN();
        SIG(E_WL);
      }
    }
    __builtin_amdgcn_s_setprio(0);
    ST(7);
    u32x4 wa[16][2];
    {
      const unsigned f7 = (unsigned)((4 * g + (c >> 2)) & 7);
      unsigned pw[2][2];
#pragma unroll
      for (int hf = 0; hf < 2; ++hf) {
        pw[hf][0] = (4 * g + (c >> 2)) * 256 + 8 * (c & 3) + ((((unsigned)w + 4u * hf) ^ f7) << 5);
        pw[hf][1] = pw[hf][0] + 65536u;
        PIN(pw[hf][0]); PIN(pw[hf][1]);
      }
      ev_wait<14>(E_WL);
#pragma unroll
      for (int st = 0; st < 16; ++st)
#pragma unroll
        for (int hf = 0; hf < 2; ++hf) wa[st][hf] = trfrag(pw[hf][st >> 3], (st & 7) * 8192, 4096);
      SIG(E_WR);
    }
    SB();
    ST(8);
    if (w == 1 || w == 2) ev_wait<1>(E_KA);
    if (w == 1) { poll_all(ws, LN_FQ + (rb >> 1) * 8 + (lane & 7), tag); SIG(E_FQ); }
    else if (w == 2) {
      poll_all(ws, LN_CK + (bid % REP) * 16 + c, tag + 16);
      SIG(E_FK);
      if (bid == 0 && lane == 0) *(u32x2*)ws = u32x2{ev.x + 1u, ev.y};
    }
    ev_wait<2>(E_FQ);
    u32x4 pq[8];
#pragma unroll
    for (int i = 0; i < 8; ++i) pq[i] = LD128(WS_PQ + (rb * 32 + 8 * w + i) * 1024 + lane * 16);
    float gate[2];
#pragma unroll
    for (int tt = 0; tt < 2; ++tt) gate[tt] = __uint_as_float(LD32(WS_G + (32 * rb + 16 * tt + c) * 4));
    unsigned l_o[4];
#pragma unroll
    for (int hp = 0; hp < 4; ++hp) { l_o[hp] = 65536u + ((4 * w + hp - 2 * i8) & 15) * 2048 + lane * 16; PIN(l_o[hp]); }
    unsigned o_kf = (unsigned)(WS_KVF + 4 * w * 1024 + lane * 16), o_zf = (unsigned)(WS_ZF + 4 * w * 64 + g * 16), l_or = 65536u + lane * 16;
    PIN(o_kf); PIN(o_zf); PIN(l_or);
    ev_wait<15>(E_WR);
    SB();
    ST(9);
    ev_wait<4>(E_FK);
    ST(10);
    {
      u32x4 kf4[4], zf4[4];
#pragma unroll
      for (int i = 0; i < 4; ++i) { kf4[i] = LD128(o_kf + i * 1024); zf4[i] = LD128(o_zf + i * 64); }
#pragma unroll
      for (int hp = 0; hp < 4; ++hp)
#pragma unroll
        for (int tt = 0; tt < 2; ++tt) {
          f32x4 o[2];
#pragma unroll
          for (int n = 0; n < 2; ++n) {
            const u32x4 pp = pq[2 * hp + n];
            const u32x2 b = tt ? u32x2{pp.z, pp.w} : u32x2{pp.x, pp.y};
            const u32x2 ka = n ? u32x2{kf4[hp].z, kf4[hp].w} : u32x2{kf4[hp].x, kf4[hp].y};
            const u32x2 za = n ? u32x2{zf4[hp].z, zf4[hp].w} : u32x2{zf4[hp].x, zf4[hp].y};
            const f32x4 num = mfma16(ka, b, zero), den = mfma16(za, b, f32x4{1e-6f, 1e-6f, 1e-6f, 1e-6f});
            o[n] = num * __builtin_amdgcn_rcpf(den[0]);
          }
          *(LAS u32x4*)(LP(char, l_o[hp]) + tt * 1024) = pack8(o[0], o[1]);
        }
    }
    SIG(E_O);
    ST(11);
    ev_wait<15>(E_O);
    f32x4 acc[2][2] = {{zero, zero}, {zero, zero}};
    {
#pragma unroll
      for (int st = 0; st < 16; ++st)
#pragma unroll
        for (int tt = 0; tt < 2; ++tt) {
          const u32x4 ob = ld16(l_or, (st * 2 + tt) * 1024);
          acc[0][tt] = mfma32(wa[st][0], ob, acc[0][tt]);
          acc[1][tt] = mfma32(wa[st][1], ob, acc[1][tt]);
        }
      __builtin_amdgcn_sched_group_barrier(0x100, 8, 0);
#pragma unroll
      for (int i = 0; i < 24; ++i) { __builtin_amdgcn_sched_group_barrier(0x8, 2, 0); __builtin_amdgcn_sched_group_barrier(0x100, 1, 0); }
      __builtin_amdgcn_sched_group_barrier(0x8, 16, 0);
    }
    ST(12);
    BAR();
    {
      const LAS f32x4* X = (const LAS f32x4*)(sm + L_SCR) + w * 256 + lane;
#pragma unroll
      for (int tt = 0; tt < 2; ++tt) {
        const int srow = (rb >> 1) + 32 * (32 * (rb & 1) + 16 * tt + c);
        const float sc = (1.f - gate[tt]) * (2.f / (2048.f * 512.f));
#pragma unroll
        for (int hf = 0; hf < 2; ++hf) {
          const f32x4 r = acc[hf][tt] * gate[tt] + X[(hf * 2 + tt) * 64] * sc;
          *(u32x2*)(out + ((size_t)srow * 512 + 256 * hf + 64 * cb + 16 * w + 4 * g) * 2) = u32x2{pk2(r[0], r[1]), pk2(r[2], r[3])};
        }
      }
    }
    ST(13);
  }
}

extern "C" void kernel_launch(void* const* d_in, const int* in_sizes, int n_in, void* d_out, int out_size,
                              void* d_ws, size_t ws_size, hipStream_t stream) {
  (void)hipFuncSetAttribute((const void*)fused_kernel, hipFuncAttributeMaxDynamicSharedMemorySize, LDS_BYTES);
  fused_kernel<<<256, 512, LDS_BYTES, stream>>>((const char*)d_in[0], (const char*)d_in[1], (const char*)d_in[2],
                                                (const char*)d_in[4], (const char*)d_in[5], (const char*)d_in[6],
                                                (char*)d_ws, (char*)d_out);
}
```

```cpp
#define OPT_GATE
#define OPT_BP
#define APRIO 1
#define OPT_FIN
#define CODE_LINES 8
#define CODE_MAX 29440
#define OPT_TW0
#define OPT_POLL
#define WO_THR 6
#include <hip/hip_runtime.h>
#include <hip/hip_bf16.h>
#include <stdint.h>

typedef short s16x4 __attribute__((ext_vector_type(4)));
typedef short s16x8 __attribute__((ext_vector_type(8)));
typedef float f32x2 __attribute__((ext_vector_type(2)));
typedef float f32x4 __attribute__((ext_vector_type(4)));
typedef unsigned u32x2 __attribute__((ext_vector_type(2)));
typedef unsigned u32x4 __attribute__((ext_vector_type(4)));
typedef unsigned u32x4u __attribute__((ext_vector_type(4), aligned(2)));
typedef __bf16 bf16x2 __attribute__((ext_vector_type(2)));
typedef unsigned long long u64;
#define DI __device__ __forceinline__
#define LAS __attribute__((address_space(3)))
#define GAS __attribute__((address_space(1)))
#define RLX_AGENT __ATOMIC_RELAXED, __HIP_MEMORY_SCOPE_AGENT
#define CFENCE() asm volatile("" ::: "memory")
#define PIN(v) asm volatile("" : "+v"(v))
#define DRAIN() asm volatile("s_waitcnt vmcnt(0)" ::: "memory")
#define LGKM0() __builtin_amdgcn_s_waitcnt(0xC07F)
#define SB() __builtin_amdgcn_sched_barrier(0)
#define VMW_(n) asm volatile("s_waitcnt vmcnt(" #n ")" ::: "memory")
#define VMW(n) VMW_(n)
#define LP(T, a) ((LAS T*)(size_t)(a))
#define WS_DBG (64u << 20)
#define BAR() do { CFENCE(); SB(); LGKM0(); __builtin_amdgcn_s_barrier(); SB(); CFENCE(); } while (0)
#define ST(i)

DI unsigned pk2(float a, float b) {
  f32x2 v = {a, b};
  return __builtin_bit_cast(unsigned, __builtin_convertvector(v, bf16x2));
}
DI float bflo(unsigned u) { return __uint_as_float(u << 16); }
DI float bfhi(unsigned u) { return __uint_as_float(u & 0xffff0000u); }
DI f32x4 lo4(u32x4 u) { return f32x4{bflo(u.x), bflo(u.y), bflo(u.z), bflo(u.w)}; }
DI f32x4 hi4(u32x4 u) { return f32x4{bfhi(u.x), bfhi(u.y), bfhi(u.z), bfhi(u.w)}; }
DI f32x4 mfma32(u32x4 a, u32x4 b, f32x4 c) {
  return __builtin_amdgcn_mfma_f32_16x16x32_bf16(__builtin_bit_cast(s16x8, a), __builtin_bit_cast(s16x8, b), c, 0, 0, 0);
}
DI f32x4 mfma16(u32x2 a, u32x2 b, f32x4 c) {
  return __builtin_amdgcn_mfma_f32_16x16x16bf16_1k(__builtin_bit_cast(s16x4, a), __builtin_bit_cast(s16x4, b), c, 0, 0, 0);
}
DI u32x4 pack8(f32x4 a, f32x4 b) { return u32x4{pk2(a[0], a[1]), pk2(a[2], a[3]), pk2(b[0], b[1]), pk2(b[2], b[3])}; }
DI u32x4 neg8(u32x4 a) {
  PIN(a);
  a ^= 0x80008000u;
  PIN(a);
  return a;
}
DI f32x4 phi4(f32x4 t) {
  const f32x4 m = t * 1.4426950408889634f, r = t + 1e-6f;
  f32x4 e, q;
#pragma unroll
  for (int i = 0; i < 4; ++i) { e[i] = __builtin_amdgcn_exp2f(fminf(m[i], 0.f)); q[i] = fmaxf(r[i], 1e-6f); }
  return e + q;
}
DI void crot(f32x4& cr, f32x4& ci, float a, float b) {
  const f32x4 nr = cr * a - ci * b, ni = cr * b + ci * a;
  cr = nr; ci = ni;
}
DI u32x4 trfrag(unsigned a, int off, int hi_off) {
  s16x4 l = __builtin_amdgcn_ds_read_tr16_b64_v4i16((LAS s16x4*)(LP(char, a) + off));
  s16x4 h = __builtin_amdgcn_ds_read_tr16_b64_v4i16((LAS s16x4*)(LP(char, a) + off + hi_off));
  return __builtin_bit_cast(u32x4, __builtin_shufflevector(l, h, 0, 1, 2, 3, 4, 5, 6, 7));
}
DI u32x4 ld16(unsigned a, int off) { return *(const LAS u32x4*)(LP(char, a) + off); }
template <int CTRL> DI float dpp_add(float v) {
  return v + __int_as_float(__builtin_amdgcn_update_dpp(0, __float_as_int(v), CTRL, 0xf, 0xf, false));
}
DI float row_sum16(float v) { return dpp_add<0x128>(dpp_add<0x124>(dpp_add<0x4E>(dpp_add<0xB1>(v)))); }

#define KS 8192
#define RS 1024
#define REPV 8
constexpr int REP = REPV;
constexpr int LN_FB = 8, LN_FQ = LN_FB + 256, LN_CZ = LN_FQ + 256, LN_CK = LN_CZ + 16 * REP;
constexpr int WS_XT = 160 * 1024;
constexpr int WS_WT = WS_XT + 256 * KS;
constexpr int WS_KVP = WS_WT + 257 * KS;
constexpr int WS_ZP = WS_KVP + 16 * 64 * RS;
constexpr int WS_KVF = WS_ZP + 16 * 64 * 64;
constexpr int WS_ZF = WS_KVF + 16 * 1024;
constexpr int WS_G = WS_ZF + 2048;
constexpr int WS_PQ = WS_G + 8192;
constexpr int WS_Z = WS_PQ + 2048 * 1024;
constexpr int WS_WOF = WS_Z;
constexpr int WS_END = WS_Z + 256 * KS;
constexpr int L_SCR = 131072, L_ST = L_SCR + 30720, L_SYNC = L_SCR + 32768 - 512, LDS_BYTES = 163840;
enum { E_QL, E_FB, E_X1, E_X2, E_ZD, E_FZ, E_ZI, E_QR, E_PQ, E_WL, E_WR, E_KA, E_FQ, E_FK, E_O, E_G, E_A };
constexpr unsigned SPIN_CAP = 1u << 16;
#define POLL_SLEEP 3
#define FBN 8
#define BPRIO 0
#define TAG_OF(e) ((e) * 0x9E3779B97F4A7C15ull + 0x632BE59BD9B4E019ull)

#define SIG(ev) do { CFENCE(); *(volatile LAS unsigned*)(LP(char, sigp) + (ev) * 16) = 1u; CFENCE(); } while (0)
template <unsigned MASK> DI void ev_wait(int ev) {
  const volatile LAS u32x4* p = (const volatile LAS u32x4*)(size_t)(L_SYNC + ev * 16);
  for (;;) {
    const u32x4 v = *p;
    const unsigned a = ((MASK & 1) ? v.x : 1u) & ((MASK & 2) ? v.y : 1u) & ((MASK & 4) ? v.z : 1u) & ((MASK & 8) ? v.w : 1u);
    if (__builtin_amdgcn_readfirstlane(a)) break;
    __builtin_amdgcn_s_sleep(1);
  }
  CFENCE();
}
DI void poll_all(const char* ws, unsigned line, u64 want) {
  const GAS u64* p = (const GAS u64*)(ws + (size_t)line * 128);
  for (unsigned spins = 0; spins < SPIN_CAP; ++spins) {
    if (__all(__hip_atomic_load(p, RLX_AGENT) == want)) break;
    __builtin_amdgcn_s_sleep(POLL_SLEEP);
  }
  __builtin_amdgcn_fence(__ATOMIC_ACQUIRE, "wavefront");
  CFENCE();
}
DI void put_flag(char* ws, unsigned line, u64 v) { __hip_atomic_store((GAS u64*)(ws + (size_t)line * 128), v, RLX_AGENT); }

DI void dma4(const unsigned (&voff)[4], const char* sb, unsigned dst) {
  unsigned keep;
  asm volatile("s_mov_b32 %0, m0\n\ts_mov_b32 m0, %6\n\ts_nop 0\n\t"
               "global_load_lds_dwordx4 %1, %5\n\t"
               "global_load_lds_dwordx4 %2, %5 offset:1024\n\t"
               "global_load_lds_dwordx4 %3, %5 offset:2048\n\t"
               "global_load_lds_dwordx4 %4, %5 offset:3072\n\t"
               "s_mov_b32 m0, %0"
               : "=&s"(keep) : "v"(voff[0]), "v"(voff[1]), "v"(voff[2]), "v"(voff[3]), "s"(sb), "s"(dst) : "memory");
}
DI int wt_pos(int u) {
  const int k0 = u & 63, u1 = u >> 6;
  return (((k0 >> 4) * 64 + ((u1 >> 2) & 3) * 16 + (k0 & 15)) << 3) + (u1 >> 4) * 4 + (u1 & 3);
}
DI int wt_row(int pos) {
  const int bt = pos >> 3;
  return 16 * (bt >> 6) + (bt & 15) + 64 * (16 * ((pos >> 2) & 1) + 4 * ((bt >> 4) & 3) + (pos & 3));
}
DI void trig4(float f, float i0, f32x4& co, f32x4& si) {
#pragma unroll
  for (int e = 0; e < 4; ++e) {
    const float a = f * (i0 + (float)e);
    co[e] = __builtin_amdgcn_cosf(a); si[e] = __builtin_amdgcn_sinf(a);
  }
}
DI void trig_frag(float f, float i0, u32x4& co, u32x4& si) {
  f32x4 c0, s0, c1, s1;
  trig4(f, i0, c0, s0); trig4(f, i0 + 16.f, c1, s1);
  co = pack8(c0, c1); si = pack8(s0, s1);
  PIN(co); PIN(si);
}

#define LD128(off) __builtin_amdgcn_raw_buffer_load_b128(rs, (off), 0, 16)
#define LD32(off) __builtin_amdgcn_raw_buffer_load_b32(rs, (off), 0, 16)
#define ST128(v, off) __builtin_amdgcn_raw_buffer_store_b128((v), rs, (off), 0, 16)
#define ST64(v, off) __builtin_amdgcn_raw_buffer_store_b64((v), rs, (off), 0, 16)
#define ST32(v, off) __builtin_amdgcn_raw_buffer_store_b32((v), rs, (off), 0, 16)

__global__ __launch_bounds__(512, 2) void fused_kernel(const char* __restrict__ x, const char* __restrict__ wqkv,
                                                       const char* __restrict__ wo, const char* __restrict__ w_r,
                                                       const char* __restrict__ w_i, const char* __restrict__ gate_w,
                                                       char* __restrict__ ws, char* __restrict__ out) {
  extern __shared__ __attribute__((aligned(16))) char smem[];
  LAS char* sm = (LAS char*)smem;
  asm volatile("" :: "s"(x), "s"(wqkv), "s"(wo), "s"(w_r), "s"(w_i), "s"(gate_w), "s"(ws), "s"(out), "s"(sm));
  u64 pc0;
  asm volatile("s_getpc_b64 %0" : "=s"(pc0));
  pc0 &= ~127ull;
  const int tid = threadIdx.x, lane = tid & 63;
  const int w = __builtin_amdgcn_readfirstlane(tid >> 6);
  const int bid = blockIdx.x, xcd = bid & 7, idx = bid >> 3;
  const int mt = (xcd & 3) * 8 + (idx & 7), ct = (xcd >> 2) * 4 + (idx >> 3);
  const int rb = xcd * 8 + (idx & 7), cb = idx >> 3;
  const int c = lane & 15, g = lane >> 4;
  const __amdgpu_buffer_rsrc_t rs = __builtin_amdgcn_make_buffer_rsrc(ws, 0, WS_END, 0x00020000);
  const f32x4 zero = {0, 0, 0, 0};
  unsigned sigp = L_SYNC + (w & 3) * 4;
  PIN(sigp);
  ST(0);
  const int i8 = idx & 7, c0 = (idx + (idx >> 3)) & 3, flip = (idx >> 2) & 1;
  if (w >= 4) {
    const int wb = w - 4, bt = tid - 256;
    unsigned vx[4], vk[4], vq[4];
    const unsigned dst0 = wb * 4096;
#pragma unroll
    for (int i = 0; i < 4; ++i) {
      const int r = 16 * wb + 4 * i + g;
      vx[i] = (unsigned)((mt + 32 * r) * 1024 + ((c ^ (r & 15)) << 4) + 4096 - i * 1024);
    }
    auto issue_x = [&](int i) { dma4(vx, x - 4096 + ((c0 - i) & 3) * 256, dst0 + i * 16384); };
    issue_x(0);
#pragma unroll
    for (int i = 0; i < 4; ++i) {
      const int r = 16 * wb + 4 * i + g, sl = c ^ ((g | ((i >> 1) << 2)) << 1);
      vk[i] = (unsigned)(r * 3072 + (sl >> 3) * 1024 + ((sl & 7) << 4) + 4096 - i * 1024);
    }
    auto issue_kv = [&](int j) {
      const int blk = 2 * ((c0 - (j >> 1)) & 3) + ((j & 1) ^ flip);
      dma4(vk, wqkv - 4096 + 1024 + ct * 128 + (unsigned)blk * (64 * 3072), dst0 + ((j + 4) & 7) * 16384);
    };
    u32x4 gw[4];
#pragma unroll
    for (int i = 0; i < 4; ++i)
      asm volatile("global_load_dwordx4 %0, %1, %2" : "=v"(gw[i]) : "v"(c * 16), "s"(gate_w + ((c0 - i) & 3) * 256) : "memory");
    issue_kv(0);
    issue_x(1);
    if (wb == 0) *(LAS u32x2*)(sm + L_SYNC + lane * 8) = u32x2{0u, 0u};
#pragma unroll
    for (int i = 0; i < 4; ++i) {
      const int r = 16 * wb + 4 * i + g, sl = c ^ ((g | ((i >> 1) << 2)) << 1);
      vq[i] = (unsigned)(r * 3072 + ((sl >> 3) ^ flip) * (64 * 3072) + ((sl & 7) << 4) + 4096 - i * 1024);
      PIN(vq[i]);
    }
    auto issue_q = [&](int j) { dma4(vq, wqkv - 4096 + ct * 128 + (unsigned)((c0 - j) & 3) * (128 * 3072), dst0 + (j + 4) * 16384); };
    const float g4 = (float)(4 * g);
    u32x4 m0c[2], m0s[2], m0n[2], mac[2], mas[2], mcc[2][2], mcs[2][2];
    f32x4 w1c[2], w1s[2], w2c[2], w2s[2];
    auto tab = [&](int row) { return (unsigned)(7 * 16384 + row * 256 + ((g ^ (row & 15)) << 4)); };
    {
      f32x4 c0v, s0v;
      trig4((float)(16 * wb + c) * (1.f / 64.f), g4, c0v, s0v);
      f32x4 c1v = c0v, s1v = s0v;
      crot(c1v, s1v, (c & 1) ? 0.f : ((c & 2) ? -1.f : 1.f), (c & 1) ? ((c & 2) ? -1.f : 1.f) : 0.f);
      const unsigned sg = (c & 1) ? 0x80008000u : 0u;
      m0c[0] = pack8(c0v, c1v); m0s[0] = pack8(s0v, s1v);
      PIN(m0c[0]); PIN(m0s[0]);
      m0c[1] = m0c[0] ^ sg; m0s[1] = m0s[0] ^ sg;
#pragma unroll
      for (int i = 0; i < 2; ++i) {
        m0n[i] = neg8(m0s[i]);
        *LP(u32x4, tab(16 * wb + c) ^ (64u * i)) = m0c[i];
        *LP(u32x4, tab(16 * wb + c) ^ (64u * i + 128u)) = m0s[i];
      }
    }
#pragma unroll
    for (int tt = 0; tt < 2; ++tt) {
      trig4((float)(16 * wb + c) * (1.f / 2048.f), g4 + 16.f * tt, w1c[tt], w1s[tt]);
      trig4((float)(16 * tt + c) * (1.f / 2048.f), g4 + 16.f * wb, w2c[tt], w2s[tt]);
      PIN(w1c[tt]); PIN(w1s[tt]); PIN(w2c[tt]); PIN(w2s[tt]);
    }
    const int gr = 8 * ct + 2 * wb + (g & 1);
    unsigned gxp = gr * 256 + ((c ^ (gr & 15)) << 4);
    PIN(gxp);
    u32x4 gx[4];
    u64 epoch;
    asm volatile("s_load_dwordx2 %0, %1, 0x0\n\ts_waitcnt lgkmcnt(0)" : "=s"(epoch) : "s"(ws) : "memory");
    const u64 tag = TAG_OF(epoch);
    VMW(12); BAR();
    __builtin_amdgcn_s_setprio(3);
    gx[0] = ld16(gxp, 0);
    issue_x(2); issue_x(3);
#pragma unroll
    for (int i = 0; i < 2; ++i) {
      const unsigned ta = tab(2 * (16 * i + c)), tc = tab(32 * (wb >> 1) + 8 * (c >> 2) + 4 * i + (c & 3));
      mac[i] = *LP(u32x4, ta); mas[i] = *LP(u32x4, ta ^ 128u);
      mcc[i][0] = *LP(u32x4, tc); mcc[i][1] = *LP(u32x4, tc ^ 64u); mcs[i][0] = *LP(u32x4, tc ^ 128u); mcs[i][1] = *LP(u32x4, tc ^ 192u);
    }
    asm volatile("s_waitcnt vmcnt(8)" : "+v"(gw[0]), "+v"(gw[1]), "+v"(gw[2]), "+v"(gw[3]) :: "memory");
    BAR();
    gx[1] = ld16(gxp, 16384);
    issue_kv(1); issue_kv(2);
    VMW(12); BAR();
    gx[2] = ld16(gxp, 32768);
    issue_kv(3); issue_kv(4);
    VMW(16); BAR();
    gx[3] = ld16(gxp, 49152);
    issue_kv(5); issue_kv(6);
    VMW(16); BAR();
    issue_kv(7); issue_q(0);
    unsigned pfw;
    asm volatile("global_load_dword %0, %1, %2" : "=v"(pfw) : "v"((lane & 31) * 128), "s"(wo + idx * 16384 + wb * 4096) : "memory");
    VMW(17); BAR();
    issue_q(1); issue_q(2);
    VMW(13); BAR();
    issue_q(3);
    ST(1);
    {
      float s = 0.f;
#pragma unroll
      for (int i = 0; i < 4; ++i)
#pragma unroll
        for (int j = 0; j < 4; ++j) s += bflo(gx[i][j]) * bflo(gw[i][j]) + bfhi(gx[i][j]) * bfhi(gw[i][j]);
      s = row_sum16(s);
      if ((lane & 47) == 0) ST32(__float_as_uint(__builtin_amdgcn_rcpf(1.f + __expf(-s))), WS_G + (64 * mt + gr) * 4);
    }
    asm volatile("s_waitcnt vmcnt(0)" : "+v"(pfw) :: "memory");
    SIG(E_QL);
    const int k = 32 * ct + mt;
    unsigned o_x = (unsigned)(WS_XT + k * KS + bt * 16), o_w = (unsigned)(WS_WT + k * KS + bt * 32);
    unsigned l_xw = L_SCR + (bt >> 4) * 256 + ((c ^ (bt >> 4)) << 4);
    unsigned l_ca[4];
#pragma unroll
    for (int i = 0; i < 4; ++i) { l_ca[i] = L_SCR + c * 256 + (((4 * i + g) ^ c) << 4); PIN(l_ca[i]); }
    unsigned l_ew = L_SCR + 8192 + (wb >> 1) * 4096 + lane * 16 + (wb & 1) * 8, l_er = L_SCR + 8192 + (wb & 1) * 2048 + lane * 16;
    unsigned o_z = (unsigned)(WS_Z + k * KS + (2 * (16 * (wb & 1) + c) + (wb >> 1)) * 128 + g * 16);
    PIN(o_x); PIN(o_w); PIN(l_xw); PIN(l_ew); PIN(l_er); PIN(o_z);
    ST(2);
    if (wb == 0) { poll_all(ws, LN_FB + ct * 32 + (lane & 31), tag); SIG(E_FB); }
    else ev_wait<1>(E_FB);
    __builtin_amdgcn_s_setprio(2);
    ST(3);
    {
      const u32x4 x0 = LD128(o_x), x1 = LD128(o_x + 4096);
      const u32x4 wv[2] = {LD128(o_w), LD128(o_w + 16)};
      u32x4 ar, ai, an;
      u32x4 ca[2][2][2];
      auto twiddle_dft32 = [&](const f32x4 (&yr)[2], const f32x4 (&yi)[2], f32x4 (&xr)[2], f32x4 (&xi)[2]) {
        const f32x4 r0 = yr[0] * w1c[0] + yi[0] * w1s[0], r1 = yr[1] * w1c[1] + yi[1] * w1s[1];
        const u32x4 br = pack8(r0, r1), bn = pack8(-r0, -r1);
        const u32x4 bi = pack8(yi[0] * w1c[0] - yr[0] * w1s[0], yi[1] * w1c[1] - yr[1] * w1s[1]);
#pragma unroll
        for (int ut = 0; ut < 2; ++ut) {
          xr[ut] = mfma32(mas[ut], bi, mfma32(mac[ut], br, zero));
          xi[ut] = mfma32(mas[ut], bn, mfma32(mac[ut], bi, zero));
        }
      };
      if (k != 0) {
        *LP(u32x4, l_xw) = x0; *(LAS u32x4*)(LP(char, l_xw) + 4096) = x1;
        SIG(E_X1);
        f32x4 fr[2] = {lo4(wv[0]), lo4(wv[1])}, fi[2] = {hi4(wv[0]), hi4(wv[1])};
        PIN(fr[0]); PIN(fr[1]); PIN(fi[0]); PIN(fi[1]);
        ST(4);
        ev_wait<15>(E_X1);
#pragma unroll
        for (int i = 0; i < 8; ++i) ca[i >> 2][(i >> 1) & 1][i & 1] = ld16(l_ca[i >> 1], (i & 1) * 4096);
        f32x4 yr[2] = {zero, zero}, yi[2] = {zero, zero}, xr[2], xi[2];
#pragma unroll
        for (int ks = 0; ks < 2; ++ks)
#pragma unroll
          for (int tt = 0; tt < 2; ++tt) { yr[tt] = mfma32(ca[0][ks][tt], m0c[ks], yr[tt]); yi[tt] = mfma32(ca[0][ks][tt], m0n[ks], yi[tt]); }
#pragma unroll
        for (int ks = 0; ks < 2; ++ks)
#pragma unroll
          for (int tt = 0; tt < 2; ++tt) { yr[tt] = mfma32(ca[1][ks][tt], m0s[ks], yr[tt]); yi[tt] = mfma32(ca[1][ks][tt], m0c[ks], yi[tt]); }
        twiddle_dft32(yr, yi, xr, xi);
        const f32x4 i0 = xr[0] * fi[0] + xi[0] * fr[0], i1 = xr[1] * fi[1] + xi[1] * fr[1];
        ar = pack8(xr[0] * fr[0] - xi[0] * fi[0], xr[1] * fr[1] - xi[1] * fi[1]);
        ai = pack8(i0, i1); an = pack8(-i0, -i1);
      } else {
        const u32x4 wn[2] = {LD128(o_w + 256 * KS), LD128(o_w + 256 * KS + 16)};
        unsigned mp[8];
#pragma unroll
        for (int e = 0; e < 8; ++e) { mp[e] = 4 * wt_pos((2048 - (16 * wb + c + 64 * (16 * (e >> 2) + 4 * g + (e & 3)))) & 2047); PIN(mp[e]); }
        *LP(u32x4, l_xw) = x0; *(LAS u32x4*)(LP(char, l_xw) + 4096) = x1;
        LAS u32x4* d = (LAS u32x4*)sm + bt * 2;
        d[0] = wv[0]; d[1] = wv[1]; d[512] = wn[0]; d[513] = wn[1];
        SIG(E_X1);
        ev_wait<15>(E_X1);
#pragma unroll
        for (int i = 0; i < 8; ++i) ca[i >> 2][(i >> 1) & 1][i & 1] = ld16(l_ca[i >> 1], (i & 1) * 4096);
        u32x4 m0[2], m1[2];
#pragma unroll
        for (int e = 0; e < 8; ++e) { m0[e >> 2][e & 3] = *LP(unsigned, mp[e]); m1[e >> 2][e & 3] = *(LAS unsigned*)(LP(char, mp[e]) + 8192); }
        f32x4 yr[2] = {zero, zero}, yi[2] = {zero, zero}, xr[2], xi[2], dr[2], di[2];
#pragma unroll
        for (int ks = 0; ks < 2; ++ks)
#pragma unroll
          for (int tt = 0; tt < 2; ++tt) { yr[tt] = mfma32(ca[0][ks][tt], m0c[ks], yr[tt]); yi[tt] = mfma32(ca[0][ks][tt], m0n[ks], yi[tt]); }
        twiddle_dft32(yr, yi, xr, xi);
#pragma unroll
        for (int ut = 0; ut < 2; ++ut) {
          const f32x4 fr = lo4(wv[ut]) + lo4(m0[ut]), fi = hi4(wv[ut]) - hi4(m0[ut]);
          dr[ut] = xr[ut] * fr - xi[ut] * fi; di[ut] = xr[ut] * fi + xi[ut] * fr;
          yr[ut] = zero; yi[ut] = zero;
        }
#pragma unroll
        for (int ks = 0; ks < 2; ++ks)
#pragma unroll
          for (int tt = 0; tt < 2; ++tt) { yr[tt] = mfma32(ca[1][ks][tt], m0s[ks], yr[tt]); yi[tt] = mfma32(ca[1][ks][tt], m0c[ks], yi[tt]); }
        twiddle_dft32(yr, yi, xr, xi);
#pragma unroll
        for (int ut = 0; ut < 2; ++ut) {
          const f32x4 fr = lo4(wn[ut]) + lo4(m1[ut]), fi = hi4(wn[ut]) - hi4(m1[ut]);
          dr[ut] += xr[ut] * fr - xi[ut] * fi; di[ut] += xr[ut] * fi + xi[ut] * fr;
        }
        ar = pack8(dr[0], dr[1]); ai = pack8(di[0], di[1]); an = pack8(-di[0], -di[1]);
      }
      ST(5);
#pragma unroll
      for (int tt = 0; tt < 2; ++tt) {
        const f32x4 vr = mfma32(an, mas[tt], mfma32(ar, mac[tt], zero));
        const f32x4 vi = mfma32(ar, mas[tt], mfma32(ai, mac[tt], zero));
        const f32x4 pr = vr * w2c[tt] - vi * w2s[tt], pi = vi * w2c[tt] + vr * w2s[tt];
        *(LAS u32x2*)(LP(char, l_ew) + tt * 2048) = u32x2{pk2(pr[0], pr[1]), pk2(pr[2], pr[3])};
        *(LAS u32x2*)(LP(char, l_ew) + tt * 2048 + 1024) = u32x2{pk2(pi[0], pi[1]), pk2(pi[2], pi[3])};
      }
      SIG(E_X2);
      ev_wait<15>(E_X2);
      f32x4 zr[2] = {zero, zero}, zi[2] = {zero, zero}, zs[2] = {zero, zero};
#pragma unroll
      for (int ks = 0; ks < 2; ++ks) {
        const u32x4 vr = ld16(l_er, ks * 4096), vi = ld16(l_er, ks * 4096 + 1024);
#pragma unroll
        for (int jt = 0; jt < 2; ++jt) {
          zr[jt] = mfma32(mcc[jt][ks], vr, zr[jt]); zi[jt] = mfma32(mcs[jt][ks], vr, zi[jt]);
          zs[jt] = mfma32(mcs[jt][ks], vi, zs[jt]); zi[jt] = mfma32(mcc[jt][ks], vi, zi[jt]);
        }
      }
      ST128(pack8(zr[0] - zs[0], zr[1] - zs[1]), o_z);
      ST128(pack8(zi[0], zi[1]), o_z + 64);
    }
    __builtin_amdgcn_s_setprio(0);
    ST(6);
    u32x4 ga[16];
    {
      const int d = 64 * cb + 16 * wb + c;
      const float df = (float)d * (1.f / 512.f);
      f32x4 ec, es, oc, os;
      trig4(-2.f * df, g4, ec, es);
      trig4(-2.f * df, g4 + 0.5f, oc, os);
      const float ra = __builtin_amdgcn_cosf(-32.f * df), rb_ = __builtin_amdgcn_sinf(-32.f * df);
#pragma unroll
      for (int ss = 0; ss < 4; ++ss) {
        ga[ss] = u32x4{pk2(ec[0], es[0]), pk2(ec[1], es[1]), pk2(ec[2], es[2]), pk2(ec[3], es[3])};
        ga[8 + ss] = u32x4{pk2(oc[0], os[0]), pk2(oc[1], os[1]), pk2(oc[2], os[2]), pk2(oc[3], os[3])};
        PIN(ga[ss]); PIN(ga[8 + ss]);
        if (ss < 3) { crot(ec, es, ra, rb_); crot(oc, os, ra, rb_); }
      }
      SB();
      DRAIN();
      SIG(E_ZD);
      if (wb == 0) {
        ev_wait<15>(E_ZD);
        if (lane < REP) __hip_atomic_fetch_add((GAS u64*)(ws + (size_t)(LN_CZ + lane * 16 + 2 * ct + (mt >> 4)) * 128), 1ull, RLX_AGENT);
      }
      ST(7);
      const unsigned sel = (c & 1) ? 0x01000302u : 0x03020100u;
      const unsigned sgn = (c & 3) == 0 ? 0u : (c & 3) == 1 ? 0x80000000u : (c & 3) == 2 ? 0x80008000u : 0x00008000u;
#pragma unroll
      for (int f = 0; f < 8; ++f) {
        const int src = (f >> 2) * 8 + (f & 3);
#pragma unroll
        for (int i = 0; i < 4; ++i) ga[src + 4][i] = __builtin_amdgcn_perm(0u, ga[src][i], sel) ^ sgn;
        PIN(ga[src + 4]);
      }
      if (g == 0) ga[0][0] = (c & 1) ? 0xBE803E80u : 0x3E803E80u;
      PIN(ga[0]);
    }
    unsigned o_zl = (unsigned)(WS_Z + (64 * wb + (lane >> 3)) * KS + rb * 128 + (lane & 7) * 16);
    unsigned l_zw, l_z0, l_z1;
    {
      const int kk = 64 * wb + (lane >> 3), R = (((kk & 1) * 128 + (kk >> 1)) << 1) + ((lane >> 2) & 1);
      l_zw = R * 64 + ((lane & 3) << 4);
      l_z0 = (8 * g + (c >> 2)) * 64 + 8 * (c & 3) + ((g & 1) << 5);
      l_z1 = l_z0 ^ 32u;
    }
    unsigned l_zw1 = l_zw ^ 32u;
    PIN(o_zl); PIN(l_zw); PIN(l_zw1); PIN(l_z0); PIN(l_z1);
    __builtin_amdgcn_s_setprio(BPRIO);
    ev_wait<15>(E_WR);
    ST(11);
    if (wb == 0) { poll_all(ws, LN_CZ + (bid % REP) * 16 + c, tag + 16); SIG(E_FZ); }
    else ev_wait<1>(E_FZ);
    ST(8);
    {
      u32x4 z[8];
#pragma unroll
      for (int i = 0; i < 8; ++i) z[i] = __builtin_amdgcn_raw_buffer_load_b128(rs, o_zl, i * 8 * KS, 16);
      ev_wait<15>(E_WR);
#pragma unroll
      for (int i = 0; i < 8; ++i) *(LAS u32x4*)(LP(char, (i & 1) ? l_zw1 : l_zw) + i * 512) = z[i];
    }
    SIG(E_ZI);
    ST(9);
    ev_wait<15>(E_ZI);
    f32x4 acc[2][2] = {{zero, zero}, {zero, zero}};
    {
      u32x4 zf[3][2][2];
      auto ldz = [&](int n) {
#pragma unroll
        for (int i = 0; i < 2; ++i) { zf[n % 3][i][0] = trfrag(l_z0, (2 * n + i) * 2048, 256); zf[n % 3][i][1] = trfrag(l_z1, (2 * n + i) * 2048, 256); }
      };
      ldz(0); ldz(1);
      SB();
#pragma unroll
      for (int n = 0; n < 8; ++n) {
        if (n < 6) ldz(n + 2);
#pragma unroll
        for (int i = 0; i < 2; ++i) {
          acc[n >> 2][0] = mfma32(ga[2 * n + i], zf[n % 3][i][0], acc[n >> 2][0]);
          acc[n >> 2][1] = mfma32(ga[2 * n + i], zf[n % 3][i][1], acc[n >> 2][1]);
        }
#pragma unroll
        for (int i = 0; i < 4; ++i) { __builtin_amdgcn_sched_group_barrier(0x100, 2, 0); __builtin_amdgcn_sched_group_barrier(0x8, 1, 0); }
        SB();
      }
    }
    ST(10);
    {
      unsigned o_out[2];
#pragma unroll
      for (int tt = 0; tt < 2; ++tt) o_out[tt] = (unsigned)(((rb >> 1) + 32 * (32 * (rb & 1) + 16 * tt + c)) * 1024 + (64 * cb + 16 * wb + 4 * g) * 2);
      const volatile LAS unsigned* fp = (const volatile LAS unsigned*)(size_t)(L_SYNC + E_A * 16 + wb * 4);
      while (!__builtin_amdgcn_readfirstlane(*fp)) __builtin_amdgcn_s_sleep(1);
      CFENCE();
      const unsigned l_a = 98304u + wb * 8192 + lane * 16;
      const f32x2 sc = *(const LAS f32x2*)(LP(char, l_a) + 4096);
#pragma unroll
      for (int tt = 0; tt < 2; ++tt)
#pragma unroll
        for (int hf = 0; hf < 2; ++hf) {
          const f32x4 av = __builtin_bit_cast(f32x4, ld16(l_a, (hf * 2 + tt) * 1024));
          const f32x4 r = av + (hf ? acc[0][tt] - acc[1][tt] : acc[0][tt] + acc[1][tt]) * sc[tt];
          *(u32x2*)(out + o_out[tt] + 512 * hf) = u32x2{pk2(r[0], r[1]), pk2(r[2], r[3])};
        }
    }
    ST(13);
  } else {
    __builtin_amdgcn_s_setprio(1);
    u32x4 wrv, wiv;
    unsigned nyq = 0, nyq2 = 0, pf = 0;
    {
      const unsigned o_w = (unsigned)(wt_row(64 * mt + (tid >> 2)) * 257 + 32 * ct + 8 * (tid & 3)) * 2u;
      wrv = *(const u32x4u*)(w_r + o_w);
      wiv = *(const u32x4u*)(w_i + o_w);
      if (ct == 0 && w == 0) {
        const unsigned o_ny = (unsigned)(wt_row(64 * mt + lane) * 257 + 256) * 2u;
        nyq = *(const unsigned short*)(w_r + o_ny); nyq2 = *(const unsigned short*)(w_i + o_ny);
      }
    }
    if (w == 2 && lane < CODE_LINES) pf = *(const GAS unsigned*)(pc0 + min((idx * CODE_LINES + lane) * 128, CODE_MAX));
    const u32x2 ev = *(const u32x2*)ws;
    const int rec = (2 * ct + (w >> 1)) * 64 + 2 * mt + (w & 1);
    unsigned o_kp = (unsigned)(WS_KVP + rec * RS + lane * 16), o_zp = (unsigned)(WS_ZP + rec * 64 + g * 16);
    ST128((u32x4{~0u, ~0u, ~0u, ~0u}), o_kp);
    if (c == 0) ST128((u32x4{~0u, ~0u, ~0u, ~0u}), o_zp);
    SB();
    const int rh = w & 1, kt = w >> 1;
    const int kf = 32 * ct + 16 * kt + c;
    u32x4 xf[4][4][2];
    unsigned bb[2][4];
    {
      const unsigned fl = (unsigned)((c >> 2) | ((g & 1) << 2));
      const unsigned bu = (unsigned)((8 * g + (c >> 2)) * 256 + 8 * (c & 3)) + (((unsigned)(2 * kt) ^ fl) << 5);
#pragma unroll
      for (int u = 0; u < 4; ++u) {
        bb[0][u] = bu ^ (unsigned)((u & 1) * 32 + (u >> 1) * 128);
        bb[1][u] = bb[0][u] + 65536u;
      }
    }
    u32x4 fb[3][4];
    auto ldf = [&](int n) {
#pragma unroll
      for (int u = 0; u < 4; ++u) {
        const int slot = (4 + (n >> 1)) & 7;
        fb[n % 3][u] = trfrag(bb[slot >> 2][u], (slot & 3) * 16384 + (n & 1) * 8192, 1024);
      }
    };
    u64 tag;
    {
      u32x4 fc[4], fs[4];
      {
        const float kk = (float)kf * (1.f / 512.f);
        const float a0 = __builtin_amdgcn_fractf(kk * (float)(8 * g + 128 * ((c0 + 1) & 3)));
#pragma unroll
        for (int ks = 0; ks < 4; ++ks) {
          const float ak = __builtin_fmaf(kk, (float)(((ks & 2) ^ (flip << 1)) * 32), a0);
#pragma unroll
          for (int i = 0; i < 4; ++i) {
            const float p0 = __builtin_fmaf(kk, (float)(32 * (ks & 1) + 2 * i), ak), p1 = __builtin_fmaf(kk, (float)(32 * (ks & 1) + 2 * i + 1), ak);
            fc[ks][i] = pk2(__builtin_amdgcn_cosf(p0), __builtin_amdgcn_cosf(p1));
            fs[ks][i] = kf == 0 ? 0xBF803F80u
                                : pk2(__builtin_amdgcn_sinf(-p0), __builtin_amdgcn_sinf(-p1));
          }
          PIN(fc[ks]); PIN(fs[ks]);
        }
      }
      float ra = (c & 1) ? 0.f : ((c & 2) ? -1.f : 1.f), rbi = (c & 1) ? ((c & 2) ? 1.f : -1.f) : 0.f;
      unsigned l_sc = L_SCR + ((tid & 3) * 512 + ((tid >> 2) ^ ((tid & 3) << 3))) * 4;
      unsigned l_rd = L_SCR + (tid >> 4) * 256 + (((tid & 15) ^ ((tid >> 7) << 1)) << 4);
      unsigned o_wt = (unsigned)(WS_WT + (32 * ct + (tid >> 4)) * KS + (64 * mt + 4 * (tid & 15)) * 4);
      unsigned o_xt = (unsigned)(WS_XT + kf * KS + mt * 256 + rh * 64 + g * 16);
      unsigned xa[4];
#pragma unroll
      for (int ks = 0; ks < 4; ++ks) { xa[ks] = rh * 8192 + c * 256 + (((4 * ks + g) ^ (flip << 3) ^ c) << 4); PIN(xa[ks]); }
#pragma unroll
      for (int u = 0; u < 4; ++u) { PIN(bb[0][u]); PIN(bb[1][u]); }
      PIN(l_sc); PIN(l_rd); PIN(o_wt); PIN(o_xt); PIN(ra); PIN(rbi); PIN(o_kp); PIN(o_zp);
      ST(1);
      f32x4 xr[4][2], xi[4][2];
      auto ldx = [&](int s) {
#pragma unroll
        for (int ks = 0; ks < 4; ++ks)
#pragma unroll
          for (int tt = 0; tt < 2; ++tt) xf[s][ks][tt] = ld16(xa[ks], s * 16384 + tt * 4096);
      };
      auto mmf = [&](int s) {
#pragma unroll
        for (int tt = 0; tt < 2; ++tt) { xr[s][tt] = zero; xi[s][tt] = zero; }
#pragma unroll
        for (int ks = 0; ks < 4; ++ks)
#pragma unroll
          for (int tt = 0; tt < 2; ++tt) {
            xr[s][tt] = mfma32(xf[s][ks][tt], fc[ks], xr[s][tt]);
            xi[s][tt] = mfma32(xf[s][ks][tt], fs[ks], xi[s][tt]);
          }
      };
      auto horner = [&](int s) {
#pragma unroll
        for (int tt = 0; tt < 2; ++tt) {
          xr[s][tt] = (xr[s][tt] + xr[s - 1][tt] * ra) - xi[s - 1][tt] * rbi;
          xi[s][tt] = (xi[s][tt] + xr[s - 1][tt] * rbi) + xi[s - 1][tt] * ra;
        }
      };
      auto mixv = [&]() {
#pragma unroll
        for (int i = 0; i < 16; ++i) { __builtin_amdgcn_sched_group_barrier(0x8, 1, 0); __builtin_amdgcn_sched_group_barrier(0x2 | 0x80 | 0x40, 2, 0); }
        SB();
      };
      BAR();
      ldx(0);
      {
        PIN(pf); PIN(nyq); PIN(nyq2);
        tag = TAG_OF(((u64)(unsigned)__builtin_amdgcn_readfirstlane(ev.y) << 32) | (unsigned)__builtin_amdgcn_readfirstlane(ev.x));
        LAS unsigned* sc = LP(unsigned, l_sc);
#pragma unroll
        for (int j = 0; j < 4; ++j) {
          sc[(2 * j) * 64] = __builtin_amdgcn_perm(wiv[j], wrv[j], 0x05040100u);
          sc[(2 * j + 1) * 64] = __builtin_amdgcn_perm(wiv[j], wrv[j], 0x07060302u);
        }
        if (ct == 0 && w == 0) ST32(nyq | (nyq2 << 16), WS_WT + 256 * KS + (64 * mt + lane) * 4);
        if (mt == 0 && w == 1 && lane < 4 * REP)
          put_flag(ws, LN_CZ + (lane >> 1) * 16 + 2 * ct + (lane & 1), tag);
      }
      BAR();
      const u32x4 wt0 = ld16(l_rd, 0), wt1 = ld16(l_rd ^ 64u, 4096);
      ldx(1); SB(); mmf(0);
      BAR();
      ldx(2); SB(); mmf(1);
      BAR();
      ldx(3); SB();
      mmf(2); horner(1);
      ST128(wt0, o_wt); ST128(wt1, o_wt + 16 * KS);
      mixv();
      mmf(3); horner(2);
      mixv();
      ldf(0); ldf(1);
      horner(3);
      ST128(pack8(xr[3][0], xr[3][1]), o_xt);
      ST128(pack8(xi[3][0], xi[3][1]), o_xt + 128);
    }
    ST(2);
    auto mix = [&]() {
#pragma unroll
      for (int i = 0; i < 8; ++i) { __builtin_amdgcn_sched_group_barrier(0x100, 1, 0); __builtin_amdgcn_sched_group_barrier(0x8, 1, 0); }
      SB();
    };
    {
      f32x4 acc[2][4];
#pragma unroll
      for (int t = 0; t < 2; ++t)
#pragma unroll
        for (int u = 0; u < 4; ++u) acc[t][u] = zero;
#pragma unroll
      for (int n = 0; n < 16; ++n) {
        if (n == FBN) DRAIN();
        if (n == 0 || n == 4 || n == 8) BAR();
        if (n == FBN) { if (tid == 0) put_flag(ws, LN_FB + ct * 32 + mt, tag); SB(); }
        if (n < 14) ldf(n + 2);
#pragma unroll
        for (int u = 0; u < 4; ++u)
#pragma unroll
          for (int t = 0; t < 2; ++t) acc[t][u] = mfma32(xf[n >> 2][n & 3][t], fb[n % 3][u], acc[t][u]);
        mix();
      }
      ST(3);
      const u32x4 ones = {0x3f803f80u, 0x3f803f80u, 0x3f803f80u, 0x3f803f80u};
      f32x4 kv[2], zz[2];
#pragma unroll
      for (int n = 0; n < 2; ++n) {
        const u32x4 pk = pack8(phi4(acc[0][n]), phi4(acc[1][n]));
        kv[n] = mfma32(pk, pack8(acc[0][2 + n], acc[1][2 + n]), zero);
        zz[n] = mfma32(pk, ones, zero);
      }
      u32x4 kvo = pack8(kv[0], kv[1]), zo = pack8(zz[0], zz[1]);
#pragma unroll
      for (int e = 0; e < 4; ++e) {
        kvo[e] = min(kvo[e], 0xfffffffeu);
        zo[e] = min(zo[e], 0xfffffffeu);
      }
      ST128(kvo, o_kp);
      if (c == 0) ST128(zo, o_zp);
    }
    ST(4);
    {
      const int hp = 2 * ct + (mt >> 4), sub = mt & 15;
      u32x4 pv[4];
      unsigned pz;
      auto ldp = [&]() {
#pragma unroll
        for (int i = 0; i < 4; ++i)
          pv[i] = __builtin_amdgcn_raw_buffer_load_b128(rs, WS_KVP + (hp * 64 + (lane >> 2)) * RS + sub * 64 + (lane & 3) * 16, i * 16 * RS, 16);
        pz = LD32(WS_ZP + (hp * 64 + lane) * 64 + sub * 4);
      };
      f32x4 acc[2][2] = {{zero, zero}, {zero, zero}};
      ev_wait<15>(E_QL);
      SB();
      ldf(0); ldf(1);
      SB();
#pragma unroll
      for (int n = 0; n < 8; ++n) {
        if (n < 6) ldf(n + 2);
        if (n == 6) {
          SIG(E_QR);
          if (w == 0) { ev_wait<1>(E_FB); ldp(); }
          SB();
        }
#pragma unroll
        for (int u = 0; u < 4; ++u)
#pragma unroll
          for (int t = 0; t < 2; ++t) acc[u & 1][t] = mfma32(fb[n % 3][u], xf[n >> 1][2 * (u >> 1) + (n & 1)][t], acc[u & 1][t]);
        mix();
      }
#pragma unroll
      for (int n = 0; n < 2; ++n)
        ST128(pack8(phi4(acc[n][0]), phi4(acc[n][1])), WS_PQ + ((2 * mt + rh) * 32 + 4 * ct + 2 * kt + n) * 1024 + lane * 16);
      SB();
      ST(5);
      if (w == 0) {
        for (unsigned spins = 0; spins < SPIN_CAP; ++spins) {
          unsigned m = pz;
#pragma unroll
          for (int i = 0; i < 4; ++i) m = max(max(m, max(pv[i][0], pv[i][1])), max(pv[i][2], pv[i][3]));
          if (__all(m != ~0u)) break;
          CFENCE();
          ldp();
        }
        ST(6);
        const unsigned one = (c & 1) ? 0x3f800000u : 0x00003f80u;
        const u32x4 sel = {(c >> 1) == 0 ? one : 0u, (c >> 1) == 1 ? one : 0u, (c >> 1) == 2 ? one : 0u, (c >> 1) == 3 ? one : 0u};
        f32x4 s = zero;
#pragma unroll
        for (int i = 0; i < 4; ++i) s = mfma32(sel, pv[i], s);
        const f32x4 zq = mfma32(sel, u32x4{pz, 0u, 0u, 0u}, zero);
#pragma unroll
        for (int e = 0; e < 4; ++e) s[e] = dpp_add<0x128>(dpp_add<0x124>(s[e]));
        const float z0 = row_sum16(zq[0]), z1 = row_sum16(zq[1]);
        if ((lane & 44) == 0) ST64((u32x2{pk2(s[0], s[1]), pk2(s[2], s[3])}), WS_KVF + hp * 1024 + (sub * 4 + c) * 16 + g * 8);
        if (lane == 0) ST32(pk2(z0, z1), WS_ZF + hp * 64 + sub * 4);
        VMW(2);
        ev_wait<14>(E_PQ);
        if (lane == 0) put_flag(ws, LN_FQ + mt * 8 + ct, tag);
        VMW(1);
        if (lane < REP) __hip_atomic_fetch_add((GAS u64*)(ws + (size_t)(LN_CK + lane * 16 + 2 * ct + (mt >> 4)) * 128), 1ull, RLX_AGENT);
        SIG(E_KA);
      } else {
        unsigned voff[4];
#pragma unroll
        for (int i = 0; i < 4; ++i) {
          const int r = 4 * i + g, sl = c ^ ((r & 7) << 1);
          voff[i] = (unsigned)(r * 1024 + (sl >> 3) * 512 + ((sl & 7) << 4) + 4096 - i * 1024);
        }
        ev_wait<15>(E_QR);
#pragma unroll 1
        for (int gj = 3 + w; gj < 36; gj += 3) {
          const int gi = gj & 31;
          if (gj >= 32 && (ct | mt) == 0) ev_wait<15>(E_X2);
          dma4(voff, wo - 4096 + cb * 128 + ((gi + 4 * i8) & 31) * 16384, gi * 4096);
          VMW(WO_THR);
          if (gj < 7) { VMW(4); SIG(E_PQ); }
        }
        DRAIN();
        SIG(E_WL);
      }
    }
    __builtin_amdgcn_s_setprio(APRIO);
    ST(7);
    u32x4 wa[16][2];
    {
      const unsigned f7 = (unsigned)((4 * g + (c >> 2)) & 7);
      unsigned pw[2][2];
#pragma unroll
      for (int hf = 0; hf < 2; ++hf) {
        pw[hf][0] = (4 * g + (c >> 2)) * 256 + 8 * (c & 3) + ((((unsigned)w + 4u * hf) ^ f7) << 5);
        pw[hf][1] = pw[hf][0] + 65536u;
        PIN(pw[hf][0]); PIN(pw[hf][1]);
      }
      if (w == 0) { poll_all(ws, LN_FQ + (rb >> 1) * 8 + (lane & 7), tag); SIG(E_FQ); }
      ev_wait<14>(E_WL);
#pragma unroll
      for (int st = 0; st < 16; ++st)
#pragma unroll
        for (int hf = 0; hf < 2; ++hf) wa[st][hf] = trfrag(pw[hf][st >> 3], (st & 7) * 8192, 4096);
      SIG(E_WR);
    }
    SB();
    ST(8);
    if (w == 1 || w == 2) ev_wait<1>(E_KA);
    if (w == 2) {
      poll_all(ws, LN_CK + (bid % REP) * 16 + c, tag + 16);
      SIG(E_FK);
      if (bid == 0 && lane == 0) *(u32x2*)ws = u32x2{ev.x + 1u, ev.y};
    }
    ev_wait<1>(E_FQ);
    u32x4 pq[8];
#pragma unroll
    for (int i = 0; i < 8; ++i) pq[i] = LD128(WS_PQ + (rb * 32 + 8 * w + i) * 1024 + lane * 16);
    float gate[2];
#pragma unroll
    for (int tt = 0; tt < 2; ++tt) gate[tt] = __uint_as_float(LD32(WS_G + (32 * rb + 16 * tt + c) * 4));
    unsigned l_o[4];
#pragma unroll
    for (int hp = 0; hp < 4; ++hp) { l_o[hp] = 65536u + ((4 * w + hp - 2 * i8) & 15) * 2048 + lane * 16; PIN(l_o[hp]); }
    unsigned o_kf = (unsigned)(WS_KVF + 4 * w * 1024 + lane * 16), o_zf = (unsigned)(WS_ZF + 4 * w * 64 + g * 16), l_or = 65536u + lane * 16;
    PIN(o_kf); PIN(o_zf); PIN(l_or);
    ev_wait<15>(E_WR);
    SB();
    ST(9);
    ev_wait<4>(E_FK);
    ST(10);
    {
      u32x4 kf4[4], zf4[4];
#pragma unroll
      for (int i = 0; i < 4; ++i) { kf4[i] = LD128(o_kf + i * 1024); zf4[i] = LD128(o_zf + i * 64); }
      f32x4 num[2][2][2], den[2][2][2];
      auto mo = [&](int hp) {
#pragma unroll
        for (int tt = 0; tt < 2; ++tt)
#pragma unroll
          for (int n = 0; n < 2; ++n) {
            const u32x4 pp = pq[2 * hp + n];
            const u32x2 b = tt ? u32x2{pp.z, pp.w} : u32x2{pp.x, pp.y};
            const u32x2 ka = n ? u32x2{kf4[hp].z, kf4[hp].w} : u32x2{kf4[hp].x, kf4[hp].y};
            const u32x2 za = n ? u32x2{zf4[hp].z, zf4[hp].w} : u32x2{zf4[hp].x, zf4[hp].y};
            den[hp & 1][tt][n] = mfma16(za, b, f32x4{1e-6f, 1e-6f, 1e-6f, 1e-6f});
            num[hp & 1][tt][n] = mfma16(ka, b, zero);
          }
      };
      mo(0);
      SB();
#pragma unroll
      for (int hp = 0; hp < 4; ++hp) {
        if (hp < 3) mo(hp + 1);
        SB();
#pragma unroll
        for (int tt = 0; tt < 2; ++tt)
          *(LAS u32x4*)(LP(char, l_o[hp]) + tt * 1024) = pack8(num[hp & 1][tt][0] * __builtin_amdgcn_rcpf(den[hp & 1][tt][0][0]),
                                                               num[hp & 1][tt][1] * __builtin_amdgcn_rcpf(den[hp & 1][tt][1][0]));
        SB();
      }
    }
    SIG(E_O);
    ST(11);
    ev_wait<15>(E_O);
    f32x4 acc[2][2] = {{zero, zero}, {zero, zero}};
    {
#pragma unroll
      for (int st = 0; st < 16; ++st)
#pragma unroll
        for (int tt = 0; tt < 2; ++tt) {
          const u32x4 ob = ld16(l_or, (st * 2 + tt) * 1024);
          acc[0][tt] = mfma32(wa[st][0], ob, acc[0][tt]);
          acc[1][tt] = mfma32(wa[st][1], ob, acc[1][tt]);
        }
      __builtin_amdgcn_sched_group_barrier(0x100, 8, 0);
#pragma unroll
      for (int i = 0; i < 24; ++i) { __builtin_amdgcn_sched_group_barrier(0x8, 2, 0); __builtin_amdgcn_sched_group_barrier(0x100, 1, 0); }
      __builtin_amdgcn_sched_group_barrier(0x8, 16, 0);
    }
#pragma unroll
    for (int i = 0; i < 4; ++i) *(LAS f32x4*)(LP(char, l_or) + 32768 + w * 8192 + i * 1024) = acc[i >> 1][i & 1] * gate[i & 1];
    *(LAS f32x2*)(LP(char, l_or) + 32768 + w * 8192 + 4096) = f32x2{(1.f - gate[0]) * (2.f / (2048.f * 512.f)), (1.f - gate[1]) * (2.f / (2048.f * 512.f))};
    SIG(E_A);
    ST(12);
  }
}

extern "C" void kernel_launch(void* const* d_in, const int* in_sizes, int n_in, void* d_out, int out_size,
                              void* d_ws, size_t ws_size, hipStream_t stream) {
  (void)hipFuncSetAttribute((const void*)fused_kernel, hipFuncAttributeMaxDynamicSharedMemorySize, LDS_BYTES);
  fused_kernel<<<256, 512, LDS_BYTES, stream>>>((const char*)d_in[0], (const char*)d_in[1], (const char*)d_in[2],
                                                (const char*)d_in[4], (const char*)d_in[5], (const char*)d_in[6],
                                                (char*)d_ws, (char*)d_out);
}
```

```cpp
#define CODE_LINES 8
#define CODE_MAX 30208
#define OPT_BW
#define OPT_TW0
#define LDPN 4
#include <hip/hip_runtime.h>
#include <hip/hip_bf16.h>
#include <stdint.h>

typedef short s16x4 __attribute__((ext_vector_type(4)));
typedef short s16x8 __attribute__((ext_vector_type(8)));
typedef float f32x2 __attribute__((ext_vector_type(2)));
typedef float f32x4 __attribute__((ext_vector_type(4)));
typedef unsigned u32x2 __attribute__((ext_vector_type(2)));
typedef unsigned u32x4 __attribute__((ext_vector_type(4)));
typedef unsigned u32x4u __attribute__((ext_vector_type(4), aligned(2)));
typedef __bf16 bf16x2 __attribute__((ext_vector_type(2)));
typedef unsigned long long u64;
#define DI __device__ __forceinline__
#define LAS __attribute__((address_space(3)))
#define GAS __attribute__((address_space(1)))
#define RLX_AGENT __ATOMIC_RELAXED, __HIP_MEMORY_SCOPE_AGENT
#define CFENCE() asm volatile("" ::: "memory")
#define PIN(v) asm volatile("" : "+v"(v))
#define DRAIN() asm volatile("s_waitcnt vmcnt(0)" ::: "memory")
#define LGKM0() __builtin_amdgcn_s_waitcnt(0xC07F)
#define SB() __builtin_amdgcn_sched_barrier(0)
#define VMW_(n) asm volatile("s_waitcnt vmcnt(" #n ")" ::: "memory")
#define VMW(n) VMW_(n)
#define LP(T, a) ((LAS T*)(size_t)(a))
#define WS_DBG (64u << 20)
#define BAR() do { CFENCE(); SB(); LGKM0(); __builtin_amdgcn_s_barrier(); SB(); CFENCE(); } while (0)
#define ST(i)

DI unsigned pk2(float a, float b) {
  f32x2 v = {a, b};
  return __builtin_bit_cast(unsigned, __builtin_convertvector(v, bf16x2));
}
DI float bflo(unsigned u) { return __uint_as_float(u << 16); }
DI float bfhi(unsigned u) { return __uint_as_float(u & 0xffff0000u); }
DI f32x4 lo4(u32x4 u) { return f32x4{bflo(u.x), bflo(u.y), bflo(u.z), bflo(u.w)}; }
DI f32x4 hi4(u32x4 u) { return f32x4{bfhi(u.x), bfhi(u.y), bfhi(u.z), bfhi(u.w)}; }
DI f32x4 mfma32(u32x4 a, u32x4 b, f32x4 c) {
  return __builtin_amdgcn_mfma_f32_16x16x32_bf16(__builtin_bit_cast(s16x8, a), __builtin_bit_cast(s16x8, b), c, 0, 0, 0);
}
DI f32x4 mfma16(u32x2 a, u32x2 b, f32x4 c) {
  return __builtin_amdgcn_mfma_f32_16x16x16bf16_1k(__builtin_bit_cast(s16x4, a), __builtin_bit_cast(s16x4, b), c, 0, 0, 0);
}
DI u32x4 pack8(f32x4 a, f32x4 b) { return u32x4{pk2(a[0], a[1]), pk2(a[2], a[3]), pk2(b[0], b[1]), pk2(b[2], b[3])}; }
DI u32x4 neg8(u32x4 a) {
  PIN(a);
  a ^= 0x80008000u;
  PIN(a);
  return a;
}
DI f32x4 phi4(f32x4 t) {
  const f32x4 m = t * 1.4426950408889634f, r = t + 1e-6f;
  f32x4 e, q;
#pragma unroll
  for (int i = 0; i < 4; ++i) { e[i] = __builtin_amdgcn_exp2f(fminf(m[i], 0.f)); q[i] = fmaxf(r[i], 1e-6f); }
  return e + q;
}
DI void crot(f32x4& cr, f32x4& ci, float a, float b) {
  const f32x4 nr = cr * a - ci * b, ni = cr * b + ci * a;
  cr = nr; ci = ni;
}
DI u32x4 trfrag(unsigned a, int off, int hi_off) {
  s16x4 l = __builtin_amdgcn_ds_read_tr16_b64_v4i16((LAS s16x4*)(LP(char, a) + off));
  s16x4 h = __builtin_amdgcn_ds_read_tr16_b64_v4i16((LAS s16x4*)(LP(char, a) + off + hi_off));
  return __builtin_bit_cast(u32x4, __builtin_shufflevector(l, h, 0, 1, 2, 3, 4, 5, 6, 7));
}
DI u32x4 ld16(unsigned a, int off) { return *(const LAS u32x4*)(LP(char, a) + off); }
template <int CTRL> DI float dpp_add(float v) {
  return v + __int_as_float(__builtin_amdgcn_update_dpp(0, __float_as_int(v), CTRL, 0xf, 0xf, false));
}
DI float row_sum16(float v) { return dpp_add<0x128>(dpp_add<0x124>(dpp_add<0x4E>(dpp_add<0xB1>(v)))); }

#define KS 8192
#define RS 1024
#define REPV 8
constexpr int REP = REPV;
constexpr int LN_FB = 8, LN_FQ = LN_FB + 256, LN_CZ = LN_FQ + 256, LN_CK = LN_CZ + 16 * REP;
constexpr int WS_XT = 160 * 1024;
constexpr int WS_WT = WS_XT + 256 * KS;
constexpr int WS_KVP = WS_WT + 257 * KS;
constexpr int WS_ZP = WS_KVP + 16 * 64 * RS;
constexpr int WS_KVF = WS_ZP + 16 * 64 * 64;
constexpr int WS_ZF = WS_KVF + 16 * 1024;
constexpr int WS_G = WS_ZF + 2048;
constexpr int WS_PQ = WS_G + 8192;
constexpr int WS_Z = WS_PQ + 2048 * 1024;
constexpr int WS_WOF = WS_Z;
constexpr int WS_END = WS_Z + 256 * KS;
constexpr int L_SCR = 131072, L_ST = L_SCR + 30720, L_SYNC = L_SCR + 32768 - 512, LDS_BYTES = 163840;
enum { E_QL, E_FB, E_X1, E_X2, E_ZD, E_FZ, E_ZI, E_QR, E_PQ, E_WL, E_WR, E_KA, E_FQ, E_FK, E_O, E_G, E_A };
constexpr unsigned SPIN_CAP = 1u << 16;
#define POLL_SLEEP 3
#define WO_THR 4
#define FBN 8
#define BPRIO 0
#define APRIO 1
#define POLLK 1
#define TAG_OF(e) ((e) * 0x9E3779B97F4A7C15ull + 0x632BE59BD9B4E019ull)

#define SIG(ev) do { CFENCE(); *(volatile LAS unsigned*)(LP(char, sigp) + (ev) * 16) = 1u; CFENCE(); } while (0)
template <unsigned MASK> DI void ev_wait(int ev) {
  const volatile LAS u32x4* p = (const volatile LAS u32x4*)(size_t)(L_SYNC + ev * 16);
  for (;;) {
    const u32x4 v = *p;
    const unsigned a = ((MASK & 1) ? v.x : 1u) & ((MASK & 2) ? v.y : 1u) & ((MASK & 4) ? v.z : 1u) & ((MASK & 8) ? v.w : 1u);
    if (__builtin_amdgcn_readfirstlane(a)) break;
    __builtin_amdgcn_s_sleep(1);
  }
  CFENCE();
}
DI void poll_all_(const char* ws, unsigned line, u64 want) {
  const GAS u64* p = (const GAS u64*)(ws + (size_t)line * 128);
  for (unsigned spins = 0; spins < SPIN_CAP; ++spins) {
    u64 v_; const u64 z_ = 0;
    asm volatile("global_atomic_add_x2 %0, %1, %2, off sc0 sc1\n\ts_waitcnt vmcnt(0)" : "=&v"(v_) : "v"(p), "v"(z_) : "memory");
    if (__all(v_ == want)) break;
    __builtin_amdgcn_s_sleep(POLL_SLEEP);
  }
  __builtin_amdgcn_fence(__ATOMIC_ACQUIRE, "wavefront");
  CFENCE();
}
#define poll_all(ws, line, want, n) do { if (lane < (n)) poll_all_(ws, line, want); CFENCE(); } while (0)
#define TGATE(kind)
DI void put_flag(char* ws, unsigned line, u64 v) { __hip_atomic_store((GAS u64*)(ws + (size_t)line * 128), v, RLX_AGENT); }

DI void dma4(const unsigned (&voff)[4], const char* sb, unsigned dst) {
  unsigned keep;
  asm volatile("s_mov_b32 %0, m0\n\ts_mov_b32 m0, %6\n\ts_nop 0\n\t"
               "global_load_lds_dwordx4 %1, %5\n\t"
               "global_load_lds_dwordx4 %2, %5 offset:1024\n\t"
               "global_load_lds_dwordx4 %3, %5 offset:2048\n\t"
               "global_load_lds_dwordx4 %4, %5 offset:3072\n\t"
               "s_mov_b32 m0, %0"
               : "=&s"(keep) : "v"(voff[0]), "v"(voff[1]), "v"(voff[2]), "v"(voff[3]), "s"(sb), "s"(dst) : "memory");
}
DI int wt_pos(int u) {
  const int k0 = u & 63, u1 = u >> 6;
  return (((k0 >> 4) * 64 + ((u1 >> 2) & 3) * 16 + (k0 & 15)) << 3) + (u1 >> 4) * 4 + (u1 & 3);
}
DI int wt_row(int pos) {
  const int bt = pos >> 3;
  return 16 * (bt >> 6) + (bt & 15) + 64 * (16 * ((pos >> 2) & 1) + 4 * ((bt >> 4) & 3) + (pos & 3));
}
DI void trig4(float f, float i0, f32x4& co, f32x4& si) {
#pragma unroll
  for (int e = 0; e < 4; ++e) {
    const float a = f * (i0 + (float)e);
    co[e] = __builtin_amdgcn_cosf(a); si[e] = __builtin_amdgcn_sinf(a);
  }
}
DI void trig_frag(float f, float i0, u32x4& co, u32x4& si) {
  f32x4 c0, s0, c1, s1;
  trig4(f, i0, c0, s0); trig4(f, i0 + 16.f, c1, s1);
  co = pack8(c0, c1); si = pack8(s0, s1);
  PIN(co); PIN(si);
}

#define LD128(off) __builtin_amdgcn_raw_buffer_load_b128(rs, (off), 0, 16)
#define LD32(off) __builtin_amdgcn_raw_buffer_load_b32(rs, (off), 0, 16)
#define ST128(v, off) __builtin_amdgcn_raw_buffer_store_b128((v), rs, (off), 0, 16)
#define ST64(v, off) __builtin_amdgcn_raw_buffer_store_b64((v), rs, (off), 0, 16)
#define ST32(v, off) __builtin_amdgcn_raw_buffer_store_b32((v), rs, (off), 0, 16)

__global__ __launch_bounds__(512, 2) void fused_kernel(const char* __restrict__ x, const char* __restrict__ wqkv,
                                                       const char* __restrict__ wo, const char* __restrict__ w_r,
                                                       const char* __restrict__ w_i, const char* __restrict__ gate_w,
                                                       char* __restrict__ ws, char* __restrict__ out) {
  extern __shared__ __attribute__((aligned(16))) char smem[];
  LAS char* sm = (LAS char*)smem;
  asm volatile("" :: "s"(x), "s"(wqkv), "s"(wo), "s"(w_r), "s"(w_i), "s"(gate_w), "s"(ws), "s"(out), "s"(sm));
  u64 pc0;
  asm volatile("s_getpc_b64 %0" : "=s"(pc0));
  pc0 &= ~127ull;
  const int tid = threadIdx.x, lane = tid & 63;
  const int w = __builtin_amdgcn_readfirstlane(tid >> 6);
  const int bid = blockIdx.x, xcd = bid & 7, idx = bid >> 3;
  const int mt = (xcd & 3) * 8 + (idx & 7), ct = (xcd >> 2) * 4 + (idx >> 3);
  const int rb = xcd * 8 + (idx & 7), cb = idx >> 3;
  const int c = lane & 15, g = lane >> 4;
  const __amdgpu_buffer_rsrc_t rs = __builtin_amdgcn_make_buffer_rsrc(ws, 0, WS_END, 0x00020000);
  const f32x4 zero = {0, 0, 0, 0};
  unsigned sigp = L_SYNC + (w & 3) * 4;
  PIN(sigp);
  ST(0);
  const int i8 = idx & 7, c0 = (idx + (idx >> 3)) & 3, flip = (idx >> 2) & 1;
  if (w >= 4) {
    const int wb = w - 4, bt = tid - 256;
    unsigned vx[4], vk[4], vq[4];
    const unsigned dst0 = wb * 4096;
#pragma unroll
    for (int i = 0; i < 4; ++i) {
      const int r = 16 * wb + 4 * i + g;
      vx[i] = (unsigned)((mt + 32 * r) * 1024 + ((c ^ (r & 15)) << 4) + 4096 - i * 1024);
    }
    auto issue_x = [&](int i) { dma4(vx, x - 4096 + ((c0 - i) & 3) * 256, dst0 + i * 16384); };
    issue_x(0);
#pragma unroll
    for (int i = 0; i < 4; ++i) {
      const int r = 16 * wb + 4 * i + g, sl = c ^ ((g | ((i >> 1) << 2)) << 1);
      vk[i] = (unsigned)(r * 3072 + (sl >> 3) * 1024 + ((sl & 7) << 4) + 4096 - i * 1024);
    }
    auto issue_kv = [&](int j) {
      const int blk = 2 * ((c0 - (j >> 1)) & 3) + ((j & 1) ^ flip);
      dma4(vk, wqkv - 4096 + 1024 + ct * 128 + (unsigned)blk * (64 * 3072), dst0 + ((j + 4) & 7) * 16384);
    };
    u32x4 gw[4];
#pragma unroll
    for (int i = 0; i < 4; ++i)
      asm volatile("global_load_dwordx4 %0, %1, %2" : "=v"(gw[i]) : "v"(c * 16), "s"(gate_w + ((c0 - i) & 3) * 256) : "memory");
    issue_kv(0);
    issue_x(1);
    if (wb == 0) *(LAS u32x2*)(sm + L_SYNC + lane * 8) = u32x2{0u, 0u};
#pragma unroll
    for (int i = 0; i < 4; ++i) {
      const int r = 16 * wb + 4 * i + g, sl = c ^ ((g | ((i >> 1) << 2)) << 1);
      vq[i] = (unsigned)(r * 3072 + ((sl >> 3) ^ flip) * (64 * 3072) + ((sl & 7) << 4) + 4096 - i * 1024);
      PIN(vq[i]);
    }
    auto issue_q = [&](int j) { dma4(vq, wqkv - 4096 + ct * 128 + (unsigned)((c0 - j) & 3) * (128 * 3072), dst0 + (j + 4) * 16384); };
    const float g4 = (float)(4 * g);
    u32x4 m0c[2], m0s[2], m0n[2], mac[2], mas[2], mcc[2][2], mcs[2][2];
    f32x4 w1c[2], w1s[2], w2c[2], w2s[2];
    auto tab = [&](int row) { return (unsigned)(7 * 16384 + row * 256 + ((g ^ (row & 15)) << 4)); };
    {
      f32x4 c0v, s0v;
      trig4((float)(16 * wb + c) * (1.f / 64.f), g4, c0v, s0v);
      f32x4 c1v = c0v, s1v = s0v;
      crot(c1v, s1v, (c & 1) ? 0.f : ((c & 2) ? -1.f : 1.f), (c & 1) ? ((c & 2) ? -1.f : 1.f) : 0.f);
      const unsigned sg = (c & 1) ? 0x80008000u : 0u;
      m0c[0] = pack8(c0v, c1v); m0s[0] = pack8(s0v, s1v);
      PIN(m0c[0]); PIN(m0s[0]);
      m0c[1] = m0c[0] ^ sg; m0s[1] = m0s[0] ^ sg;
#pragma unroll
      for (int i = 0; i < 2; ++i) {
        m0n[i] = neg8(m0s[i]);
        *LP(u32x4, tab(16 * wb + c) ^ (64u * i)) = m0c[i];
        *LP(u32x4, tab(16 * wb + c) ^ (64u * i + 128u)) = m0s[i];
      }
    }
#pragma unroll
    for (int tt = 0; tt < 2; ++tt) {
      trig4((float)(16 * wb + c) * (1.f / 2048.f), g4 + 16.f * tt, w1c[tt], w1s[tt]);
      trig4((float)(16 * tt + c) * (1.f / 2048.f), g4 + 16.f * wb, w2c[tt], w2s[tt]);
      PIN(w1c[tt]); PIN(w1s[tt]); PIN(w2c[tt]); PIN(w2s[tt]);
    }
    const int gr = 8 * ct + 2 * wb + (g & 1);
    unsigned gxp = gr * 256 + ((c ^ (gr & 15)) << 4);
    PIN(gxp);
    u32x4 gx[4];
    u64 epoch;
    asm volatile("s_load_dwordx2 %0, %1, 0x0\n\ts_waitcnt lgkmcnt(0)" : "=s"(epoch) : "s"(ws) : "memory");
    const u64 tag = TAG_OF(epoch);
    VMW(12); BAR();
    __builtin_amdgcn_s_setprio(3);
    gx[0] = ld16(gxp, 0);
    issue_x(2); issue_x(3);
#pragma unroll
    for (int i = 0; i < 2; ++i) {
      const unsigned ta = tab(2 * (16 * i + c)), tc = tab(32 * (wb >> 1) + 8 * (c >> 2) + 4 * i + (c & 3));
      mac[i] = *LP(u32x4, ta); mas[i] = *LP(u32x4, ta ^ 128u);
      mcc[i][0] = *LP(u32x4, tc); mcc[i][1] = *LP(u32x4, tc ^ 64u); mcs[i][0] = *LP(u32x4, tc ^ 128u); mcs[i][1] = *LP(u32x4, tc ^ 192u);
    }
    asm volatile("s_waitcnt vmcnt(8)" : "+v"(gw[0]), "+v"(gw[1]), "+v"(gw[2]), "+v"(gw[3]) :: "memory");
    BAR();
    gx[1] = ld16(gxp, 16384);
    issue_kv(1); issue_kv(2);
    VMW(12); BAR();
    gx[2] = ld16(gxp, 32768);
    issue_kv(3); issue_kv(4);
    VMW(16); BAR();
    gx[3] = ld16(gxp, 49152);
    issue_kv(5); issue_kv(6);
    VMW(16); BAR();
    issue_kv(7); issue_q(0);
    unsigned pfw;
    asm volatile("global_load_dword %0, %1, %2" : "=v"(pfw) : "v"((lane & 31) * 128), "s"(wo + idx * 16384 + wb * 4096) : "memory");
    VMW(17); BAR();
    issue_q(1); issue_q(2);
    VMW(13); BAR();
    issue_q(3);
#pragma unroll
    for (int i = 0; i < 4; ++i) {
      const int r = 4 * i + g, sl = c ^ ((r & 7) << 1);
      vx[i] = (unsigned)((16 * wb + r) * 1024 + (sl >> 3) * 512 + ((sl & 7) << 4) + 4096 - i * 1024);
    }
    if ((ct | mt) != 0) dma4(vx, wo - 4096 + cb * 128 + (i8 & 7) * 65536, dst0);
    ST(1);
    {
      float s = 0.f;
#pragma unroll
      for (int i = 0; i < 4; ++i)
#pragma unroll
        for (int j = 0; j < 4; ++j) s += bflo(gx[i][j]) * bflo(gw[i][j]) + bfhi(gx[i][j]) * bfhi(gw[i][j]);
      s = row_sum16(s);
      if ((lane & 47) == 0) ST32(__float_as_uint(__builtin_amdgcn_rcpf(1.f + __expf(-s))), WS_G + (64 * mt + gr) * 4);
    }
    asm volatile("s_waitcnt vmcnt(0)" : "+v"(pfw) :: "memory");
    SIG(E_QL);
    ev_wait<15>(E_G);
    dma4(vx, wo - 4096 + cb * 128 + ((1 + i8) & 7) * 65536, dst0 + 16384);
    dma4(vx, wo - 4096 + cb * 128 + ((2 + i8) & 7) * 65536, dst0 + 32768);
    const int k = 32 * ct + mt;
    unsigned o_x = (unsigned)(WS_XT + k * KS + bt * 16), o_w = (unsigned)(WS_WT + k * KS + bt * 32);
    unsigned l_xw = L_SCR + (bt >> 4) * 256 + ((c ^ (bt >> 4)) << 4);
    unsigned l_ca[4];
#pragma unroll
    for (int i = 0; i < 4; ++i) { l_ca[i] = L_SCR + c * 256 + (((4 * i + g) ^ c) << 4); PIN(l_ca[i]); }
    unsigned l_ew = L_SCR + 8192 + (wb >> 1) * 4096 + lane * 16 + (wb & 1) * 8, l_er = L_SCR + 8192 + (wb & 1) * 2048 + lane * 16;
    unsigned o_z = (unsigned)(WS_Z + k * KS + (2 * (16 * (wb & 1) + c) + (wb >> 1)) * 128 + g * 16);
    PIN(o_x); PIN(o_w); PIN(l_xw); PIN(l_ew); PIN(l_er); PIN(o_z);
    ST(2);
    if (wb == 0) { poll_all(ws, LN_FB + ct * 32 + (lane & 31), tag, 32); SIG(E_FB); }
    else ev_wait<1>(E_FB);
    __builtin_amdgcn_s_setprio(2);
    ST(3);
    {
      const u32x4 x0 = LD128(o_x), x1 = LD128(o_x + 4096);
      const u32x4 wv[2] = {LD128(o_w), LD128(o_w + 16)};
      u32x4 ar, ai, an;
      u32x4 ca[2][2][2];
      auto twiddle_dft32 = [&](const f32x4 (&yr)[2], const f32x4 (&yi)[2], f32x4 (&xr)[2], f32x4 (&xi)[2]) {
        const f32x4 r0 = yr[0] * w1c[0] + yi[0] * w1s[0], r1 = yr[1] * w1c[1] + yi[1] * w1s[1];
        const u32x4 br = pack8(r0, r1), bn = pack8(-r0, -r1);
        const u32x4 bi = pack8(yi[0] * w1c[0] - yr[0] * w1s[0], yi[1] * w1c[1] - yr[1] * w1s[1]);
#pragma unroll
        for (int ut = 0; ut < 2; ++ut) {
          xr[ut] = mfma32(mas[ut], bi, mfma32(mac[ut], br, zero));
          xi[ut] = mfma32(mas[ut], bn, mfma32(mac[ut], bi, zero));
        }
      };
      if (k != 0) {
        *LP(u32x4, l_xw) = x0; *(LAS u32x4*)(LP(char, l_xw) + 4096) = x1;
        SIG(E_X1);
        f32x4 fr[2] = {lo4(wv[0]), lo4(wv[1])}, fi[2] = {hi4(wv[0]), hi4(wv[1])};
        PIN(fr[0]); PIN(fr[1]); PIN(fi[0]); PIN(fi[1]);
        ST(4);
        ev_wait<15>(E_X1);
#pragma unroll
        for (int i = 0; i < 8; ++i) ca[i >> 2][(i >> 1) & 1][i & 1] = ld16(l_ca[i >> 1], (i & 1) * 4096);
        f32x4 yr[2] = {zero, zero}, yi[2] = {zero, zero}, xr[2], xi[2];
#pragma unroll
        for (int ks = 0; ks < 2; ++ks)
#pragma unroll
          for (int tt = 0; tt < 2; ++tt) { yr[tt] = mfma32(ca[0][ks][tt], m0c[ks], yr[tt]); yi[tt] = mfma32(ca[0][ks][tt], m0n[ks], yi[tt]); }
#pragma unroll
        for (int ks = 0; ks < 2; ++ks)
#pragma unroll
          for (int tt = 0; tt < 2; ++tt) { yr[tt] = mfma32(ca[1][ks][tt], m0s[ks], yr[tt]); yi[tt] = mfma32(ca[1][ks][tt], m0c[ks], yi[tt]); }
        twiddle_dft32(yr, yi, xr, xi);
        const f32x4 i0 = xr[0] * fi[0] + xi[0] * fr[0], i1 = xr[1] * fi[1] + xi[1] * fr[1];
        ar = pack8(xr[0] * fr[0] - xi[0] * fi[0], xr[1] * fr[1] - xi[1] * fi[1]);
        ai = pack8(i0, i1); an = pack8(-i0, -i1);
      } else {
        const u32x4 wn[2] = {LD128(o_w + 256 * KS), LD128(o_w + 256 * KS + 16)};
        unsigned mp[8];
#pragma unroll
        for (int e = 0; e < 8; ++e) { mp[e] = 4 * wt_pos((2048 - (16 * wb + c + 64 * (16 * (e >> 2) + 4 * g + (e & 3)))) & 2047); PIN(mp[e]); }
        *LP(u32x4, l_xw) = x0; *(LAS u32x4*)(LP(char, l_xw) + 4096) = x1;
        LAS u32x4* d = (LAS u32x4*)sm + bt * 2;
        d[0] = wv[0]; d[1] = wv[1]; d[512] = wn[0]; d[513] = wn[1];
        SIG(E_X1);
        ev_wait<15>(E_X1);
#pragma unroll
        for (int i = 0; i < 8; ++i) ca[i >> 2][(i >> 1) & 1][i & 1] = ld16(l_ca[i >> 1], (i & 1) * 4096);
        u32x4 m0[2], m1[2];
#pragma unroll
        for (int e = 0; e < 8; ++e) { m0[e >> 2][e & 3] = *LP(unsigned, mp[e]); m1[e >> 2][e & 3] = *(LAS unsigned*)(LP(char, mp[e]) + 8192); }
        f32x4 yr[2] = {zero, zero}, yi[2] = {zero, zero}, xr[2], xi[2], dr[2], di[2];
#pragma unroll
        for (int ks = 0; ks < 2; ++ks)
#pragma unroll
          for (int tt = 0; tt < 2; ++tt) { yr[tt] = mfma32(ca[0][ks][tt], m0c[ks], yr[tt]); yi[tt] = mfma32(ca[0][ks][tt], m0n[ks], yi[tt]); }
        twiddle_dft32(yr, yi, xr, xi);
#pragma unroll
        for (int ut = 0; ut < 2; ++ut) {
          const f32x4 fr = lo4(wv[ut]) + lo4(m0[ut]), fi = hi4(wv[ut]) - hi4(m0[ut]);
          dr[ut] = xr[ut] * fr - xi[ut] * fi; di[ut] = xr[ut] * fi + xi[ut] * fr;
          yr[ut] = zero; yi[ut] = zero;
        }
#pragma unroll
        for (int ks = 0; ks < 2; ++ks)
#pragma unroll
          for (int tt = 0; tt < 2; ++tt) { yr[tt] = mfma32(ca[1][ks][tt], m0s[ks], yr[tt]); yi[tt] = mfma32(ca[1][ks][tt], m0c[ks], yi[tt]); }
        twiddle_dft32(yr, yi, xr, xi);
#pragma unroll
        for (int ut = 0; ut < 2; ++ut) {
          const f32x4 fr = lo4(wn[ut]) + lo4(m1[ut]), fi = hi4(wn[ut]) - hi4(m1[ut]);
          dr[ut] += xr[ut] * fr - xi[ut] * fi; di[ut] += xr[ut] * fi + xi[ut] * fr;
        }
        ar = pack8(dr[0], dr[1]); ai = pack8(di[0], di[1]); an = pack8(-di[0], -di[1]);
      }
      ST(5);
#pragma unroll
      for (int tt = 0; tt < 2; ++tt) {
        const f32x4 vr = mfma32(an, mas[tt], mfma32(ar, mac[tt], zero));
        const f32x4 vi = mfma32(ar, mas[tt], mfma32(ai, mac[tt], zero));
        const f32x4 pr = vr * w2c[tt] - vi * w2s[tt], pi = vi * w2c[tt] + vr * w2s[tt];
        *(LAS u32x2*)(LP(char, l_ew) + tt * 2048) = u32x2{pk2(pr[0], pr[1]), pk2(pr[2], pr[3])};
        *(LAS u32x2*)(LP(char, l_ew) + tt * 2048 + 1024) = u32x2{pk2(pi[0], pi[1]), pk2(pi[2], pi[3])};
      }
      SIG(E_X2);
      ev_wait<15>(E_X2);
      f32x4 zr[2] = {zero, zero}, zi[2] = {zero, zero}, zs[2] = {zero, zero};
#pragma unroll
      for (int ks = 0; ks < 2; ++ks) {
        const u32x4 vr = ld16(l_er, ks * 4096), vi = ld16(l_er, ks * 4096 + 1024);
#pragma unroll
        for (int jt = 0; jt < 2; ++jt) {
          zr[jt] = mfma32(mcc[jt][ks], vr, zr[jt]); zi[jt] = mfma32(mcs[jt][ks], vr, zi[jt]);
          zs[jt] = mfma32(mcs[jt][ks], vi, zs[jt]); zi[jt] = mfma32(mcc[jt][ks], vi, zi[jt]);
        }
      }
      ST128(pack8(zr[0] - zs[0], zr[1] - zs[1]), o_z);
      ST128(pack8(zi[0], zi[1]), o_z + 64);
    }
    __builtin_amdgcn_s_setprio(0);
    ST(6);
    u32x4 ga[16];
    {
      const int d = 64 * cb + 16 * wb + c;
      const float df = (float)d * (1.f / 512.f);
      f32x4 ec, es, oc, os;
      trig4(-2.f * df, g4, ec, es);
      trig4(-2.f * df, g4 + 0.5f, oc, os);
      const float ra = __builtin_amdgcn_cosf(-32.f * df), rb_ = __builtin_amdgcn_sinf(-32.f * df);
#pragma unroll
      for (int ss = 0; ss < 4; ++ss) {
        ga[ss] = u32x4{pk2(ec[0], es[0]), pk2(ec[1], es[1]), pk2(ec[2], es[2]), pk2(ec[3], es[3])};
        ga[8 + ss] = u32x4{pk2(oc[0], os[0]), pk2(oc[1], os[1]), pk2(oc[2], os[2]), pk2(oc[3], os[3])};
        PIN(ga[ss]); PIN(ga[8 + ss]);
        if (ss < 3) { crot(ec, es, ra, rb_); crot(oc, os, ra, rb_); }
      }
      SB();
      DRAIN();
      SIG(E_ZD);
      if (wb == 0) {
        ev_wait<15>(E_ZD);
        if (lane < REP) __hip_atomic_fetch_add((GAS u64*)(ws + (size_t)(LN_CZ + lane * 16 + 2 * ct + (mt >> 4)) * 128), 1ull, RLX_AGENT);
      }
      ST(7);
      const unsigned sel = (c & 1) ? 0x01000302u : 0x03020100u;
      const unsigned sgn = (c & 3) == 0 ? 0u : (c & 3) == 1 ? 0x80000000u : (c & 3) == 2 ? 0x80008000u : 0x00008000u;
#pragma unroll
      for (int f = 0; f < 8; ++f) {
        const int src = (f >> 2) * 8 + (f & 3);
#pragma unroll
        for (int i = 0; i < 4; ++i) ga[src + 4][i] = __builtin_amdgcn_perm(0u, ga[src][i], sel) ^ sgn;
        PIN(ga[src + 4]);
      }
      if (g == 0) ga[0][0] = (c & 1) ? 0xBE803E80u : 0x3E803E80u;
      PIN(ga[0]);
    }
    unsigned o_zl = (unsigned)(WS_Z + (64 * wb + (lane >> 3)) * KS + rb * 128 + (lane & 7) * 16);
    unsigned l_zw, l_z0, l_z1;
    {
      const int kk = 64 * wb + (lane >> 3), R = (((kk & 1) * 128 + (kk >> 1)) << 1) + ((lane >> 2) & 1);
      l_zw = R * 64 + ((lane & 3) << 4);
      l_z0 = (8 * g + (c >> 2)) * 64 + 8 * (c & 3) + ((g & 1) << 5);
      l_z1 = l_z0 ^ 32u;
    }
    unsigned l_zw1 = l_zw ^ 32u;
    PIN(o_zl); PIN(l_zw); PIN(l_zw1); PIN(l_z0); PIN(l_z1);
    __builtin_amdgcn_s_setprio(BPRIO);
    ST(11);
    if (wb == 0) { TGATE(4); poll_all(ws, LN_CZ + (bid % REP) * 16 + c, tag + 16, 16); SIG(E_FZ); }
    else ev_wait<1>(E_FZ);
    ST(8);
    {
      u32x4 z[8];
#pragma unroll
      for (int i = 0; i < 8; ++i) z[i] = __builtin_amdgcn_raw_buffer_load_b128(rs, o_zl, i * 8 * KS, 16);
      ev_wait<15>(E_WR);
#pragma unroll
      for (int i = 0; i < 8; ++i) *(LAS u32x4*)(LP(char, (i & 1) ? l_zw1 : l_zw) + i * 512) = z[i];
    }
    SIG(E_ZI);
    ST(9);
    ev_wait<15>(E_ZI);
    f32x4 acc[2][2] = {{zero, zero}, {zero, zero}};
    {
      u32x4 zf[3][2][2];
      auto ldz = [&](int n) {
#pragma unroll
        for (int i = 0; i < 2; ++i) { zf[n % 3][i][0] = trfrag(l_z0, (2 * n + i) * 2048, 256); zf[n % 3][i][1] = trfrag(l_z1, (2 * n + i) * 2048, 256); }
      };
      ldz(0); ldz(1);
      SB();
#pragma unroll
      for (int n = 0; n < 8; ++n) {
        if (n < 6) ldz(n + 2);
#pragma unroll
        for (int i = 0; i < 2; ++i) {
          acc[n >> 2][0] = mfma32(ga[2 * n + i], zf[n % 3][i][0], acc[n >> 2][0]);
          acc[n >> 2][1] = mfma32(ga[2 * n + i], zf[n % 3][i][1], acc[n >> 2][1]);
        }
#pragma unroll
        for (int i = 0; i < 4; ++i) { __builtin_amdgcn_sched_group_barrier(0x100, 2, 0); __builtin_amdgcn_sched_group_barrier(0x8, 1, 0); }
        SB();
      }
    }
    ST(10);
    {
      unsigned o_out[2];
#pragma unroll
      for (int tt = 0; tt < 2; ++tt) o_out[tt] = (unsigned)(((rb >> 1) + 32 * (32 * (rb & 1) + 16 * tt + c)) * 1024 + (64 * cb + 16 * wb + 4 * g) * 2);
      const volatile LAS unsigned* fp = (const volatile LAS unsigned*)(size_t)(L_SYNC + E_A * 16 + wb * 4);
      while (!__builtin_amdgcn_readfirstlane(*fp)) __builtin_amdgcn_s_sleep(1);
      CFENCE();
      const unsigned l_a = 98304u + wb * 8192 + lane * 16;
      const f32x2 sc = *(const LAS f32x2*)(LP(char, l_a) + 4096);
#pragma unroll
      for (int tt = 0; tt < 2; ++tt)
#pragma unroll
        for (int hf = 0; hf < 2; ++hf) {
          const f32x4 av = __builtin_bit_cast(f32x4, ld16(l_a, (hf * 2 + tt) * 1024));
          const f32x4 r = av + (hf ? acc[0][tt] - acc[1][tt] : acc[0][tt] + acc[1][tt]) * sc[tt];
          *(u32x2*)(out + o_out[tt] + 512 * hf) = u32x2{pk2(r[0], r[1]), pk2(r[2], r[3])};
        }
    }
    ST(13);
  } else {
    __builtin_amdgcn_s_setprio(1);
    u32x4 wrv, wiv;
    unsigned nyq = 0, nyq2 = 0, pf = 0;
    {
      const unsigned o_w = (unsigned)(wt_row(64 * mt + (tid >> 2)) * 257 + 32 * ct + 8 * (tid & 3)) * 2u;
      wrv = *(const u32x4u*)(w_r + o_w);
      wiv = *(const u32x4u*)(w_i + o_w);
      if (ct == 0 && w == 0) {
        const unsigned o_ny = (unsigned)(wt_row(64 * mt + lane) * 257 + 256) * 2u;
        nyq = *(const unsigned short*)(w_r + o_ny); nyq2 = *(const unsigned short*)(w_i + o_ny);
      }
    }
    if (w == 2 && lane < CODE_LINES) pf = *(const GAS unsigned*)(pc0 + min((idx * CODE_LINES + lane) * 128, CODE_MAX));
    const u32x2 ev = *(const u32x2*)ws;
    const int rec = (2 * ct + (w >> 1)) * 64 + 2 * mt + (w & 1);
    unsigned o_kp = (unsigned)(WS_KVP + rec * RS + lane * 16), o_zp = (unsigned)(WS_ZP + rec * 64 + g * 16);
    ST128((u32x4{~0u, ~0u, ~0u, ~0u}), o_kp);
    if (c == 0) ST128((u32x4{~0u, ~0u, ~0u, ~0u}), o_zp);
    SB();
    const int rh = w & 1, kt = w >> 1;
    const int kf = 32 * ct + 16 * kt + c;
    u32x4 xf[4][4][2];
    unsigned bb[2][4];
    {
      const unsigned fl = (unsigned)((c >> 2) | ((g & 1) << 2));
      const unsigned bu = (unsigned)((8 * g + (c >> 2)) * 256 + 8 * (c & 3)) + (((unsigned)(2 * kt) ^ fl) << 5);
#pragma unroll
      for (int u = 0; u < 4; ++u) {
        bb[0][u] = bu ^ (unsigned)((u & 1) * 32 + (u >> 1) * 128);
        bb[1][u] = bb[0][u] + 65536u;
      }
    }
    u32x4 fb[3][4];
    auto ldf = [&](int n) {
#pragma unroll
      for (int u = 0; u < 4; ++u) {
        const int slot = (4 + (n >> 1)) & 7;
        fb[n % 3][u] = trfrag(bb[slot >> 2][u], (slot & 3) * 16384 + (n & 1) * 8192, 1024);
      }
    };
    u64 tag;
    {
      u32x4 fc[4], fs[4];
      {
        const float kk = (float)kf * (1.f / 512.f);
        const float a0 = __builtin_amdgcn_fractf(kk * (float)(8 * g + 128 * ((c0 + 1) & 3)));
#pragma unroll
        for (int ks = 0; ks < 4; ++ks) {
          const float ak = __builtin_fmaf(kk, (float)(((ks & 2) ^ (flip << 1)) * 32), a0);
#pragma unroll
          for (int i = 0; i < 4; ++i) {
            const float p0 = __builtin_fmaf(kk, (float)(32 * (ks & 1) + 2 * i), ak), p1 = __builtin_fmaf(kk, (float)(32 * (ks & 1) + 2 * i + 1), ak);
            fc[ks][i] = pk2(__builtin_amdgcn_cosf(p0), __builtin_amdgcn_cosf(p1));
            fs[ks][i] = kf == 0 ? 0xBF803F80u
                                : pk2(__builtin_amdgcn_sinf(-p0), __builtin_amdgcn_sinf(-p1));
          }
          PIN(fc[ks]); PIN(fs[ks]);
        }
      }
      float ra = (c & 1) ? 0.f : ((c & 2) ? -1.f : 1.f), rbi = (c & 1) ? ((c & 2) ? 1.f : -1.f) : 0.f;
      unsigned l_sc = L_SCR + ((tid & 3) * 512 + ((tid >> 2) ^ ((tid & 3) << 3))) * 4;
      unsigned l_rd = L_SCR + (tid >> 4) * 256 + (((tid & 15) ^ ((tid >> 7) << 1)) << 4);
      unsigned o_wt = (unsigned)(WS_WT + (32 * ct + (tid >> 4)) * KS + (64 * mt + 4 * (tid & 15)) * 4);
      unsigned o_xt = (unsigned)(WS_XT + kf * KS + mt * 256 + rh * 64 + g * 16);
      unsigned xa[4];
#pragma unroll
      for (int ks = 0; ks < 4; ++ks) { xa[ks] = rh * 8192 + c * 256 + (((4 * ks + g) ^ (flip << 3) ^ c) << 4); PIN(xa[ks]); }
#pragma unroll
      for (int u = 0; u < 4; ++u) { PIN(bb[0][u]); PIN(bb[1][u]); }
      PIN(l_sc); PIN(l_rd); PIN(o_wt); PIN(o_xt); PIN(ra); PIN(rbi); PIN(o_kp); PIN(o_zp);
      ST(1);
      f32x4 xr[4][2], xi[4][2];
      auto ldx = [&](int s) {
#pragma unroll
        for (int ks = 0; ks < 4; ++ks)
#pragma unroll
          for (int tt = 0; tt < 2; ++tt) xf[s][ks][tt] = ld16(xa[ks], s * 16384 + tt * 4096);
      };
      auto mmf = [&](int s) {
#pragma unroll
        for (int tt = 0; tt < 2; ++tt) { xr[s][tt] = zero; xi[s][tt] = zero; }
#pragma unroll
        for (int ks = 0; ks < 4; ++ks)
#pragma unroll
          for (int tt = 0; tt < 2; ++tt) {
            xr[s][tt] = mfma32(xf[s][ks][tt], fc[ks], xr[s][tt]);
            xi[s][tt] = mfma32(xf[s][ks][tt], fs[ks], xi[s][tt]);
          }
      };
      auto horner = [&](int s) {
#pragma unroll
        for (int tt = 0; tt < 2; ++tt) {
          xr[s][tt] = (xr[s][tt] + xr[s - 1][tt] * ra) - xi[s - 1][tt] * rbi;
          xi[s][tt] = (xi[s][tt] + xr[s - 1][tt] * rbi) + xi[s - 1][tt] * ra;
        }
      };
      auto mixv = [&]() {
#pragma unroll
        for (int i = 0; i < 16; ++i) { __builtin_amdgcn_sched_group_barrier(0x8, 1, 0); __builtin_amdgcn_sched_group_barrier(0x2 | 0x80 | 0x40, 2, 0); }
        SB();
      };
      BAR();
      ldx(0);
      {
        PIN(pf); PIN(nyq); PIN(nyq2);
        tag = TAG_OF(((u64)(unsigned)__builtin_amdgcn_readfirstlane(ev.y) << 32) | (unsigned)__builtin_amdgcn_readfirstlane(ev.x));
        LAS unsigned* sc = LP(unsigned, l_sc);
#pragma unroll
        for (int j = 0; j < 4; ++j) {
          sc[(2 * j) * 64] = __builtin_amdgcn_perm(wiv[j], wrv[j], 0x05040100u);
          sc[(2 * j + 1) * 64] = __builtin_amdgcn_perm(wiv[j], wrv[j], 0x07060302u);
        }
        if (ct == 0 && w == 0) ST32(nyq | (nyq2 << 16), WS_WT + 256 * KS + (64 * mt + lane) * 4);
        if (mt == 0 && w == 1 && lane < 4 * REP)
          put_flag(ws, LN_CZ + (lane >> 1) * 16 + 2 * ct + (lane & 1), tag);
      }
      BAR();
      const u32x4 wt0 = ld16(l_rd, 0), wt1 = ld16(l_rd ^ 64u, 4096);
      ldx(1); SB(); mmf(0);
      BAR();
      ldx(2); SB(); mmf(1);
      BAR();
      ldx(3); SB();
      mmf(2); horner(1);
      ST128(wt0, o_wt); ST128(wt1, o_wt + 16 * KS);
      mixv();
      mmf(3); horner(2);
      mixv();
      ldf(0); ldf(1);
      horner(3);
      ST128(pack8(xr[3][0], xr[3][1]), o_xt);
      ST128(pack8(xi[3][0], xi[3][1]), o_xt + 128);
    }
    ST(2);
    auto mix = [&]() {
#pragma unroll
      for (int i = 0; i < 8; ++i) { __builtin_amdgcn_sched_group_barrier(0x100, 1, 0); __builtin_amdgcn_sched_group_barrier(0x8, 1, 0); }
      SB();
    };
    {
      f32x4 acc[2][4];
#pragma unroll
      for (int t = 0; t < 2; ++t)
#pragma unroll
        for (int u = 0; u < 4; ++u) acc[t][u] = zero;
#pragma unroll
      for (int n = 0; n < 16; ++n) {
        if (n == FBN) DRAIN();
        if (n == 0 || n == 4 || n == 8) BAR();
        if (n == FBN) { if (tid == 0) put_flag(ws, LN_FB + ct * 32 + mt, tag); SB(); }
        if (n == 12) { SIG(E_G); SB(); }
        if (n < 14) ldf(n + 2);
#pragma unroll
        for (int u = 0; u < 4; ++u)
#pragma unroll
          for (int t = 0; t < 2; ++t) acc[t][u] = mfma32(xf[n >> 2][n & 3][t], fb[n % 3][u], acc[t][u]);
        mix();
      }
      ST(3);
      const u32x4 ones = {0x3f803f80u, 0x3f803f80u, 0x3f803f80u, 0x3f803f80u};
      f32x4 kv[2], zz[2];
#pragma unroll
      for (int n = 0; n < 2; ++n) {
        const u32x4 pk = pack8(phi4(acc[0][n]), phi4(acc[1][n]));
        kv[n] = mfma32(pk, pack8(acc[0][2 + n], acc[1][2 + n]), zero);
        zz[n] = mfma32(pk, ones, zero);
      }
      u32x4 kvo = pack8(kv[0], kv[1]), zo = pack8(zz[0], zz[1]);
#pragma unroll
      for (int e = 0; e < 4; ++e) {
        kvo[e] = min(kvo[e], 0xfffffffeu);
        zo[e] = min(zo[e], 0xfffffffeu);
      }
      ST128(kvo, o_kp);
      if (c == 0) ST128(zo, o_zp);
    }
    ST(4);
    {
      const int hp = 2 * ct + (mt >> 4), sub = mt & 15;
      u32x4 pv[4];
      unsigned pz;
      auto ldp = [&]() {
#pragma unroll
        for (int i = 0; i < 4; ++i)
          pv[i] = __builtin_amdgcn_raw_buffer_load_b128(rs, WS_KVP + (hp * 64 + (lane >> 2)) * RS + sub * 64 + (lane & 3) * 16, i * 16 * RS, 16);
        pz = LD32(WS_ZP + (hp * 64 + lane) * 64 + sub * 4);
      };
      f32x4 acc[2][2] = {{zero, zero}, {zero, zero}};
      ev_wait<15>(E_QL);
      SB();
      ldf(0); ldf(1);
      SB();
#pragma unroll
      for (int n = 0; n < 8; ++n) {
        if (n < 6) ldf(n + 2);
        if (n == 6) { SIG(E_QR); SB(); }
        if (n == LDPN) {
          if (w == 0) { ev_wait<1>(E_FB); ldp(); }
          SB();
        }
#pragma unroll
        for (int u = 0; u < 4; ++u)
#pragma unroll
          for (int t = 0; t < 2; ++t) acc[u & 1][t] = mfma32(fb[n % 3][u], xf[n >> 1][2 * (u >> 1) + (n & 1)][t], acc[u & 1][t]);
        mix();
      }
      auto st_pq = [&]() {
#pragma unroll
        for (int n = 0; n < 2; ++n)
          ST128(pack8(phi4(acc[n][0]), phi4(acc[n][1])), WS_PQ + ((2 * mt + rh) * 32 + 4 * ct + 2 * kt + n) * 1024 + lane * 16);
        SB();
      };
      st_pq();
      ST(5);
      if (w == 0) {
        for (unsigned spins = 0; spins < SPIN_CAP; ++spins) {
          unsigned m = pz;
#pragma unroll
          for (int i = 0; i < 4; ++i) m = max(max(m, max(pv[i][0], pv[i][1])), max(pv[i][2], pv[i][3]));
          if (__all(m != ~0u)) break;
          CFENCE();
          ldp();
        }
        ST(6);
        const unsigned one = (c & 1) ? 0x3f800000u : 0x00003f80u;
        const u32x4 sel = {(c >> 1) == 0 ? one : 0u, (c >> 1) == 1 ? one : 0u, (c >> 1) == 2 ? one : 0u, (c >> 1) == 3 ? one : 0u};
        f32x4 s = zero;
#pragma unroll
        for (int i = 0; i < 4; ++i) s = mfma32(sel, pv[i], s);
        const f32x4 zq = mfma32(sel, u32x4{pz, 0u, 0u, 0u}, zero);
#pragma unroll
        for (int e = 0; e < 4; ++e) s[e] = dpp_add<0x128>(dpp_add<0x124>(s[e]));
        const float z0 = row_sum16(zq[0]), z1 = row_sum16(zq[1]);
        if ((lane & 44) == 0) ST64((u32x2{pk2(s[0], s[1]), pk2(s[2], s[3])}), WS_KVF + hp * 1024 + (sub * 4 + c) * 16 + g * 8);
        if (lane == 0) ST32(pk2(z0, z1), WS_ZF + hp * 64 + sub * 4);
        SB();
      }
      if (w == 0) {
        VMW(2);
        ev_wait<14>(E_PQ);
        if (lane == 0) put_flag(ws, LN_FQ + mt * 8 + ct, tag);
        VMW(1);
        if (lane < REP) __hip_atomic_fetch_add((GAS u64*)(ws + (size_t)(LN_CK + lane * 16 + 2 * ct + (mt >> 4)) * 128), 1ull, RLX_AGENT);
        SIG(E_KA);
      } else {
        unsigned voff[4];
#pragma unroll
        for (int i = 0; i < 4; ++i) {
          const int r = 4 * i + g, sl = c ^ ((r & 7) << 1);
          voff[i] = (unsigned)(r * 1024 + (sl >> 3) * 512 + ((sl & 7) << 4) + 4096 - i * 1024);
        }
        ev_wait<15>(E_QR);
#pragma unroll 1
        for (int gj = 11 + w; gj < ((ct | mt) == 0 ? 36 : 32); gj += 3) {
          const int gi = gj & 31;
          if (gj >= 32 && (ct | mt) == 0) ev_wait<15>(E_X2);
          dma4(voff, wo - 4096 + cb * 128 + ((gi + 4 * i8) & 31) * 16384, gi * 4096);
          VMW(WO_THR);
          if (gj < 15) { VMW(4); SIG(E_PQ); }
        }
        DRAIN();
        SIG(E_WL);
      }
    }
    __builtin_amdgcn_s_setprio(APRIO);
    ST(7);
    u32x4 wa[16][2];
    {
      const unsigned f7 = (unsigned)((4 * g + (c >> 2)) & 7);
      unsigned pw[2][2];
#pragma unroll
      for (int hf = 0; hf < 2; ++hf) {
        pw[hf][0] = (4 * g + (c >> 2)) * 256 + 8 * (c & 3) + ((((unsigned)w + 4u * hf) ^ f7) << 5);
        pw[hf][1] = pw[hf][0] + 65536u;
        PIN(pw[hf][0]); PIN(pw[hf][1]);
      }
      ev_wait<15>(E_X1);
      ev_wait<14>(E_WL);
#pragma unroll
      for (int st = 0; st < 16; ++st)
#pragma unroll
        for (int hf = 0; hf < 2; ++hf) wa[st][hf] = trfrag(pw[hf][st >> 3], (st & 7) * 8192, 4096);
      SIG(E_WR);
    }
    SB();
    ST(8);
    if (w == 1 || w == 2) ev_wait<1>(E_KA);
    if (w == 1) { TGATE(2); poll_all(ws, LN_FQ + (rb >> 1) * 8 + (lane & 7), tag, 8); SIG(E_FQ); }
    else if (w == 2) {
      TGATE(8);
      poll_all(ws, LN_CK + (bid % REP) * 16 + c, tag + 16, 16);
      SIG(E_FK);
      if (bid == 0 && lane == 0) *(u32x2*)ws = u32x2{ev.x + 1u, ev.y};
    }
    ev_wait<2>(E_FQ);
    u32x4 pq[8];
#pragma unroll
    for (int i = 0; i < 8; ++i) pq[i] = LD128(WS_PQ + (rb * 32 + 8 * w + i) * 1024 + lane * 16);
    float gate[2];
#pragma unroll
    for (int tt = 0; tt < 2; ++tt) gate[tt] = __uint_as_float(LD32(WS_G + (32 * rb + 16 * tt + c) * 4));
    unsigned l_o[4];
#pragma unroll
    for (int hp = 0; hp < 4; ++hp) { l_o[hp] = 65536u + ((4 * w + hp - 2 * i8) & 15) * 2048 + lane * 16; PIN(l_o[hp]); }
    unsigned o_kf = (unsigned)(WS_KVF + 4 * w * 1024 + lane * 16), o_zf = (unsigned)(WS_ZF + 4 * w * 64 + g * 16), l_or = 65536u + lane * 16;
    PIN(o_kf); PIN(o_zf); PIN(l_or);
    ev_wait<15>(E_WR);
    SB();
    ST(9);
    ev_wait<4>(E_FK);
    ST(10);
    {
      u32x4 kf4[4], zf4[4];
#pragma unroll
      for (int i = 0; i < 4; ++i) { kf4[i] = LD128(o_kf + i * 1024); zf4[i] = LD128(o_zf + i * 64); }
      f32x4 num[2][2][2], den[2][2][2];
      auto mo = [&](int hp) {
#pragma unroll
        for (int tt = 0; tt < 2; ++tt)
#pragma unroll
          for (int n = 0; n < 2; ++n) {
            const u32x4 pp = pq[2 * hp + n];
            const u32x2 b = tt ? u32x2{pp.z, pp.w} : u32x2{pp.x, pp.y};
            const u32x2 ka = n ? u32x2{kf4[hp].z, kf4[hp].w} : u32x2{kf4[hp].x, kf4[hp].y};
            const u32x2 za = n ? u32x2{zf4[hp].z, zf4[hp].w} : u32x2{zf4[hp].x, zf4[hp].y};
            den[hp & 1][tt][n] = mfma16(za, b, f32x4{1e-6f, 1e-6f, 1e-6f, 1e-6f});
            num[hp & 1][tt][n] = mfma16(ka, b, zero);
          }
      };
      mo(0);
      SB();
#pragma unroll
      for (int hp = 0; hp < 4; ++hp) {
        if (hp < 3) mo(hp + 1);
        SB();
#pragma unroll
        for (int tt = 0; tt < 2; ++tt)
          *(LAS u32x4*)(LP(char, l_o[hp]) + tt * 1024) = pack8(num[hp & 1][tt][0] * __builtin_amdgcn_rcpf(den[hp & 1][tt][0][0]),
                                                               num[hp & 1][tt][1] * __builtin_amdgcn_rcpf(den[hp & 1][tt][1][0]));
        SB();
      }
    }
    SIG(E_O);
    ST(11);
    ev_wait<15>(E_O);
    f32x4 acc[2][2] = {{zero, zero}, {zero, zero}};
    {
#pragma unroll
      for (int st = 0; st < 16; ++st)
#pragma unroll
        for (int tt = 0; tt < 2; ++tt) {
          const u32x4 ob = ld16(l_or, (st * 2 + tt) * 1024);
          acc[0][tt] = mfma32(wa[st][0], ob, acc[0][tt]);
          acc[1][tt] = mfma32(wa[st][1], ob, acc[1][tt]);
        }
      __builtin_amdgcn_sched_group_barrier(0x100, 8, 0);
#pragma unroll
      for (int i = 0; i < 24; ++i) { __builtin_amdgcn_sched_group_barrier(0x8, 2, 0); __builtin_amdgcn_sched_group_barrier(0x100, 1, 0); }
      __builtin_amdgcn_sched_group_barrier(0x8, 16, 0);
    }
#pragma unroll
    for (int i = 0; i < 4; ++i) *(LAS f32x4*)(LP(char, l_or) + 32768 + w * 8192 + i * 1024) = acc[i >> 1][i & 1] * gate[i & 1];
    *(LAS f32x2*)(LP(char, l_or) + 32768 + w * 8192 + 4096) = f32x2{(1.f - gate[0]) * (2.f / (2048.f * 512.f)), (1.f - gate[1]) * (2.f / (2048.f * 512.f))};
    SIG(E_A);
    ST(12);
  }
}

extern "C" void kernel_launch(void* const* d_in, const int* in_sizes, int n_in, void* d_out, int out_size,
                              void* d_ws, size_t ws_size, hipStream_t stream) {
  (void)hipFuncSetAttribute((const void*)fused_kernel, hipFuncAttributeMaxDynamicSharedMemorySize, LDS_BYTES);
  fused_kernel<<<256, 512, LDS_BYTES, stream>>>((const char*)d_in[0], (const char*)d_in[1], (const char*)d_in[2],
                                                (const char*)d_in[4], (const char*)d_in[5], (const char*)d_in[6],
                                                (char*)d_ws, (char*)d_out);
}
```

```cpp
#define POLL_SLEEP 1
#include <hip/hip_runtime.h>
#include <hip/hip_bf16.h>
#include <stdint.h>

typedef short s16x4 __attribute__((ext_vector_type(4)));
typedef short s16x8 __attribute__((ext_vector_type(8)));
typedef float f32x2 __attribute__((ext_vector_type(2)));
typedef float f32x4 __attribute__((ext_vector_type(4)));
typedef unsigned u32x2 __attribute__((ext_vector_type(2)));
typedef unsigned u32x4 __attribute__((ext_vector_type(4)));
typedef unsigned u32x4u __attribute__((ext_vector_type(4), aligned(2)));
typedef __bf16 bf16x2 __attribute__((ext_vector_type(2)));
typedef unsigned long long u64;
#define DI __device__ __forceinline__
#define LAS __attribute__((address_space(3)))
#define GAS __attribute__((address_space(1)))
#define RLX_AGENT __ATOMIC_RELAXED, __HIP_MEMORY_SCOPE_AGENT
#define CFENCE() asm volatile("" ::: "memory")
#define PIN(v) asm volatile("" : "+v"(v))
#define DRAIN() asm volatile("s_waitcnt vmcnt(0)" ::: "memory")
#define LGKM0() __builtin_amdgcn_s_waitcnt(0xC07F)
#define SB() __builtin_amdgcn_sched_barrier(0)
#define VMW_(n) asm volatile("s_waitcnt vmcnt(" #n ")" ::: "memory")
#define VMW(n) VMW_(n)
#define LP(T, a) ((LAS T*)(size_t)(a))
#define WS_DBG (64u << 20)
#define BAR() do { CFENCE(); SB(); LGKM0(); __builtin_amdgcn_s_barrier(); SB(); CFENCE(); } while (0)
#define ST(i)

DI unsigned pk2(float a, float b) {
  f32x2 v = {a, b};
  return __builtin_bit_cast(unsigned, __builtin_convertvector(v, bf16x2));
}
DI float bflo(unsigned u) { return __uint_as_float(u << 16); }
DI float bfhi(unsigned u) { return __uint_as_float(u & 0xffff0000u); }
DI f32x4 lo4(u32x4 u) { return f32x4{bflo(u.x), bflo(u.y), bflo(u.z), bflo(u.w)}; }
DI f32x4 hi4(u32x4 u) { return f32x4{bfhi(u.x), bfhi(u.y), bfhi(u.z), bfhi(u.w)}; }
DI f32x4 mfma32(u32x4 a, u32x4 b, f32x4 c) {
  return __builtin_amdgcn_mfma_f32_16x16x32_bf16(__builtin_bit_cast(s16x8, a), __builtin_bit_cast(s16x8, b), c, 0, 0, 0);
}
DI f32x4 mfma16(u32x2 a, u32x2 b, f32x4 c) {
  return __builtin_amdgcn_mfma_f32_16x16x16bf16_1k(__builtin_bit_cast(s16x4, a), __builtin_bit_cast(s16x4, b), c, 0, 0, 0);
}
DI u32x4 pack8(f32x4 a, f32x4 b) { return u32x4{pk2(a[0], a[1]), pk2(a[2], a[3]), pk2(b[0], b[1]), pk2(b[2], b[3])}; }
DI u32x4 neg8(u32x4 a) {
  PIN(a);
  a ^= 0x80008000u;
  PIN(a);
  return a;
}
DI f32x4 phi4(f32x4 t) {
  const f32x4 m = t * 1.4426950408889634f, r = t + 1e-6f;
  f32x4 e, q;
#pragma unroll
  for (int i = 0; i < 4; ++i) { e[i] = __builtin_amdgcn_exp2f(fminf(m[i], 0.f)); q[i] = fmaxf(r[i], 1e-6f); }
  return e + q;
}
DI void crot(f32x4& cr, f32x4& ci, float a, float b) {
  const f32x4 nr = cr * a - ci * b, ni = cr * b + ci * a;
  cr = nr; ci = ni;
}
DI u32x4 trfrag(unsigned a, int off, int hi_off) {
  s16x4 l = __builtin_amdgcn_ds_read_tr16_b64_v4i16((LAS s16x4*)(LP(char, a) + off));
  s16x4 h = __builtin_amdgcn_ds_read_tr16_b64_v4i16((LAS s16x4*)(LP(char, a) + off + hi_off));
  return __builtin_bit_cast(u32x4, __builtin_shufflevector(l, h, 0, 1, 2, 3, 4, 5, 6, 7));
}
DI u32x4 ld16(unsigned a, int off) { return *(const LAS u32x4*)(LP(char, a) + off); }
template <int CTRL> DI float dpp_add(float v) {
  return v + __int_as_float(__builtin_amdgcn_update_dpp(0, __float_as_int(v), CTRL, 0xf, 0xf, false));
}
DI float row_sum16(float v) { return dpp_add<0x128>(dpp_add<0x124>(dpp_add<0x4E>(dpp_add<0xB1>(v)))); }

#define KS 8192
#define RS 1024
#define REPV 8
constexpr int REP = REPV;
constexpr int LN_FB = 8, LN_FQ = LN_FB + 256, LN_CZ = LN_FQ + 256, LN_CK = LN_CZ + 16 * REP;
constexpr int WS_XT = 160 * 1024;
constexpr int WS_WT = WS_XT + 256 * KS;
constexpr int WS_KVP = WS_WT + 257 * KS;
constexpr int WS_ZP = WS_KVP + 16 * 64 * RS;
constexpr int WS_KVF = WS_ZP + 16 * 64 * 64;
constexpr int WS_ZF = WS_KVF + 16 * 1024;
constexpr int WS_G = WS_ZF + 2048;
constexpr int WS_PQ = WS_G + 8192;
constexpr int WS_Z = WS_PQ + 2048 * 1024;
constexpr int WS_WOF = WS_Z;
constexpr int WS_END = WS_Z + 256 * KS;
constexpr int L_SCR = 131072, L_ST = L_SCR + 30720, L_SYNC = L_SCR + 32768 - 512, LDS_BYTES = 163840;
enum { E_QL, E_FB, E_X1, E_X2, E_ZD, E_FZ, E_ZI, E_QR, E_PQ, E_WL, E_WR, E_KA, E_FQ, E_FK, E_O, E_G, E_A, E_S1, E_XD };
constexpr unsigned SPIN_CAP = 1u << 16;
#define WO_THR 4
#define FBN 8
#define BPRIO 0
#define APRIO 1
#define POLLK 1
#define LDPN 4
#define CODE_MAX 31232
#define CODE_LINES 8
#define TAG_OF(e) ((e) * 0x9E3779B97F4A7C15ull + 0x632BE59BD9B4E019ull)

#define SIG(ev) do { CFENCE(); *(volatile LAS unsigned*)(LP(char, sigp) + (ev) * 16) = 1u; CFENCE(); } while (0)
template <unsigned MASK> DI void ev_wait(int ev) {
  const volatile LAS u32x4* p = (const volatile LAS u32x4*)(size_t)(L_SYNC + ev * 16);
  for (;;) {
    const u32x4 v = *p;
    const unsigned a = ((MASK & 1) ? v.x : 1u) & ((MASK & 2) ? v.y : 1u) & ((MASK & 4) ? v.z : 1u) & ((MASK & 8) ? v.w : 1u);
    if (__builtin_amdgcn_readfirstlane(a)) break;
    __builtin_amdgcn_s_sleep(1);
  }
  CFENCE();
}
DI void poll_all_(const char* ws, unsigned line, u64 want) {
  const GAS u64* p = (const GAS u64*)(ws + (size_t)line * 128);
  for (unsigned spins = 0; spins < SPIN_CAP; ++spins) {
    u64 v; const u64 z = 0;
    asm volatile("global_atomic_add_x2 %0, %1, %2, off sc0 sc1\n\ts_waitcnt vmcnt(0)" : "=&v"(v) : "v"(p), "v"(z) : "memory");
    if (__all(v == want)) break;
    __builtin_amdgcn_s_sleep(POLL_SLEEP);
  }
  __builtin_amdgcn_fence(__ATOMIC_ACQUIRE, "wavefront");
  CFENCE();
}
#define poll_all(ws, line, want, n) do { if (lane < (n)) poll_all_(ws, line, want); CFENCE(); } while (0)
#define TGATE(kind)
DI void put_flag(char* ws, unsigned line, u64 v) { __hip_atomic_store((GAS u64*)(ws + (size_t)line * 128), v, RLX_AGENT); }

DI void dma4(const unsigned (&voff)[4], const char* sb, unsigned dst) {
  unsigned keep;
  asm volatile("s_mov_b32 %0, m0\n\ts_mov_b32 m0, %6\n\ts_nop 0\n\t"
               "global_load_lds_dwordx4 %1, %5\n\t"
               "global_load_lds_dwordx4 %2, %5 offset:1024\n\t"
               "global_load_lds_dwordx4 %3, %5 offset:2048\n\t"
               "global_load_lds_dwordx4 %4, %5 offset:3072\n\t"
               "s_mov_b32 m0, %0"
               : "=&s"(keep) : "v"(voff[0]), "v"(voff[1]), "v"(voff[2]), "v"(voff[3]), "s"(sb), "s"(dst) : "memory");
}
DI int wt_pos(int u) {
  const int k0 = u & 63, u1 = u >> 6;
  return (((k0 >> 4) * 64 + ((u1 >> 2) & 3) * 16 + (k0 & 15)) << 3) + (u1 >> 4) * 4 + (u1 & 3);
}
DI int wt_row(int pos) {
  const int bt = pos >> 3;
  return 16 * (bt >> 6) + (bt & 15) + 64 * (16 * ((pos >> 2) & 1) + 4 * ((bt >> 4) & 3) + (pos & 3));
}
DI void trig4(float f, float i0, f32x4& co, f32x4& si) {
#pragma unroll
  for (int e = 0; e < 4; ++e) {
    const float a = f * (i0 + (float)e);
    co[e] = __builtin_amdgcn_cosf(a); si[e] = __builtin_amdgcn_sinf(a);
  }
}
DI void trig_frag(float f, float i0, u32x4& co, u32x4& si) {
  f32x4 c0, s0, c1, s1;
  trig4(f, i0, c0, s0); trig4(f, i0 + 16.f, c1, s1);
  co = pack8(c0, c1); si = pack8(s0, s1);
  PIN(co); PIN(si);
}

#define LD128(off) __builtin_amdgcn_raw_buffer_load_b128(rs, (off), 0, 16)
#define LD32(off) __builtin_amdgcn_raw_buffer_load_b32(rs, (off), 0, 16)
#define ST128(v, off) __builtin_amdgcn_raw_buffer_store_b128((v), rs, (off), 0, 16)
#define ST64(v, off) __builtin_amdgcn_raw_buffer_store_b64((v), rs, (off), 0, 16)
#define ST32(v, off) __builtin_amdgcn_raw_buffer_store_b32((v), rs, (off), 0, 16)

__global__ __launch_bounds__(512, 2) void fused_kernel(const char* __restrict__ x, const char* __restrict__ wqkv,
                                                       const char* __restrict__ wo, const char* __restrict__ w_r,
                                                       const char* __restrict__ w_i, const char* __restrict__ gate_w,
                                                       char* __restrict__ ws, char* __restrict__ out) {
  extern __shared__ __attribute__((aligned(16))) char smem[];
  LAS char* sm = (LAS char*)smem;
  asm volatile("" :: "s"(x), "s"(wqkv), "s"(wo), "s"(w_r), "s"(w_i), "s"(gate_w), "s"(ws), "s"(out), "s"(sm));
  u64 pc0;
  asm volatile("s_getpc_b64 %0" : "=s"(pc0));
  pc0 &= ~127ull;
  const int tid = threadIdx.x, lane = tid & 63;
  const int w = __builtin_amdgcn_readfirstlane(tid >> 6);
  const int bid = blockIdx.x, xcd = bid & 7, idx = bid >> 3;
  const int mt = (xcd & 3) * 8 + (idx & 7), ct = (xcd >> 2) * 4 + (idx >> 3);
  const int rb = xcd * 8 + (idx & 7), cb = idx >> 3;
  const int c = lane & 15, g = lane >> 4;
  const __amdgpu_buffer_rsrc_t rs = __builtin_amdgcn_make_buffer_rsrc(ws, 0, WS_END, 0x00020000);
  const f32x4 zero = {0, 0, 0, 0};
  unsigned sigp = L_SYNC + (w & 3) * 4;
  PIN(sigp);
  ST(0);
  const int i8 = idx & 7, c0 = (idx + (idx >> 3)) & 3, flip = (idx >> 2) & 1;
  if (w >= 4) {
    const int wb = w - 4, bt = tid - 256;
    unsigned vx[4], vk[4], vq[4];
    const unsigned dst0 = wb * 4096;
#pragma unroll
    for (int i = 0; i < 4; ++i) {
      const int r = 16 * wb + 4 * i + g;
      vx[i] = (unsigned)((mt + 32 * r) * 1024 + ((c ^ (r & 15)) << 4) + 4096 - i * 1024);
    }
    auto issue_x = [&](int i) { dma4(vx, x - 4096 + ((c0 - i) & 3) * 256, dst0 + i * 16384); };
    issue_x(0);
#pragma unroll
    for (int i = 0; i < 4; ++i) {
      const int r = 16 * wb + 4 * i + g, sl = c ^ ((g | ((i >> 1) << 2)) << 1);
      vk[i] = (unsigned)(r * 3072 + (sl >> 3) * 1024 + ((sl & 7) << 4) + 4096 - i * 1024);
    }
    auto issue_kv = [&](int j) {
      const int blk = 2 * ((c0 - (j >> 1)) & 3) + ((j & 1) ^ flip);
      dma4(vk, wqkv - 4096 + 1024 + ct * 128 + (unsigned)blk * (64 * 3072), dst0 + ((j + 4) & 7) * 16384);
    };
    u32x4 gw[4];
#pragma unroll
    for (int i = 0; i < 4; ++i)
      asm volatile("global_load_dwordx4 %0, %1, %2" : "=v"(gw[i]) : "v"(c * 16), "s"(gate_w + ((c0 - i) & 3) * 256) : "memory");
    issue_kv(0);
    issue_x(1);
    if (wb == 0) *(LAS u32x2*)(sm + L_SYNC + lane * 8) = u32x2{0u, 0u};
#pragma unroll
    for (int i = 0; i < 4; ++i) {
      const int r = 16 * wb + 4 * i + g, sl = c ^ ((g | ((i >> 1) << 2)) << 1);
      vq[i] = (unsigned)(r * 3072 + ((sl >> 3) ^ flip) * (64 * 3072) + ((sl & 7) << 4) + 4096 - i * 1024);
      PIN(vq[i]);
    }
    auto issue_q = [&](int j) { dma4(vq, wqkv - 4096 + ct * 128 + (unsigned)((c0 - j) & 3) * (128 * 3072), dst0 + (j + 4) * 16384); };
    const float g4 = (float)(4 * g);
    u32x4 m0c[2], m0s[2], m0n[2], mac[2], mas[2], mcc[2][2], mcs[2][2];
    f32x4 w1c[2], w1s[2], w2c[2], w2s[2];
    auto tab = [&](int row) { return (unsigned)(7 * 16384 + row * 256 + ((g ^ (row & 15)) << 4)); };
    {
      f32x4 c0v, s0v;
      trig4((float)(16 * wb + c) * (1.f / 64.f), g4, c0v, s0v);
      f32x4 c1v = c0v, s1v = s0v;
      crot(c1v, s1v, (c & 1) ? 0.f : ((c & 2) ? -1.f : 1.f), (c & 1) ? ((c & 2) ? -1.f : 1.f) : 0.f);
      const unsigned sg = (c & 1) ? 0x80008000u : 0u;
      m0c[0] = pack8(c0v, c1v); m0s[0] = pack8(s0v, s1v);
      PIN(m0c[0]); PIN(m0s[0]);
      m0c[1] = m0c[0] ^ sg; m0s[1] = m0s[0] ^ sg;
#pragma unroll
      for (int i = 0; i < 2; ++i) {
        m0n[i] = neg8(m0s[i]);
        *LP(u32x4, tab(16 * wb + c) ^ (64u * i)) = m0c[i];
        *LP(u32x4, tab(16 * wb + c) ^ (64u * i + 128u)) = m0s[i];
      }
    }
#pragma unroll
    for (int tt = 0; tt < 2; ++tt) {
      trig4((float)(16 * wb + c) * (1.f / 2048.f), g4 + 16.f * tt, w1c[tt], w1s[tt]);
      trig4((float)(16 * tt + c) * (1.f / 2048.f), g4 + 16.f * wb, w2c[tt], w2s[tt]);
      PIN(w1c[tt]); PIN(w1s[tt]); PIN(w2c[tt]); PIN(w2s[tt]);
    }
    u32x4 ga[16];
    auto make_g = [&]() {
      const int d = 64 * cb + 16 * wb + c;
      const float df = (float)d * (1.f / 512.f);
      f32x4 ec, es, oc, os;
      trig4(-2.f * df, g4, ec, es);
      trig4(-2.f * df, g4 + 0.5f, oc, os);
      const float ra = __builtin_amdgcn_cosf(-32.f * df), rb_ = __builtin_amdgcn_sinf(-32.f * df);
#pragma unroll
      for (int ss = 0; ss < 4; ++ss) {
        ga[ss] = u32x4{pk2(ec[0], es[0]), pk2(ec[1], es[1]), pk2(ec[2], es[2]), pk2(ec[3], es[3])};
        ga[8 + ss] = u32x4{pk2(oc[0], os[0]), pk2(oc[1], os[1]), pk2(oc[2], os[2]), pk2(oc[3], os[3])};
        PIN(ga[ss]); PIN(ga[8 + ss]);
        if (ss < 3) { crot(ec, es, ra, rb_); crot(oc, os, ra, rb_); }
      }
    };
    const int gr = 8 * ct + 2 * wb + (g & 1);
    unsigned gxp = gr * 256 + ((c ^ (gr & 15)) << 4);
    PIN(gxp);
    u32x4 gx[4];
    u64 epoch;
    asm volatile("s_load_dwordx2 %0, %1, 0x0\n\ts_waitcnt lgkmcnt(0)" : "=s"(epoch) : "s"(ws) : "memory");
    const u64 tag = TAG_OF(epoch);
    VMW(12); BAR();
    __builtin_amdgcn_s_setprio(3);
    gx[0] = ld16(gxp, 0);
    issue_x(2); issue_x(3);
#pragma unroll
    for (int i = 0; i < 2; ++i) {
      const unsigned ta = tab(2 * (16 * i + c)), tc = tab(32 * (wb >> 1) + 8 * (c >> 2) + 4 * i + (c & 3));
      mac[i] = *LP(u32x4, ta); mas[i] = *LP(u32x4, ta ^ 128u);
      mcc[i][0] = *LP(u32x4, tc); mcc[i][1] = *LP(u32x4, tc ^ 64u); mcs[i][0] = *LP(u32x4, tc ^ 128u); mcs[i][1] = *LP(u32x4, tc ^ 192u);
    }
    asm volatile("s_waitcnt vmcnt(8)" : "+v"(gw[0]), "+v"(gw[1]), "+v"(gw[2]), "+v"(gw[3]) :: "memory");
    BAR();
    gx[1] = ld16(gxp, 16384);
    issue_kv(1); issue_kv(2);
    VMW(12); BAR();
    gx[2] = ld16(gxp, 32768);
    issue_kv(3); issue_kv(4);
    VMW(16); BAR();
    gx[3] = ld16(gxp, 49152);
    issue_kv(5); issue_kv(6);
    VMW(16); BAR();
    issue_kv(7); issue_q(0);
    unsigned pfw;
    asm volatile("global_load_dword %0, %1, %2" : "=v"(pfw) : "v"((lane & 31) * 128), "s"(wo + idx * 16384 + wb * 4096) : "memory");
    VMW(17); BAR();
    issue_q(1); issue_q(2);
    VMW(13); BAR();
    issue_q(3);
#pragma unroll
    for (int i = 0; i < 4; ++i) {
      const int r = 4 * i + g, sl = c ^ ((r & 7) << 1);
      vx[i] = (unsigned)((16 * wb + r) * 1024 + (sl >> 3) * 512 + ((sl & 7) << 4) + 4096 - i * 1024);
    }
    if ((ct | mt) != 0) dma4(vx, wo - 4096 + cb * 128 + (i8 & 7) * 65536, dst0);
    ST(1);
    {
      float s = 0.f;
#pragma unroll
      for (int i = 0; i < 4; ++i)
#pragma unroll
        for (int j = 0; j < 4; ++j) s += bflo(gx[i][j]) * bflo(gw[i][j]) + bfhi(gx[i][j]) * bfhi(gw[i][j]);
      s = row_sum16(s);
      if ((lane & 47) == 0) ST32(__float_as_uint(__builtin_amdgcn_rcpf(1.f + __expf(-s))), WS_G + (64 * mt + gr) * 4);
    }
    asm volatile("s_waitcnt vmcnt(0)" : "+v"(pfw) :: "memory");
    SIG(E_QL);
    ev_wait<15>(E_G);
    dma4(vx, wo - 4096 + cb * 128 + ((1 + i8) & 7) * 65536, dst0 + 16384);
    dma4(vx, wo - 4096 + cb * 128 + ((2 + i8) & 7) * 65536, dst0 + 32768);
    const int k = 32 * ct + mt;
    unsigned o_x = (unsigned)(WS_XT + k * KS + bt * 16), o_w = (unsigned)(WS_WT + k * KS + bt * 32);
    unsigned l_xw = L_SCR + (bt >> 4) * 256 + ((c ^ (bt >> 4)) << 4);
    unsigned l_ca[4];
#pragma unroll
    for (int i = 0; i < 4; ++i) { l_ca[i] = L_SCR + c * 256 + (((4 * i + g) ^ c) << 4); PIN(l_ca[i]); }
    unsigned l_ew = L_SCR + 8192 + (wb >> 1) * 4096 + lane * 16 + (wb & 1) * 8, l_er = L_SCR + 8192 + (wb & 1) * 2048 + lane * 16;
    unsigned o_z = (unsigned)(WS_Z + k * KS + (2 * (16 * (wb & 1) + c) + (wb >> 1)) * 128 + g * 16);
    PIN(o_x); PIN(o_w); PIN(l_xw); PIN(l_ew); PIN(l_er); PIN(o_z);
    ST(2);
    if (wb == 0) { poll_all(ws, LN_FB + ct * 32 + (lane & 31), tag, 32); SIG(E_FB); }
    else ev_wait<1>(E_FB);
    __builtin_amdgcn_s_setprio(2);
    ST(3);
    {
      const u32x4 x0 = LD128(o_x), x1 = LD128(o_x + 4096);
      const u32x4 wv[2] = {LD128(o_w), LD128(o_w + 16)};
      u32x4 ar, ai, an;
      u32x4 ca[2][2][2];
      auto twiddle_dft32 = [&](const f32x4 (&yr)[2], const f32x4 (&yi)[2], f32x4 (&xr)[2], f32x4 (&xi)[2]) {
        const f32x4 r0 = yr[0] * w1c[0] + yi[0] * w1s[0], r1 = yr[1] * w1c[1] + yi[1] * w1s[1];
        const u32x4 br = pack8(r0, r1), bn = pack8(-r0, -r1);
        const u32x4 bi = pack8(yi[0] * w1c[0] - yr[0] * w1s[0], yi[1] * w1c[1] - yr[1] * w1s[1]);
#pragma unroll
        for (int ut = 0; ut < 2; ++ut) {
          xr[ut] = mfma32(mas[ut], bi, mfma32(mac[ut], br, zero));
          xi[ut] = mfma32(mas[ut], bn, mfma32(mac[ut], bi, zero));
        }
      };
      if (k != 0) {
        *LP(u32x4, l_xw) = x0; *(LAS u32x4*)(LP(char, l_xw) + 4096) = x1;
        SIG(E_X1);
        f32x4 fr[2] = {lo4(wv[0]), lo4(wv[1])}, fi[2] = {hi4(wv[0]), hi4(wv[1])};
        PIN(fr[0]); PIN(fr[1]); PIN(fi[0]); PIN(fi[1]);
        ST(4);
        ev_wait<15>(E_X1);
#pragma unroll
        for (int i = 0; i < 8; ++i) ca[i >> 2][(i >> 1) & 1][i & 1] = ld16(l_ca[i >> 1], (i & 1) * 4096);
        f32x4 yr[2] = {zero, zero}, yi[2] = {zero, zero}, xr[2], xi[2];
#pragma unroll
        for (int ks = 0; ks < 2; ++ks)
#pragma unroll
          for (int tt = 0; tt < 2; ++tt) { yr[tt] = mfma32(ca[0][ks][tt], m0c[ks], yr[tt]); yi[tt] = mfma32(ca[0][ks][tt], m0n[ks], yi[tt]); }
#pragma unroll
        for (int ks = 0; ks < 2; ++ks)
#pragma unroll
          for (int tt = 0; tt < 2; ++tt) { yr[tt] = mfma32(ca[1][ks][tt], m0s[ks], yr[tt]); yi[tt] = mfma32(ca[1][ks][tt], m0c[ks], yi[tt]); }
        twiddle_dft32(yr, yi, xr, xi);
        const f32x4 i0 = xr[0] * fi[0] + xi[0] * fr[0], i1 = xr[1] * fi[1] + xi[1] * fr[1];
        ar = pack8(xr[0] * fr[0] - xi[0] * fi[0], xr[1] * fr[1] - xi[1] * fi[1]);
        ai = pack8(i0, i1); an = pack8(-i0, -i1);
      } else {
        const u32x4 wn[2] = {LD128(o_w + 256 * KS), LD128(o_w + 256 * KS + 16)};
        unsigned mp[8];
#pragma unroll
        for (int e = 0; e < 8; ++e) { mp[e] = 4 * wt_pos((2048 - (16 * wb + c + 64 * (16 * (e >> 2) + 4 * g + (e & 3)))) & 2047); PIN(mp[e]); }
        *LP(u32x4, l_xw) = x0; *(LAS u32x4*)(LP(char, l_xw) + 4096) = x1;
        LAS u32x4* d = (LAS u32x4*)sm + bt * 2;
        d[0] = wv[0]; d[1] = wv[1]; d[512] = wn[0]; d[513] = wn[1];
        SIG(E_X1);
        ev_wait<15>(E_X1);
#pragma unroll
        for (int i = 0; i < 8; ++i) ca[i >> 2][(i >> 1) & 1][i & 1] = ld16(l_ca[i >> 1], (i & 1) * 4096);
        u32x4 m0[2], m1[2];
#pragma unroll
        for (int e = 0; e < 8; ++e) { m0[e >> 2][e & 3] = *LP(unsigned, mp[e]); m1[e >> 2][e & 3] = *(LAS unsigned*)(LP(char, mp[e]) + 8192); }
        f32x4 yr[2] = {zero, zero}, yi[2] = {zero, zero}, xr[2], xi[2], dr[2], di[2];
#pragma unroll
        for (int ks = 0; ks < 2; ++ks)
#pragma unroll
          for (int tt = 0; tt < 2; ++tt) { yr[tt] = mfma32(ca[0][ks][tt], m0c[ks], yr[tt]); yi[tt] = mfma32(ca[0][ks][tt], m0n[ks], yi[tt]); }
        twiddle_dft32(yr, yi, xr, xi);
#pragma unroll
        for (int ut = 0; ut < 2; ++ut) {
          const f32x4 fr = lo4(wv[ut]) + lo4(m0[ut]), fi = hi4(wv[ut]) - hi4(m0[ut]);
          dr[ut] = xr[ut] * fr - xi[ut] * fi; di[ut] = xr[ut] * fi + xi[ut] * fr;
          yr[ut] = zero; yi[ut] = zero;
        }
#pragma unroll
        for (int ks = 0; ks < 2; ++ks)
#pragma unroll
          for (int tt = 0; tt < 2; ++tt) { yr[tt] = mfma32(ca[1][ks][tt], m0s[ks], yr[tt]); yi[tt] = mfma32(ca[1][ks][tt], m0c[ks], yi[tt]); }
        twiddle_dft32(yr, yi, xr, xi);
#pragma unroll
        for (int ut = 0; ut < 2; ++ut) {
          const f32x4 fr = lo4(wn[ut]) + lo4(m1[ut]), fi = hi4(wn[ut]) - hi4(m1[ut]);
          dr[ut] += xr[ut] * fr - xi[ut] * fi; di[ut] += xr[ut] * fi + xi[ut] * fr;
        }
        ar = pack8(dr[0], dr[1]); ai = pack8(di[0], di[1]); an = pack8(-di[0], -di[1]);
      }
      ST(5);
#pragma unroll
      for (int tt = 0; tt < 2; ++tt) {
        const f32x4 vr = mfma32(an, mas[tt], mfma32(ar, mac[tt], zero));
        const f32x4 vi = mfma32(ar, mas[tt], mfma32(ai, mac[tt], zero));
        const f32x4 pr = vr * w2c[tt] - vi * w2s[tt], pi = vi * w2c[tt] + vr * w2s[tt];
        *(LAS u32x2*)(LP(char, l_ew) + tt * 2048) = u32x2{pk2(pr[0], pr[1]), pk2(pr[2], pr[3])};
        *(LAS u32x2*)(LP(char, l_ew) + tt * 2048 + 1024) = u32x2{pk2(pi[0], pi[1]), pk2(pi[2], pi[3])};
      }
      SIG(E_X2);
      ev_wait<15>(E_X2);
      f32x4 zr[2] = {zero, zero}, zi[2] = {zero, zero}, zs[2] = {zero, zero};
#pragma unroll
      for (int ks = 0; ks < 2; ++ks) {
        const u32x4 vr = ld16(l_er, ks * 4096), vi = ld16(l_er, ks * 4096 + 1024);
#pragma unroll
        for (int jt = 0; jt < 2; ++jt) {
          zr[jt] = mfma32(mcc[jt][ks], vr, zr[jt]); zi[jt] = mfma32(mcs[jt][ks], vr, zi[jt]);
          zs[jt] = mfma32(mcs[jt][ks], vi, zs[jt]); zi[jt] = mfma32(mcc[jt][ks], vi, zi[jt]);
        }
      }
      ST128(pack8(zr[0] - zs[0], zr[1] - zs[1]), o_z);
      ST128(pack8(zi[0], zi[1]), o_z + 64);
    }
    __builtin_amdgcn_s_setprio(0);
    ST(6);
    {
      make_g();
      const unsigned sel = (c & 1) ? 0x01000302u : 0x03020100u;
      const unsigned sgn = (c & 3) == 0 ? 0u : (c & 3) == 1 ? 0x80000000u : (c & 3) == 2 ? 0x80008000u : 0x00008000u;
      auto g_rest = [&]() {
#pragma unroll
        for (int f = 0; f < 8; ++f) {
          const int src = (f >> 2) * 8 + (f & 3);
#pragma unroll
          for (int i = 0; i < 4; ++i) ga[src + 4][i] = __builtin_amdgcn_perm(0u, ga[src][i], sel) ^ sgn;
          PIN(ga[src + 4]);
        }
        if (g == 0) ga[0][0] = (c & 1) ? 0xBE803E80u : 0x3E803E80u;
        PIN(ga[0]);
      };
      SB();
      DRAIN();
      SIG(E_ZD);
      if (wb == 0) {
        ev_wait<15>(E_ZD);
        if (lane < REP) __hip_atomic_fetch_add((GAS u64*)(ws + (size_t)(LN_CZ + lane * 16 + 2 * ct + (mt >> 4)) * 128), 1ull, RLX_AGENT);
      }
      ST(7);
      g_rest();
    }
    unsigned o_zl = (unsigned)(WS_Z + (64 * wb + (lane >> 3)) * KS + rb * 128 + (lane & 7) * 16);
    unsigned l_zw, l_z0, l_z1;
    {
      const int kk = 64 * wb + (lane >> 3), R = (((kk & 1) * 128 + (kk >> 1)) << 1) + ((lane >> 2) & 1);
      l_zw = R * 64 + ((lane & 3) << 4);
      l_z0 = (8 * g + (c >> 2)) * 64 + 8 * (c & 3) + ((g & 1) << 5);
      l_z1 = l_z0 ^ 32u;
    }
    unsigned l_zw1 = l_zw ^ 32u;
    PIN(o_zl); PIN(l_zw); PIN(l_zw1); PIN(l_z0); PIN(l_z1);
    __builtin_amdgcn_s_setprio(BPRIO);
    ST(11);
    if (wb == 0) { TGATE(4); poll_all(ws, LN_CZ + (bid % REP) * 16 + c, tag + 16, 16); SIG(E_FZ); }
    else ev_wait<1>(E_FZ);
    ST(8);
    {
      u32x4 z[8];
#pragma unroll
      for (int i = 0; i < 8; ++i) z[i] = __builtin_amdgcn_raw_buffer_load_b128(rs, o_zl, i * 8 * KS, 16);
      ev_wait<15>(E_WR);
#pragma unroll
      for (int i = 0; i < 8; ++i) *(LAS u32x4*)(LP(char, (i & 1) ? l_zw1 : l_zw) + i * 512) = z[i];
    }
    SIG(E_ZI);
    ST(9);
    ev_wait<15>(E_ZI);
    f32x4 acc[2][2] = {{zero, zero}, {zero, zero}};
    {
      u32x4 zf[3][2][2];
      auto ldz = [&](int n) {
#pragma unroll
        for (int i = 0; i < 2; ++i) { zf[n % 3][i][0] = trfrag(l_z0, (2 * n + i) * 2048, 256); zf[n % 3][i][1] = trfrag(l_z1, (2 * n + i) * 2048, 256); }
      };
      ldz(0); ldz(1);
      SB();
#pragma unroll
      for (int n = 0; n < 8; ++n) {
        if (n < 6) ldz(n + 2);
#pragma unroll
        for (int i = 0; i < 2; ++i) {
          acc[n >> 2][0] = mfma32(ga[2 * n + i], zf[n % 3][i][0], acc[n >> 2][0]);
          acc[n >> 2][1] = mfma32(ga[2 * n + i], zf[n % 3][i][1], acc[n >> 2][1]);
        }
#pragma unroll
        for (int i = 0; i < 4; ++i) { __builtin_amdgcn_sched_group_barrier(0x100, 2, 0); __builtin_amdgcn_sched_group_barrier(0x8, 1, 0); }
        SB();
      }
    }
    ST(10);
    {
      unsigned o_out[2];
#pragma unroll
      for (int tt = 0; tt < 2; ++tt) o_out[tt] = (unsigned)(((rb >> 1) + 32 * (32 * (rb & 1) + 16 * tt + c)) * 1024 + (64 * cb + 16 * wb + 4 * g) * 2);
      const volatile LAS unsigned* fp = (const volatile LAS unsigned*)(size_t)(L_SYNC + E_A * 16 + wb * 4);
      while (!__builtin_amdgcn_readfirstlane(*fp)) __builtin_amdgcn_s_sleep(1);
      CFENCE();
      const unsigned l_a = 98304u + wb * 8192 + lane * 16;
      const f32x2 sc = *(const LAS f32x2*)(LP(char, l_a) + 4096);
#pragma unroll
      for (int tt = 0; tt < 2; ++tt)
#pragma unroll
        for (int hf = 0; hf < 2; ++hf) {
          const f32x4 av = __builtin_bit_cast(f32x4, ld16(l_a, (hf * 2 + tt) * 1024));
          const f32x4 r = av + (hf ? acc[0][tt] - acc[1][tt] : acc[0][tt] + acc[1][tt]) * sc[tt];
          *(u32x2*)(out + o_out[tt] + 512 * hf) = u32x2{pk2(r[0], r[1]), pk2(r[2], r[3])};
        }
    }
    ST(13);
  } else {
    __builtin_amdgcn_s_setprio(1);
    u32x4 wrv, wiv;
    unsigned nyq = 0, nyq2 = 0, pf = 0;
    {
      const unsigned o_w = (unsigned)(wt_row(64 * mt + (tid >> 2)) * 257 + 32 * ct + 8 * (tid & 3)) * 2u;
      wrv = *(const u32x4u*)(w_r + o_w);
      wiv = *(const u32x4u*)(w_i + o_w);
      if (ct == 0 && w == 0) {
        const unsigned o_ny = (unsigned)(wt_row(64 * mt + lane) * 257 + 256) * 2u;
        nyq = *(const unsigned short*)(w_r + o_ny); nyq2 = *(const unsigned short*)(w_i + o_ny);
      }
    }
    if (w == 2 && lane < CODE_LINES) pf = *(const GAS unsigned*)(pc0 + min((idx * CODE_LINES + lane) * 128, CODE_MAX));
    const u32x2 ev = *(const u32x2*)ws;
    const int rec = (2 * ct + (w >> 1)) * 64 + 2 * mt + (w & 1);
    unsigned o_kp = (unsigned)(WS_KVP + rec * RS + lane * 16), o_zp = (unsigned)(WS_ZP + rec * 64 + g * 16);
    ST128((u32x4{~0u, ~0u, ~0u, ~0u}), o_kp);
    if (c == 0) ST128((u32x4{~0u, ~0u, ~0u, ~0u}), o_zp);
    SB();
    const int rh = w & 1, kt = w >> 1;
    const int kf = 32 * ct + 16 * kt + c;
    u32x4 xf[4][4][2];
    unsigned bb[2][4];
    {
      const unsigned fl = (unsigned)((c >> 2) | ((g & 1) << 2));
      const unsigned bu = (unsigned)((8 * g + (c >> 2)) * 256 + 8 * (c & 3)) + (((unsigned)(2 * kt) ^ fl) << 5);
#pragma unroll
      for (int u = 0; u < 4; ++u) {
        bb[0][u] = bu ^ (unsigned)((u & 1) * 32 + (u >> 1) * 128);
        bb[1][u] = bb[0][u] + 65536u;
      }
    }
    u32x4 fb[3][4];
    auto ldf = [&](int n) {
#pragma unroll
      for (int u = 0; u < 4; ++u) {
        const int slot = (4 + (n >> 1)) & 7;
        fb[n % 3][u] = trfrag(bb[slot >> 2][u], (slot & 3) * 16384 + (n & 1) * 8192, 1024);
      }
    };
    u64 tag;
    {
      u32x4 fc[4], fs[4];
      {
        const float kk = (float)kf * (1.f / 512.f);
        const float a0 = __builtin_amdgcn_fractf(kk * (float)(8 * g + 128 * ((c0 + 1) & 3)));
#pragma unroll
        for (int ks = 0; ks < 4; ++ks) {
          const float ak = __builtin_fmaf(kk, (float)(((ks & 2) ^ (flip << 1)) * 32), a0);
#pragma unroll
          for (int i = 0; i < 4; ++i) {
            const float p0 = __builtin_fmaf(kk, (float)(32 * (ks & 1) + 2 * i), ak), p1 = __builtin_fmaf(kk, (float)(32 * (ks & 1) + 2 * i + 1), ak);
            fc[ks][i] = pk2(__builtin_amdgcn_cosf(p0), __builtin_amdgcn_cosf(p1));
            fs[ks][i] = kf == 0 ? 0xBF803F80u
                                : pk2(__builtin_amdgcn_sinf(-p0), __builtin_amdgcn_sinf(-p1));
          }
          PIN(fc[ks]); PIN(fs[ks]);
        }
      }
      float ra = (c & 1) ? 0.f : ((c & 2) ? -1.f : 1.f), rbi = (c & 1) ? ((c & 2) ? 1.f : -1.f) : 0.f;
      unsigned l_sc = L_SCR + ((tid & 3) * 512 + ((tid >> 2) ^ ((tid & 3) << 3))) * 4;
      unsigned l_rd = L_SCR + (tid >> 4) * 256 + (((tid & 15) ^ ((tid >> 7) << 1)) << 4);
      unsigned o_wt = (unsigned)(WS_WT + (32 * ct + (tid >> 4)) * KS + (64 * mt + 4 * (tid & 15)) * 4);
      unsigned o_xt = (unsigned)(WS_XT + kf * KS + mt * 256 + rh * 64 + g * 16);
      unsigned xa[4];
#pragma unroll
      for (int ks = 0; ks < 4; ++ks) { xa[ks] = rh * 8192 + c * 256 + (((4 * ks + g) ^ (flip << 3) ^ c) << 4); PIN(xa[ks]); }
#pragma unroll
      for (int u = 0; u < 4; ++u) { PIN(bb[0][u]); PIN(bb[1][u]); }
      PIN(l_sc); PIN(l_rd); PIN(o_wt); PIN(o_xt); PIN(ra); PIN(rbi); PIN(o_kp); PIN(o_zp);
      ST(1);
      f32x4 xr[4][2], xi[4][2];
      auto ldx = [&](int s) {
#pragma unroll
        for (int ks = 0; ks < 4; ++ks)
#pragma unroll
          for (int tt = 0; tt < 2; ++tt) xf[s][ks][tt] = ld16(xa[ks], s * 16384 + tt * 4096);
      };
      auto mmf = [&](int s) {
#pragma unroll
        for (int tt = 0; tt < 2; ++tt) { xr[s][tt] = zero; xi[s][tt] = zero; }
#pragma unroll
        for (int ks = 0; ks < 4; ++ks)
#pragma unroll
          for (int tt = 0; tt < 2; ++tt) {
            xr[s][tt] = mfma32(xf[s][ks][tt], fc[ks], xr[s][tt]);
            xi[s][tt] = mfma32(xf[s][ks][tt], fs[ks], xi[s][tt]);
          }
      };
      auto horner = [&](int s) {
#pragma unroll
        for (int tt = 0; tt < 2; ++tt) {
          xr[s][tt] = (xr[s][tt] + xr[s - 1][tt] * ra) - xi[s - 1][tt] * rbi;
          xi[s][tt] = (xi[s][tt] + xr[s - 1][tt] * rbi) + xi[s - 1][tt] * ra;
        }
      };
      auto mixv = [&]() {
#pragma unroll
        for (int i = 0; i < 16; ++i) { __builtin_amdgcn_sched_group_barrier(0x8, 1, 0); __builtin_amdgcn_sched_group_barrier(0x2 | 0x80 | 0x40, 2, 0); }
        SB();
      };
      BAR();
      ldx(0);
      {
        PIN(pf); PIN(nyq); PIN(nyq2);
        tag = TAG_OF(((u64)(unsigned)__builtin_amdgcn_readfirstlane(ev.y) << 32) | (unsigned)__builtin_amdgcn_readfirstlane(ev.x));
        LAS unsigned* sc = LP(unsigned, l_sc);
#pragma unroll
        for (int j = 0; j < 4; ++j) {
          sc[(2 * j) * 64] = __builtin_amdgcn_perm(wiv[j], wrv[j], 0x05040100u);
          sc[(2 * j + 1) * 64] = __builtin_amdgcn_perm(wiv[j], wrv[j], 0x07060302u);
        }
        if (ct == 0 && w == 0) ST32(nyq | (nyq2 << 16), WS_WT + 256 * KS + (64 * mt + lane) * 4);
        if (mt == 0 && w == 1 && lane < 4 * REP)
          put_flag(ws, LN_CZ + (lane >> 1) * 16 + 2 * ct + (lane & 1), tag);
      }
      BAR();
      const u32x4 wt0 = ld16(l_rd, 0), wt1 = ld16(l_rd ^ 64u, 4096);
      ldx(1); SB(); mmf(0);
      BAR();
      ldx(2); SB(); mmf(1);
      BAR();
      ldx(3); SB();
      mmf(2); horner(1);
      ST128(wt0, o_wt); ST128(wt1, o_wt + 16 * KS);
      mixv();
      mmf(3); horner(2);
      mixv();
      ldf(0); ldf(1);
      horner(3);
      ST128(pack8(xr[3][0], xr[3][1]), o_xt);
      ST128(pack8(xi[3][0], xi[3][1]), o_xt + 128);
    }
    ST(2);
    auto mix = [&]() {
#pragma unroll
      for (int i = 0; i < 8; ++i) { __builtin_amdgcn_sched_group_barrier(0x100, 1, 0); __builtin_amdgcn_sched_group_barrier(0x8, 1, 0); }
      SB();
    };
    {
      f32x4 acc[2][4];
#pragma unroll
      for (int t = 0; t < 2; ++t)
#pragma unroll
        for (int u = 0; u < 4; ++u) acc[t][u] = zero;
#pragma unroll
      for (int n = 0; n < 16; ++n) {
        if (n == FBN) DRAIN();
        if (n == 0 || n == 4 || n == 8) BAR();
        if (n == FBN) { if (tid == 0) put_flag(ws, LN_FB + ct * 32 + mt, tag); SB(); }
        if (n == 12) { SIG(E_G); SB(); }
        if (n < 14) ldf(n + 2);
#pragma unroll
        for (int u = 0; u < 4; ++u)
#pragma unroll
          for (int t = 0; t < 2; ++t) acc[t][u] = mfma32(xf[n >> 2][n & 3][t], fb[n % 3][u], acc[t][u]);
        mix();
      }
      ST(3);
      const u32x4 ones = {0x3f803f80u, 0x3f803f80u, 0x3f803f80u, 0x3f803f80u};
      f32x4 kv[2], zz[2];
#pragma unroll
      for (int n = 0; n < 2; ++n) {
        const u32x4 pk = pack8(phi4(acc[0][n]), phi4(acc[1][n]));
        kv[n] = mfma32(pk, pack8(acc[0][2 + n], acc[1][2 + n]), zero);
        zz[n] = mfma32(pk, ones, zero);
      }
      u32x4 kvo = pack8(kv[0], kv[1]), zo = pack8(zz[0], zz[1]);
#pragma unroll
      for (int e = 0; e < 4; ++e) {
        kvo[e] = min(kvo[e], 0xfffffffeu);
        zo[e] = min(zo[e], 0xfffffffeu);
      }
      ST128(kvo, o_kp);
      if (c == 0) ST128(zo, o_zp);
    }
    ST(4);
    {
      const int hp = 2 * ct + (mt >> 4), sub = mt & 15;
      u32x4 pv[4];
      unsigned pz;
      auto ldp = [&]() {
#pragma unroll
        for (int i = 0; i < 4; ++i)
          pv[i] = __builtin_amdgcn_raw_buffer_load_b128(rs, WS_KVP + (hp * 64 + (lane >> 2)) * RS + sub * 64 + (lane & 3) * 16, i * 16 * RS, 16);
        pz = LD32(WS_ZP + (hp * 64 + lane) * 64 + sub * 4);
      };
      f32x4 acc[2][2] = {{zero, zero}, {zero, zero}};
      ev_wait<15>(E_QL);
      SB();
      ldf(0); ldf(1);
      SB();
#pragma unroll
      for (int n = 0; n < 8; ++n) {
        if (n < 6) ldf(n + 2);
        if (n == 6) { SIG(E_QR); SB(); }
        if (n == LDPN) {
          if (w == 0) { ev_wait<1>(E_FB); ldp(); }
          SB();
        }
#pragma unroll
        for (int u = 0; u < 4; ++u)
#pragma unroll
          for (int t = 0; t < 2; ++t) acc[u & 1][t] = mfma32(fb[n % 3][u], xf[n >> 1][2 * (u >> 1) + (n & 1)][t], acc[u & 1][t]);
        mix();
      }
      auto st_pq = [&]() {
#pragma unroll
        for (int n = 0; n < 2; ++n)
          ST128(pack8(phi4(acc[n][0]), phi4(acc[n][1])), WS_PQ + ((2 * mt + rh) * 32 + 4 * ct + 2 * kt + n) * 1024 + lane * 16);
        SB();
      };
      st_pq();
      ST(5);
      if (w == 0) {
        for (unsigned spins = 0; spins < SPIN_CAP; ++spins) {
          unsigned m = pz;
#pragma unroll
          for (int i = 0; i < 4; ++i) m = max(max(m, max(pv[i][0], pv[i][1])), max(pv[i][2], pv[i][3]));
          if (__all(m != ~0u)) break;
          CFENCE();
          ldp();
        }
        ST(6);
        const unsigned one = (c & 1) ? 0x3f800000u : 0x00003f80u;
        const u32x4 sel = {(c >> 1) == 0 ? one : 0u, (c >> 1) == 1 ? one : 0u, (c >> 1) == 2 ? one : 0u, (c >> 1) == 3 ? one : 0u};
        f32x4 s = zero;
#pragma unroll
        for (int i = 0; i < 4; ++i) s = mfma32(sel, pv[i], s);
        const f32x4 zq = mfma32(sel, u32x4{pz, 0u, 0u, 0u}, zero);
#pragma unroll
        for (int e = 0; e < 4; ++e) s[e] = dpp_add<0x128>(dpp_add<0x124>(s[e]));
        const float z0 = row_sum16(zq[0]), z1 = row_sum16(zq[1]);
        if ((lane & 44) == 0) ST64((u32x2{pk2(s[0], s[1]), pk2(s[2], s[3])}), WS_KVF + hp * 1024 + (sub * 4 + c) * 16 + g * 8);
        if (lane == 0) ST32(pk2(z0, z1), WS_ZF + hp * 64 + sub * 4);
        SB();
      }
      if (w == 0) {
        VMW(2);
        ev_wait<14>(E_PQ);
        if (lane == 0) put_flag(ws, LN_FQ + mt * 8 + ct, tag);
        VMW(1);
        if (lane < REP) __hip_atomic_fetch_add((GAS u64*)(ws + (size_t)(LN_CK + lane * 16 + 2 * ct + (mt >> 4)) * 128), 1ull, RLX_AGENT);
        SIG(E_KA);
      } else {
        unsigned voff[4];
#pragma unroll
        for (int i = 0; i < 4; ++i) {
          const int r = 4 * i + g, sl = c ^ ((r & 7) << 1);
          voff[i] = (unsigned)(r * 1024 + (sl >> 3) * 512 + ((sl & 7) << 4) + 4096 - i * 1024);
        }
        ev_wait<15>(E_QR);
#pragma unroll 1
        for (int gj = 11 + w; gj < ((ct | mt) == 0 ? 36 : 32); gj += 3) {
          const int gi = gj & 31;
          if (gj >= 32 && (ct | mt) == 0) ev_wait<15>(E_X2);
          dma4(voff, wo - 4096 + cb * 128 + ((gi + 4 * i8) & 31) * 16384, gi * 4096);
          VMW(WO_THR);
          if (gj < 15) { VMW(4); SIG(E_PQ); }
        }
        DRAIN();
        SIG(E_WL);
      }
    }
    __builtin_amdgcn_s_setprio(APRIO);
    ST(7);
    u32x4 wa[16][2];
    {
      const unsigned f7 = (unsigned)((4 * g + (c >> 2)) & 7);
      unsigned pw[2][2];
#pragma unroll
      for (int hf = 0; hf < 2; ++hf) {
        pw[hf][0] = (4 * g + (c >> 2)) * 256 + 8 * (c & 3) + ((((unsigned)w + 4u * hf) ^ f7) << 5);
        pw[hf][1] = pw[hf][0] + 65536u;
        PIN(pw[hf][0]); PIN(pw[hf][1]);
      }
      ev_wait<15>(E_X1);
      ev_wait<14>(E_WL);
#pragma unroll
      for (int st = 0; st < 16; ++st)
#pragma unroll
        for (int hf = 0; hf < 2; ++hf) wa[st][hf] = trfrag(pw[hf][st >> 3], (st & 7) * 8192, 4096);
      SIG(E_WR);
    }
    SB();
    ST(8);
    if (w == 1 || w == 2) ev_wait<1>(E_KA);
    if (w == 1) { TGATE(2); poll_all(ws, LN_FQ + (rb >> 1) * 8 + (lane & 7), tag, 8); SIG(E_FQ); }
    else if (w == 2) {
      TGATE(8);
      poll_all(ws, LN_CK + (bid % REP) * 16 + c, tag + 16, 16);
      SIG(E_FK);
      if (bid == 0 && lane == 0) *(u32x2*)ws = u32x2{ev.x + 1u, ev.y};
    }
    ev_wait<2>(E_FQ);
    u32x4 pq[8];
#pragma unroll
    for (int i = 0; i < 8; ++i) pq[i] = LD128(WS_PQ + (rb * 32 + 8 * w + i) * 1024 + lane * 16);
    float gate[2];
#pragma unroll
    for (int tt = 0; tt < 2; ++tt) gate[tt] = __uint_as_float(LD32(WS_G + (32 * rb + 16 * tt + c) * 4));
    unsigned l_o[4];
#pragma unroll
    for (int hp = 0; hp < 4; ++hp) { l_o[hp] = 65536u + ((4 * w + hp - 2 * i8) & 15) * 2048 + lane * 16; PIN(l_o[hp]); }
    unsigned o_kf = (unsigned)(WS_KVF + 4 * w * 1024 + lane * 16), o_zf = (unsigned)(WS_ZF + 4 * w * 64 + g * 16), l_or = 65536u + lane * 16;
    PIN(o_kf); PIN(o_zf); PIN(l_or);
    ev_wait<15>(E_WR);
    SB();
    ST(9);
    ev_wait<4>(E_FK);
    ST(10);
    {
      u32x4 kf4[4], zf4[4];
#pragma unroll
      for (int i = 0; i < 4; ++i) { kf4[i] = LD128(o_kf + i * 1024); zf4[i] = LD128(o_zf + i * 64); }
      f32x4 num[2][2][2], den[2][2][2];
      auto mo = [&](int hp) {
#pragma unroll
        for (int tt = 0; tt < 2; ++tt)
#pragma unroll
          for (int n = 0; n < 2; ++n) {
            const u32x4 pp = pq[2 * hp + n];
            const u32x2 b = tt ? u32x2{pp.z, pp.w} : u32x2{pp.x, pp.y};
            const u32x2 ka = n ? u32x2{kf4[hp].z, kf4[hp].w} : u32x2{kf4[hp].x, kf4[hp].y};
            const u32x2 za = n ? u32x2{zf4[hp].z, zf4[hp].w} : u32x2{zf4[hp].x, zf4[hp].y};
            den[hp & 1][tt][n] = mfma16(za, b, f32x4{1e-6f, 1e-6f, 1e-6f, 1e-6f});
            num[hp & 1][tt][n] = mfma16(ka, b, zero);
          }
      };
      mo(0);
      SB();
#pragma unroll
      for (int hp = 0; hp < 4; ++hp) {
        if (hp < 3) mo(hp + 1);
        SB();
#pragma unroll
        for (int tt = 0; tt < 2; ++tt)
          *(LAS u32x4*)(LP(char, l_o[hp]) + tt * 1024) = pack8(num[hp & 1][tt][0] * __builtin_amdgcn_rcpf(den[hp & 1][tt][0][0]),
                                                               num[hp & 1][tt][1] * __builtin_amdgcn_rcpf(den[hp & 1][tt][1][0]));
        SB();
      }
    }
    SIG(E_O);
    ST(11);
    ev_wait<15>(E_O);
    f32x4 acc[2][2] = {{zero, zero}, {zero, zero}};
    {
#pragma unroll
      for (int st = 0; st < 16; ++st)
#pragma unroll
        for (int tt = 0; tt < 2; ++tt) {
          const u32x4 ob = ld16(l_or, (st * 2 + tt) * 1024);
          acc[0][tt] = mfma32(wa[st][0], ob, acc[0][tt]);
          acc[1][tt] = mfma32(wa[st][1], ob, acc[1][tt]);
        }
      __builtin_amdgcn_sched_group_barrier(0x100, 8, 0);
#pragma unroll
      for (int i = 0; i < 24; ++i) { __builtin_amdgcn_sched_group_barrier(0x8, 2, 0); __builtin_amdgcn_sched_group_barrier(0x100, 1, 0); }
      __builtin_amdgcn_sched_group_barrier(0x8, 16, 0);
    }
#pragma unroll
    for (int i = 0; i < 4; ++i) *(LAS f32x4*)(LP(char, l_or) + 32768 + w * 8192 + i * 1024) = acc[i >> 1][i & 1] * gate[i & 1];
    *(LAS f32x2*)(LP(char, l_or) + 32768 + w * 8192 + 4096) = f32x2{(1.f - gate[0]) * (2.f / (2048.f * 512.f)), (1.f - gate[1]) * (2.f / (2048.f * 512.f))};
    SIG(E_A);
    ST(12);
  }
}

extern "C" void kernel_launch(void* const* d_in, const int* in_sizes, int n_in, void* d_out, int out_size,
                              void* d_ws, size_t ws_size, hipStream_t stream) {
  (void)hipFuncSetAttribute((const void*)fused_kernel, hipFuncAttributeMaxDynamicSharedMemorySize, LDS_BYTES);
  fused_kernel<<<256, 512, LDS_BYTES, stream>>>((const char*)d_in[0], (const char*)d_in[1], (const char*)d_in[2],
                                                (const char*)d_in[4], (const char*)d_in[5], (const char*)d_in[6],
                                                (char*)d_ws, (char*)d_out);
}
```

```cpp
#include <hip/hip_runtime.h>
#include <hip/hip_bf16.h>
#include <stdint.h>

typedef short s16x4 __attribute__((ext_vector_type(4)));
typedef short s16x8 __attribute__((ext_vector_type(8)));
typedef float f32x2 __attribute__((ext_vector_type(2)));
typedef float f32x4 __attribute__((ext_vector_type(4)));
typedef unsigned u32x2 __attribute__((ext_vector_type(2)));
typedef unsigned u32x4 __attribute__((ext_vector_type(4)));
typedef unsigned u32x4u __attribute__((ext_vector_type(4), aligned(2)));
typedef __bf16 bf16x2 __attribute__((ext_vector_type(2)));
typedef unsigned long long u64;
#define DI __device__ __forceinline__
#define LAS __attribute__((address_space(3)))
#define GAS __attribute__((address_space(1)))
#define RLX_AGENT __ATOMIC_RELAXED, __HIP_MEMORY_SCOPE_AGENT
#define CFENCE() asm volatile("" ::: "memory")
#define PIN(v) asm volatile("" : "+v"(v))
#define DRAIN() asm volatile("s_waitcnt vmcnt(0)" ::: "memory")
#define LGKM0() __builtin_amdgcn_s_waitcnt(0xC07F)
#define SB() __builtin_amdgcn_sched_barrier(0)
#define VMW_(n) asm volatile("s_waitcnt vmcnt(" #n ")" ::: "memory")
#define VMW(n) VMW_(n)
#define LP(T, a) ((LAS T*)(size_t)(a))
#define WS_DBG (64u << 20)
#define BAR() do { CFENCE(); SB(); LGKM0(); __builtin_amdgcn_s_barrier(); SB(); CFENCE(); } while (0)
#define ST(i)

DI unsigned pk2(float a, float b) {
  f32x2 v = {a, b};
  return __builtin_bit_cast(unsigned, __builtin_convertvector(v, bf16x2));
}
DI float bflo(unsigned u) { return __uint_as_float(u << 16); }
DI float bfhi(unsigned u) { return __uint_as_float(u & 0xffff0000u); }
DI f32x4 lo4(u32x4 u) { return f32x4{bflo(u.x), bflo(u.y), bflo(u.z), bflo(u.w)}; }
DI f32x4 hi4(u32x4 u) { return f32x4{bfhi(u.x), bfhi(u.y), bfhi(u.z), bfhi(u.w)}; }
DI f32x4 mfma32(u32x4 a, u32x4 b, f32x4 c) {
  return __builtin_amdgcn_mfma_f32_16x16x32_bf16(__builtin_bit_cast(s16x8, a), __builtin_bit_cast(s16x8, b), c, 0, 0, 0);
}
DI f32x4 mfma16(u32x2 a, u32x2 b, f32x4 c) {
  return __builtin_amdgcn_mfma_f32_16x16x16bf16_1k(__builtin_bit_cast(s16x4, a), __builtin_bit_cast(s16x4, b), c, 0, 0, 0);
}
DI u32x4 pack8(f32x4 a, f32x4 b) { return u32x4{pk2(a[0], a[1]), pk2(a[2], a[3]), pk2(b[0], b[1]), pk2(b[2], b[3])}; }
DI u32x4 neg8(u32x4 a) {
  PIN(a);
  a ^= 0x80008000u;
  PIN(a);
  return a;
}
DI f32x4 phi4(f32x4 t) {
  const f32x4 m = t * 1.4426950408889634f, r = t + 1e-6f;
  f32x4 e, q;
#pragma unroll
  for (int i = 0; i < 4; ++i) { e[i] = __builtin_amdgcn_exp2f(fminf(m[i], 0.f)); q[i] = fmaxf(r[i], 1e-6f); }
  return e + q;
}
DI void crot(f32x4& cr, f32x4& ci, float a, float b) {
  const f32x4 nr = cr * a - ci * b, ni = cr * b + ci * a;
  cr = nr; ci = ni;
}
DI u32x4 trfrag(unsigned a, int off, int hi_off) {
  s16x4 l = __builtin_amdgcn_ds_read_tr16_b64_v4i16((LAS s16x4*)(LP(char, a) + off));
  s16x4 h = __builtin_amdgcn_ds_read_tr16_b64_v4i16((LAS s16x4*)(LP(char, a) + off + hi_off));
  return __builtin_bit_cast(u32x4, __builtin_shufflevector(l, h, 0, 1, 2, 3, 4, 5, 6, 7));
}
DI u32x4 ld16(unsigned a, int off) { return *(const LAS u32x4*)(LP(char, a) + off); }
template <int CTRL> DI float dpp_add(float v) {
  return v + __int_as_float(__builtin_amdgcn_update_dpp(0, __float_as_int(v), CTRL, 0xf, 0xf, false));
}
DI float row_sum16(float v) { return dpp_add<0x128>(dpp_add<0x124>(dpp_add<0x4E>(dpp_add<0xB1>(v)))); }

#define KS 8192
#define RS 1024
#define REPV 8
constexpr int REP = REPV;
constexpr int LN_FB = 8, LN_FQ = LN_FB + 256, LN_CZ = LN_FQ + 256, LN_CK = LN_CZ + 16 * REP;
constexpr int WS_XT = 160 * 1024;
constexpr int WS_WT = WS_XT + 256 * KS;
constexpr int WS_KVP = WS_WT + 257 * KS;
constexpr int WS_ZP = WS_KVP + 16 * 64 * RS;
constexpr int WS_KVF = WS_ZP + 16 * 64 * 64;
constexpr int WS_ZF = WS_KVF + 16 * 1024;
constexpr int WS_G = WS_ZF + 2048;
constexpr int WS_PQ = WS_G + 8192;
constexpr int WS_Z = WS_PQ + 2048 * 1024;
constexpr int WS_WOF = WS_Z;
constexpr int WS_END = WS_Z + 256 * KS;
constexpr int L_SCR = 131072, L_ST = L_SCR + 30720, L_SYNC = L_SCR + 32768 - 512, LDS_BYTES = 163840;
enum { E_QL, E_FB, E_X1, E_X2, E_ZD, E_FZ, E_ZI, E_QR, E_PQ, E_WL, E_WR, E_KA, E_FQ, E_FK, E_O, E_G, E_A, E_S1, E_XD };
constexpr unsigned SPIN_CAP = 1u << 16;
#define POLL_SLEEP 2
#define WO_THR 4
#define FBN 8
#define BPRIO 0
#define APRIO 1
#define POLLK 1
#define LDPN 4
#define CODE_MAX 30336
#define CODE_LINES 8
#define TAG_OF(e) ((e) * 0x9E3779B97F4A7C15ull + 0x632BE59BD9B4E019ull)

#define SIG(ev) do { CFENCE(); *(volatile LAS unsigned*)(LP(char, sigp) + (ev) * 16) = 1u; CFENCE(); } while (0)
template <unsigned MASK> DI void ev_wait(int ev) {
  const volatile LAS u32x4* p = (const volatile LAS u32x4*)(size_t)(L_SYNC + ev * 16);
  for (;;) {
    const u32x4 v = *p;
    const unsigned a = ((MASK & 1) ? v.x : 1u) & ((MASK & 2) ? v.y : 1u) & ((MASK & 4) ? v.z : 1u) & ((MASK & 8) ? v.w : 1u);
    if (__builtin_amdgcn_readfirstlane(a)) break;
    __builtin_amdgcn_s_sleep(1);
  }
  CFENCE();
}
DI void poll_all_(const char* ws, unsigned line, u64 want) {
  const GAS u64* p = (const GAS u64*)(ws + (size_t)line * 128);
  for (unsigned spins = 0; spins < SPIN_CAP; ++spins) {
    u64 v; const u64 z = 0;
    asm volatile("global_atomic_add_x2 %0, %1, %2, off sc0 sc1\n\ts_waitcnt vmcnt(0)" : "=&v"(v) : "v"(p), "v"(z) : "memory");
    if (__all(v == want)) break;
    __builtin_amdgcn_s_sleep(POLL_SLEEP);
  }
  __builtin_amdgcn_fence(__ATOMIC_ACQUIRE, "wavefront");
  CFENCE();
}
#define poll_all(ws, line, want, n) do { if (lane < (n)) poll_all_(ws, line, want); CFENCE(); } while (0)
#define TGATE(kind)
DI void put_flag(char* ws, unsigned line, u64 v) { __hip_atomic_store((GAS u64*)(ws + (size_t)line * 128), v, RLX_AGENT); }

DI void dma4(const unsigned (&voff)[4], const char* sb, unsigned dst) {
  unsigned keep;
  asm volatile("s_mov_b32 %0, m0\n\ts_mov_b32 m0, %6\n\ts_nop 0\n\t"
               "global_load_lds_dwordx4 %1, %5\n\t"
               "global_load_lds_dwordx4 %2, %5 offset:1024\n\t"
               "global_load_lds_dwordx4 %3, %5 offset:2048\n\t"
               "global_load_lds_dwordx4 %4, %5 offset:3072\n\t"
               "s_mov_b32 m0, %0"
               : "=&s"(keep) : "v"(voff[0]), "v"(voff[1]), "v"(voff[2]), "v"(voff[3]), "s"(sb), "s"(dst) : "memory");
}
DI int wt_pos(int u) {
  const int k0 = u & 63, u1 = u >> 6;
  return (((k0 >> 4) * 64 + ((u1 >> 2) & 3) * 16 + (k0 & 15)) << 3) + (u1 >> 4) * 4 + (u1 & 3);
}
DI int wt_row(int pos) {
  const int bt = pos >> 3;
  return 16 * (bt >> 6) + (bt & 15) + 64 * (16 * ((pos >> 2) & 1) + 4 * ((bt >> 4) & 3) + (pos & 3));
}
DI void trig4(float f, float i0, f32x4& co, f32x4& si) {
#pragma unroll
  for (int e = 0; e < 4; ++e) {
    const float a = f * (i0 + (float)e);
    co[e] = __builtin_amdgcn_cosf(a); si[e] = __builtin_amdgcn_sinf(a);
  }
}
DI void trig_frag(float f, float i0, u32x4& co, u32x4& si) {
  f32x4 c0, s0, c1, s1;
  trig4(f, i0, c0, s0); trig4(f, i0 + 16.f, c1, s1);
  co = pack8(c0, c1); si = pack8(s0, s1);
  PIN(co); PIN(si);
}

#define LOOK(off) ((unsigned)__builtin_amdgcn_raw_ptr_buffer_atomic_add_i32(0, rs, (off), 0, 16))
#define LD128(off) __builtin_amdgcn_raw_buffer_load_b128(rs, (off), 0, 16)
#define LD32(off) __builtin_amdgcn_raw_buffer_load_b32(rs, (off), 0, 16)
#define ST128(v, off) __builtin_amdgcn_raw_buffer_store_b128((v), rs, (off), 0, 16)
#define ST64(v, off) __builtin_amdgcn_raw_buffer_store_b64((v), rs, (off), 0, 16)
#define ST32(v, off) __builtin_amdgcn_raw_buffer_store_b32((v), rs, (off), 0, 16)

__global__ __launch_bounds__(512, 2) void fused_kernel(const char* __restrict__ x, const char* __restrict__ wqkv,
                                                       const char* __restrict__ wo, const char* __restrict__ w_r,
                                                       const char* __restrict__ w_i, const char* __restrict__ gate_w,
                                                       char* __restrict__ ws, char* __restrict__ out) {
  extern __shared__ __attribute__((aligned(16))) char smem[];
  LAS char* sm = (LAS char*)smem;
  asm volatile("" :: "s"(x), "s"(wqkv), "s"(wo), "s"(w_r), "s"(w_i), "s"(gate_w), "s"(ws), "s"(out), "s"(sm));
  u64 pc0;
  asm volatile("s_getpc_b64 %0" : "=s"(pc0));
  pc0 &= ~127ull;
  const int tid = threadIdx.x, lane = tid & 63;
  const int w = __builtin_amdgcn_readfirstlane(tid >> 6);
  const int bid = blockIdx.x, xcd = bid & 7, idx = bid >> 3;
  const int mt = (xcd & 3) * 8 + (idx & 7), ct = (xcd >> 2) * 4 + (idx >> 3);
  const int rb = xcd * 8 + (idx & 7), cb = idx >> 3;
  const int c = lane & 15, g = lane >> 4;
  const __amdgpu_buffer_rsrc_t rs = __builtin_amdgcn_make_buffer_rsrc(ws, 0, WS_END, 0x00020000);
  const f32x4 zero = {0, 0, 0, 0};
  unsigned sigp = L_SYNC + (w & 3) * 4;
  PIN(sigp);
  ST(0);
  const int i8 = idx & 7, c0 = (idx + (idx >> 3)) & 3, flip = (idx >> 2) & 1;
  if (w >= 4) {
    const int wb = w - 4, bt = tid - 256;
    unsigned vx[4], vk[4], vq[4];
    const unsigned dst0 = wb * 4096;
#pragma unroll
    for (int i = 0; i < 4; ++i) {
      const int r = 16 * wb + 4 * i + g;
      vx[i] = (unsigned)((mt + 32 * r) * 1024 + ((c ^ (r & 15)) << 4) + 4096 - i * 1024);
    }
    auto issue_x = [&](int i) { dma4(vx, x - 4096 + ((c0 - i) & 3) * 256, dst0 + i * 16384); };
    issue_x(0);
#pragma unroll
    for (int i = 0; i < 4; ++i) {
      const int r = 16 * wb + 4 * i + g, sl = c ^ ((g | ((i >> 1) << 2)) << 1);
      vk[i] = (unsigned)(r * 3072 + (sl >> 3) * 1024 + ((sl & 7) << 4) + 4096 - i * 1024);
    }
    auto issue_kv = [&](int j) {
      const int blk = 2 * ((c0 - (j >> 1)) & 3) + ((j & 1) ^ flip);
      dma4(vk, wqkv - 4096 + 1024 + ct * 128 + (unsigned)blk * (64 * 3072), dst0 + ((j + 4) & 7) * 16384);
    };
    u32x4 gw[4];
#pragma unroll
    for (int i = 0; i < 4; ++i)
      asm volatile("global_load_dwordx4 %0, %1, %2" : "=v"(gw[i]) : "v"(c * 16), "s"(gate_w + ((c0 - i) & 3) * 256) : "memory");
    issue_kv(0);
    issue_x(1);
    if (wb == 0) *(LAS u32x2*)(sm + L_SYNC + lane * 8) = u32x2{0u, 0u};
#pragma unroll
    for (int i = 0; i < 4; ++i) {
      const int r = 16 * wb + 4 * i + g, sl = c ^ ((g | ((i >> 1) << 2)) << 1);
      vq[i] = (unsigned)(r * 3072 + ((sl >> 3) ^ flip) * (64 * 3072) + ((sl & 7) << 4) + 4096 - i * 1024);
      PIN(vq[i]);
    }
    auto issue_q = [&](int j) { dma4(vq, wqkv - 4096 + ct * 128 + (unsigned)((c0 - j) & 3) * (128 * 3072), dst0 + (j + 4) * 16384); };
    const float g4 = (float)(4 * g);
    u32x4 m0c[2], m0s[2], m0n[2], mac[2], mas[2], mcc[2][2], mcs[2][2];
    f32x4 w1c[2], w1s[2], w2c[2], w2s[2];
    auto tab = [&](int row) { return (unsigned)(7 * 16384 + row * 256 + ((g ^ (row & 15)) << 4)); };
    {
      f32x4 c0v, s0v;
      trig4((float)(16 * wb + c) * (1.f / 64.f), g4, c0v, s0v);
      f32x4 c1v = c0v, s1v = s0v;
      crot(c1v, s1v, (c & 1) ? 0.f : ((c & 2) ? -1.f : 1.f), (c & 1) ? ((c & 2) ? -1.f : 1.f) : 0.f);
      const unsigned sg = (c & 1) ? 0x80008000u : 0u;
      m0c[0] = pack8(c0v, c1v); m0s[0] = pack8(s0v, s1v);
      PIN(m0c[0]); PIN(m0s[0]);
      m0c[1] = m0c[0] ^ sg; m0s[1] = m0s[0] ^ sg;
#pragma unroll
      for (int i = 0; i < 2; ++i) {
        m0n[i] = neg8(m0s[i]);
        *LP(u32x4, tab(16 * wb + c) ^ (64u * i)) = m0c[i];
        *LP(u32x4, tab(16 * wb + c) ^ (64u * i + 128u)) = m0s[i];
      }
    }
#pragma unroll
    for (int tt = 0; tt < 2; ++tt) {
      trig4((float)(16 * wb + c) * (1.f / 2048.f), g4 + 16.f * tt, w1c[tt], w1s[tt]);
      trig4((float)(16 * tt + c) * (1.f / 2048.f), g4 + 16.f * wb, w2c[tt], w2s[tt]);
      PIN(w1c[tt]); PIN(w1s[tt]); PIN(w2c[tt]); PIN(w2s[tt]);
    }
    u32x4 ga[16];
    auto make_g = [&]() {
      const int d = 64 * cb + 16 * wb + c;
      const float df = (float)d * (1.f / 512.f);
      f32x4 ec, es, oc, os;
      trig4(-2.f * df, g4, ec, es);
      trig4(-2.f * df, g4 + 0.5f, oc, os);
      const float ra = __builtin_amdgcn_cosf(-32.f * df), rb_ = __builtin_amdgcn_sinf(-32.f * df);
#pragma unroll
      for (int ss = 0; ss < 4; ++ss) {
        ga[ss] = u32x4{pk2(ec[0], es[0]), pk2(ec[1], es[1]), pk2(ec[2], es[2]), pk2(ec[3], es[3])};
        ga[8 + ss] = u32x4{pk2(oc[0], os[0]), pk2(oc[1], os[1]), pk2(oc[2], os[2]), pk2(oc[3], os[3])};
        PIN(ga[ss]); PIN(ga[8 + ss]);
        if (ss < 3) { crot(ec, es, ra, rb_); crot(oc, os, ra, rb_); }
      }
    };
    const int gr = 8 * ct + 2 * wb + (g & 1);
    unsigned gxp = gr * 256 + ((c ^ (gr & 15)) << 4);
    PIN(gxp);
    u32x4 gx[4];
    u64 epoch;
    asm volatile("s_load_dwordx2 %0, %1, 0x0\n\ts_waitcnt lgkmcnt(0)" : "=s"(epoch) : "s"(ws) : "memory");
    const u64 tag = TAG_OF(epoch);
    VMW(12); BAR();
    __builtin_amdgcn_s_setprio(3);
    gx[0] = ld16(gxp, 0);
    issue_x(2); issue_x(3);
#pragma unroll
    for (int i = 0; i < 2; ++i) {
      const unsigned ta = tab(2 * (16 * i + c)), tc = tab(32 * (wb >> 1) + 8 * (c >> 2) + 4 * i + (c & 3));
      mac[i] = *LP(u32x4, ta); mas[i] = *LP(u32x4, ta ^ 128u);
      mcc[i][0] = *LP(u32x4, tc); mcc[i][1] = *LP(u32x4, tc ^ 64u); mcs[i][0] = *LP(u32x4, tc ^ 128u); mcs[i][1] = *LP(u32x4, tc ^ 192u);
    }
    asm volatile("s_waitcnt vmcnt(8)" : "+v"(gw[0]), "+v"(gw[1]), "+v"(gw[2]), "+v"(gw[3]) :: "memory");
    BAR();
    gx[1] = ld16(gxp, 16384);
    issue_kv(1); issue_kv(2);
    VMW(12); BAR();
    gx[2] = ld16(gxp, 32768);
    issue_kv(3); issue_kv(4);
    VMW(16); BAR();
    gx[3] = ld16(gxp, 49152);
    issue_kv(5); issue_kv(6);
    VMW(16); BAR();
    issue_kv(7); issue_q(0);
    unsigned pfw;
    asm volatile("global_load_dword %0, %1, %2" : "=v"(pfw) : "v"((lane & 31) * 128), "s"(wo + idx * 16384 + wb * 4096) : "memory");
    VMW(17); BAR();
    issue_q(1); issue_q(2);
    VMW(13); BAR();
    issue_q(3);
#pragma unroll
    for (int i = 0; i < 4; ++i) {
      const int r = 4 * i + g, sl = c ^ ((r & 7) << 1);
      vx[i] = (unsigned)((16 * wb + r) * 1024 + (sl >> 3) * 512 + ((sl & 7) << 4) + 4096 - i * 1024);
    }
    if ((ct | mt) != 0) dma4(vx, wo - 4096 + cb * 128 + (i8 & 7) * 65536, dst0);
    ST(1);
    {
      float s = 0.f;
#pragma unroll
      for (int i = 0; i < 4; ++i)
#pragma unroll
        for (int j = 0; j < 4; ++j) s += bflo(gx[i][j]) * bflo(gw[i][j]) + bfhi(gx[i][j]) * bfhi(gw[i][j]);
      s = row_sum16(s);
      if ((lane & 47) == 0) ST32(__float_as_uint(__builtin_amdgcn_rcpf(1.f + __expf(-s))), WS_G + (64 * mt + gr) * 4);
    }
    asm volatile("s_waitcnt vmcnt(0)" : "+v"(pfw) :: "memory");
    SIG(E_QL);
    ev_wait<15>(E_G);
    dma4(vx, wo - 4096 + cb * 128 + ((1 + i8) & 7) * 65536, dst0 + 16384);
    dma4(vx, wo - 4096 + cb * 128 + ((2 + i8) & 7) * 65536, dst0 + 32768);
    const int k = 32 * ct + mt;
    unsigned o_x = (unsigned)(WS_XT + k * KS + bt * 16), o_w = (unsigned)(WS_WT + k * KS + bt * 32);
    unsigned l_xw = L_SCR + (bt >> 4) * 256 + ((c ^ (bt >> 4)) << 4);
    unsigned l_ca[4];
#pragma unroll
    for (int i = 0; i < 4; ++i) { l_ca[i] = L_SCR + c * 256 + (((4 * i + g) ^ c) << 4); PIN(l_ca[i]); }
    unsigned l_ew = L_SCR + 8192 + (wb >> 1) * 4096 + lane * 16 + (wb & 1) * 8, l_er = L_SCR + 8192 + (wb & 1) * 2048 + lane * 16;
    unsigned o_z = (unsigned)(WS_Z + k * KS + (2 * (16 * (wb & 1) + c) + (wb >> 1)) * 128 + g * 16);
    PIN(o_x); PIN(o_w); PIN(l_xw); PIN(l_ew); PIN(l_er); PIN(o_z);
    ST(2);
    if (wb == 0) { poll_all(ws, LN_FB + ct * 32 + (lane & 31), tag, 32); SIG(E_FB); }
    else ev_wait<1>(E_FB);
    __builtin_amdgcn_s_setprio(2);
    ST(3);
    {
      const u32x4 x0 = LD128(o_x), x1 = LD128(o_x + 4096);
      const u32x4 wv[2] = {LD128(o_w), LD128(o_w + 16)};
      u32x4 ar, ai, an;
      u32x4 ca[2][2][2];
      auto twiddle_dft32 = [&](const f32x4 (&yr)[2], const f32x4 (&yi)[2], f32x4 (&xr)[2], f32x4 (&xi)[2]) {
        const f32x4 r0 = yr[0] * w1c[0] + yi[0] * w1s[0], r1 = yr[1] * w1c[1] + yi[1] * w1s[1];
        const u32x4 br = pack8(r0, r1), bn = pack8(-r0, -r1);
        const u32x4 bi = pack8(yi[0] * w1c[0] - yr[0] * w1s[0], yi[1] * w1c[1] - yr[1] * w1s[1]);
#pragma unroll
        for (int ut = 0; ut < 2; ++ut) {
          xr[ut] = mfma32(mas[ut], bi, mfma32(mac[ut], br, zero));
          xi[ut] = mfma32(mas[ut], bn, mfma32(mac[ut], bi, zero));
        }
      };
      if (k != 0) {
        *LP(u32x4, l_xw) = x0; *(LAS u32x4*)(LP(char, l_xw) + 4096) = x1;
        SIG(E_X1);
        f32x4 fr[2] = {lo4(wv[0]), lo4(wv[1])}, fi[2] = {hi4(wv[0]), hi4(wv[1])};
        PIN(fr[0]); PIN(fr[1]); PIN(fi[0]); PIN(fi[1]);
        ST(4);
        ev_wait<15>(E_X1);
#pragma unroll
        for (int i = 0; i < 8; ++i) ca[i >> 2][(i >> 1) & 1][i & 1] = ld16(l_ca[i >> 1], (i & 1) * 4096);
        f32x4 yr[2] = {zero, zero}, yi[2] = {zero, zero}, xr[2], xi[2];
#pragma unroll
        for (int ks = 0; ks < 2; ++ks)
#pragma unroll
          for (int tt = 0; tt < 2; ++tt) { yr[tt] = mfma32(ca[0][ks][tt], m0c[ks], yr[tt]); yi[tt] = mfma32(ca[0][ks][tt], m0n[ks], yi[tt]); }
#pragma unroll
        for (int ks = 0; ks < 2; ++ks)
#pragma unroll
          for (int tt = 0; tt < 2; ++tt) { yr[tt] = mfma32(ca[1][ks][tt], m0s[ks], yr[tt]); yi[tt] = mfma32(ca[1][ks][tt], m0c[ks], yi[tt]); }
        twiddle_dft32(yr, yi, xr, xi);
        const f32x4 i0 = xr[0] * fi[0] + xi[0] * fr[0], i1 = xr[1] * fi[1] + xi[1] * fr[1];
        ar = pack8(xr[0] * fr[0] - xi[0] * fi[0], xr[1] * fr[1] - xi[1] * fi[1]);
        ai = pack8(i0, i1); an = pack8(-i0, -i1);
      } else {
        const u32x4 wn[2] = {LD128(o_w + 256 * KS), LD128(o_w + 256 * KS + 16)};
        unsigned mp[8];
#pragma unroll
        for (int e = 0; e < 8; ++e) { mp[e] = 4 * wt_pos((2048 - (16 * wb + c + 64 * (16 * (e >> 2) + 4 * g + (e & 3)))) & 2047); PIN(mp[e]); }
        *LP(u32x4, l_xw) = x0; *(LAS u32x4*)(LP(char, l_xw) + 4096) = x1;
        LAS u32x4* d = (LAS u32x4*)sm + bt * 2;
        d[0] = wv[0]; d[1] = wv[1]; d[512] = wn[0]; d[513] = wn[1];
        SIG(E_X1);
        ev_wait<15>(E_X1);
#pragma unroll
        for (int i = 0; i < 8; ++i) ca[i >> 2][(i >> 1) & 1][i & 1] = ld16(l_ca[i >> 1], (i & 1) * 4096);
        u32x4 m0[2], m1[2];
#pragma unroll
        for (int e = 0; e < 8; ++e) { m0[e >> 2][e & 3] = *LP(unsigned, mp[e]); m1[e >> 2][e & 3] = *(LAS unsigned*)(LP(char, mp[e]) + 8192); }
        f32x4 yr[2] = {zero, zero}, yi[2] = {zero, zero}, xr[2], xi[2], dr[2], di[2];
#pragma unroll
        for (int ks = 0; ks < 2; ++ks)
#pragma unroll
          for (int tt = 0; tt < 2; ++tt) { yr[tt] = mfma32(ca[0][ks][tt], m0c[ks], yr[tt]); yi[tt] = mfma32(ca[0][ks][tt], m0n[ks], yi[tt]); }
        twiddle_dft32(yr, yi, xr, xi);
#pragma unroll
        for (int ut = 0; ut < 2; ++ut) {
          const f32x4 fr = lo4(wv[ut]) + lo4(m0[ut]), fi = hi4(wv[ut]) - hi4(m0[ut]);
          dr[ut] = xr[ut] * fr - xi[ut] * fi; di[ut] = xr[ut] * fi + xi[ut] * fr;
          yr[ut] = zero; yi[ut] = zero;
        }
#pragma unroll
        for (int ks = 0; ks < 2; ++ks)
#pragma unroll
          for (int tt = 0; tt < 2; ++tt) { yr[tt] = mfma32(ca[1][ks][tt], m0s[ks], yr[tt]); yi[tt] = mfma32(ca[1][ks][tt], m0c[ks], yi[tt]); }
        twiddle_dft32(yr, yi, xr, xi);
#pragma unroll
        for (int ut = 0; ut < 2; ++ut) {
          const f32x4 fr = lo4(wn[ut]) + lo4(m1[ut]), fi = hi4(wn[ut]) - hi4(m1[ut]);
          dr[ut] += xr[ut] * fr - xi[ut] * fi; di[ut] += xr[ut] * fi + xi[ut] * fr;
        }
        ar = pack8(dr[0], dr[1]); ai = pack8(di[0], di[1]); an = pack8(-di[0], -di[1]);
      }
      ST(5);
#pragma unroll
      for (int tt = 0; tt < 2; ++tt) {
        const f32x4 vr = mfma32(an, mas[tt], mfma32(ar, mac[tt], zero));
        const f32x4 vi = mfma32(ar, mas[tt], mfma32(ai, mac[tt], zero));
        const f32x4 pr = vr * w2c[tt] - vi * w2s[tt], pi = vi * w2c[tt] + vr * w2s[tt];
        *(LAS u32x2*)(LP(char, l_ew) + tt * 2048) = u32x2{pk2(pr[0], pr[1]), pk2(pr[2], pr[3])};
        *(LAS u32x2*)(LP(char, l_ew) + tt * 2048 + 1024) = u32x2{pk2(pi[0], pi[1]), pk2(pi[2], pi[3])};
      }
      SIG(E_X2);
      ev_wait<15>(E_X2);
      f32x4 zr[2] = {zero, zero}, zi[2] = {zero, zero}, zs[2] = {zero, zero};
#pragma unroll
      for (int ks = 0; ks < 2; ++ks) {
        const u32x4 vr = ld16(l_er, ks * 4096), vi = ld16(l_er, ks * 4096 + 1024);
#pragma unroll
        for (int jt = 0; jt < 2; ++jt) {
          zr[jt] = mfma32(mcc[jt][ks], vr, zr[jt]); zi[jt] = mfma32(mcs[jt][ks], vr, zi[jt]);
          zs[jt] = mfma32(mcs[jt][ks], vi, zs[jt]); zi[jt] = mfma32(mcc[jt][ks], vi, zi[jt]);
        }
      }
      ST128(pack8(zr[0] - zs[0], zr[1] - zs[1]), o_z);
      ST128(pack8(zi[0], zi[1]), o_z + 64);
    }
    __builtin_amdgcn_s_setprio(0);
    ST(6);
    {
      make_g();
      const unsigned sel = (c & 1) ? 0x01000302u : 0x03020100u;
      const unsigned sgn = (c & 3) == 0 ? 0u : (c & 3) == 1 ? 0x80000000u : (c & 3) == 2 ? 0x80008000u : 0x00008000u;
      auto g_rest = [&]() {
#pragma unroll
        for (int f = 0; f < 8; ++f) {
          const int src = (f >> 2) * 8 + (f & 3);
#pragma unroll
          for (int i = 0; i < 4; ++i) ga[src + 4][i] = __builtin_amdgcn_perm(0u, ga[src][i], sel) ^ sgn;
          PIN(ga[src + 4]);
        }
        if (g == 0) ga[0][0] = (c & 1) ? 0xBE803E80u : 0x3E803E80u;
        PIN(ga[0]);
      };
      SB();
      DRAIN();
      SIG(E_ZD);
      if (wb == 0) {
        ev_wait<15>(E_ZD);
        if (lane < REP) __hip_atomic_fetch_add((GAS u64*)(ws + (size_t)(LN_CZ + lane * 16 + 2 * ct + (mt >> 4)) * 128), 1ull, RLX_AGENT);
      }
      ST(7);
      g_rest();
    }
    unsigned o_zl = (unsigned)(WS_Z + (64 * wb + (lane >> 3)) * KS + rb * 128 + (lane & 7) * 16);
    unsigned l_zw, l_z0, l_z1;
    {
      const int kk = 64 * wb + (lane >> 3), R = (((kk & 1) * 128 + (kk >> 1)) << 1) + ((lane >> 2) & 1);
      l_zw = R * 64 + ((lane & 3) << 4);
      l_z0 = (8 * g + (c >> 2)) * 64 + 8 * (c & 3) + ((g & 1) << 5);
      l_z1 = l_z0 ^ 32u;
    }
    unsigned l_zw1 = l_zw ^ 32u, o_out[2], l_a = 98304u + wb * 8192 + lane * 16;
#pragma unroll
    for (int tt = 0; tt < 2; ++tt) { o_out[tt] = (unsigned)(((rb >> 1) + 32 * (32 * (rb & 1) + 16 * tt + c)) * 1024 + (64 * cb + 16 * wb + 4 * g) * 2); PIN(o_out[tt]); }
    PIN(l_a);
    PIN(o_zl); PIN(l_zw); PIN(l_zw1); PIN(l_z0); PIN(l_z1);
    __builtin_amdgcn_s_setprio(BPRIO);
    ST(11);
    poll_all(ws, LN_CZ + (bid % REP) * 16 + 4 * wb + lane, tag + 16, 4);
    ST(8);
    {
      u32x4 z[8];
#pragma unroll
      for (int i = 0; i < 8; ++i) z[i] = __builtin_amdgcn_raw_buffer_load_b128(rs, o_zl, i * 8 * KS, 16);
      ev_wait<15>(E_WR);
#pragma unroll
      for (int i = 0; i < 8; ++i) *(LAS u32x4*)(LP(char, (i & 1) ? l_zw1 : l_zw) + i * 512) = z[i];
    }
    SIG(E_ZI);
    ST(9);
    ev_wait<15>(E_ZI);
    f32x4 acc[2][2] = {{zero, zero}, {zero, zero}};
    {
      u32x4 zf[3][2][2];
      auto ldz = [&](int n) {
#pragma unroll
        for (int i = 0; i < 2; ++i) { zf[n % 3][i][0] = trfrag(l_z0, (2 * n + i) * 2048, 256); zf[n % 3][i][1] = trfrag(l_z1, (2 * n + i) * 2048, 256); }
      };
      ldz(0); ldz(1);
      SB();
#pragma unroll
      for (int n = 0; n < 8; ++n) {
        if (n < 6) ldz(n + 2);
#pragma unroll
        for (int i = 0; i < 2; ++i) {
          acc[n >> 2][0] = mfma32(ga[2 * n + i], zf[n % 3][i][0], acc[n >> 2][0]);
          acc[n >> 2][1] = mfma32(ga[2 * n + i], zf[n % 3][i][1], acc[n >> 2][1]);
        }
#pragma unroll
        for (int i = 0; i < 4; ++i) { __builtin_amdgcn_sched_group_barrier(0x100, 2, 0); __builtin_amdgcn_sched_group_barrier(0x8, 1, 0); }
        SB();
      }
    }
    ST(10);
    {
      const volatile LAS unsigned* fp = (const volatile LAS unsigned*)(size_t)(L_SYNC + E_A * 16 + wb * 4);
      while (!__builtin_amdgcn_readfirstlane(*fp)) __builtin_amdgcn_s_sleep(1);
      CFENCE();
      const f32x2 sc = *(const LAS f32x2*)(LP(char, l_a) + 4096);
#pragma unroll
      for (int tt = 0; tt < 2; ++tt)
#pragma unroll
        for (int hf = 0; hf < 2; ++hf) {
          const f32x4 av = __builtin_bit_cast(f32x4, ld16(l_a, (hf * 2 + tt) * 1024));
          const f32x4 r = av + (hf ? acc[0][tt] - acc[1][tt] : acc[0][tt] + acc[1][tt]) * sc[tt];
          *(u32x2*)(out + o_out[tt] + 512 * hf) = u32x2{pk2(r[0], r[1]), pk2(r[2], r[3])};
        }
    }
    ST(13);
  } else {
    __builtin_amdgcn_s_setprio(1);
    u32x4 wrv, wiv;
    unsigned nyq = 0, nyq2 = 0, pf = 0;
    {
      const unsigned o_w = (unsigned)(wt_row(64 * mt + (tid >> 2)) * 257 + 32 * ct + 8 * (tid & 3)) * 2u;
      wrv = *(const u32x4u*)(w_r + o_w);
      wiv = *(const u32x4u*)(w_i + o_w);
      if (ct == 0 && w == 0) {
        const unsigned o_ny = (unsigned)(wt_row(64 * mt + lane) * 257 + 256) * 2u;
        nyq = *(const unsigned short*)(w_r + o_ny); nyq2 = *(const unsigned short*)(w_i + o_ny);
      }
    }
    if (w == 2 && lane < CODE_LINES) pf = *(const GAS unsigned*)(pc0 + min((idx * CODE_LINES + lane) * 128, CODE_MAX));
    const u32x2 ev = *(const u32x2*)ws;
    const int rec = (2 * ct + (w >> 1)) * 64 + 2 * mt + (w & 1);
    unsigned o_kp = (unsigned)(WS_KVP + rec * RS + lane * 16), o_zp = (unsigned)(WS_ZP + rec * 64 + g * 16);
    ST128((u32x4{~0u, ~0u, ~0u, ~0u}), o_kp);
    if (c == 0) ST128((u32x4{~0u, ~0u, ~0u, ~0u}), o_zp);
    SB();
    const int rh = w & 1, kt = w >> 1;
    const int kf = 32 * ct + 16 * kt + c;
    u32x4 xf[4][4][2];
    unsigned bb[2][4];
    {
      const unsigned fl = (unsigned)((c >> 2) | ((g & 1) << 2));
      const unsigned bu = (unsigned)((8 * g + (c >> 2)) * 256 + 8 * (c & 3)) + (((unsigned)(2 * kt) ^ fl) << 5);
#pragma unroll
      for (int u = 0; u < 4; ++u) {
        bb[0][u] = bu ^ (unsigned)((u & 1) * 32 + (u >> 1) * 128);
        bb[1][u] = bb[0][u] + 65536u;
      }
    }
    u32x4 fb[3][4];
    auto ldf = [&](int n) {
#pragma unroll
      for (int u = 0; u < 4; ++u) {
        const int slot = (4 + (n >> 1)) & 7;
        fb[n % 3][u] = trfrag(bb[slot >> 2][u], (slot & 3) * 16384 + (n & 1) * 8192, 1024);
      }
    };
    u64 tag;
    {
      u32x4 fc[4], fs[4];
      {
        const float kk = (float)kf * (1.f / 512.f);
        const float a0 = __builtin_amdgcn_fractf(kk * (float)(8 * g + 128 * ((c0 + 1) & 3)));
#pragma unroll
        for (int ks = 0; ks < 4; ++ks) {
          const float ak = __builtin_fmaf(kk, (float)(((ks & 2) ^ (flip << 1)) * 32), a0);
#pragma unroll
          for (int i = 0; i < 4; ++i) {
            const float p0 = __builtin_fmaf(kk, (float)(32 * (ks & 1) + 2 * i), ak), p1 = __builtin_fmaf(kk, (float)(32 * (ks & 1) + 2 * i + 1), ak);
            fc[ks][i] = pk2(__builtin_amdgcn_cosf(p0), __builtin_amdgcn_cosf(p1));
            fs[ks][i] = kf == 0 ? 0xBF803F80u
                                : pk2(__builtin_amdgcn_sinf(-p0), __builtin_amdgcn_sinf(-p1));
          }
          PIN(fc[ks]); PIN(fs[ks]);
        }
      }
      float ra = (c & 1) ? 0.f : ((c & 2) ? -1.f : 1.f), rbi = (c & 1) ? ((c & 2) ? 1.f : -1.f) : 0.f;
      unsigned l_sc = L_SCR + ((tid & 3) * 512 + ((tid >> 2) ^ ((tid & 3) << 3))) * 4;
      unsigned l_rd = L_SCR + (tid >> 4) * 256 + (((tid & 15) ^ ((tid >> 7) << 1)) << 4);
      unsigned o_wt = (unsigned)(WS_WT + (32 * ct + (tid >> 4)) * KS + (64 * mt + 4 * (tid & 15)) * 4);
      unsigned o_xt = (unsigned)(WS_XT + kf * KS + mt * 256 + rh * 64 + g * 16);
      unsigned xa[4];
#pragma unroll
      for (int ks = 0; ks < 4; ++ks) { xa[ks] = rh * 8192 + c * 256 + (((4 * ks + g) ^ (flip << 3) ^ c) << 4); PIN(xa[ks]); }
#pragma unroll
      for (int u = 0; u < 4; ++u) { PIN(bb[0][u]); PIN(bb[1][u]); }
      PIN(l_sc); PIN(l_rd); PIN(o_wt); PIN(o_xt); PIN(ra); PIN(rbi); PIN(o_kp); PIN(o_zp);
      ST(1);
      f32x4 xr[4][2], xi[4][2];
      auto ldx = [&](int s) {
#pragma unroll
        for (int ks = 0; ks < 4; ++ks)
#pragma unroll
          for (int tt = 0; tt < 2; ++tt) xf[s][ks][tt] = ld16(xa[ks], s * 16384 + tt * 4096);
      };
      auto mmf = [&](int s) {
#pragma unroll
        for (int tt = 0; tt < 2; ++tt) { xr[s][tt] = zero; xi[s][tt] = zero; }
#pragma unroll
        for (int ks = 0; ks < 4; ++ks)
#pragma unroll
          for (int tt = 0; tt < 2; ++tt) {
            xr[s][tt] = mfma32(xf[s][ks][tt], fc[ks], xr[s][tt]);
            xi[s][tt] = mfma32(xf[s][ks][tt], fs[ks], xi[s][tt]);
          }
      };
      auto horner = [&](int s) {
#pragma unroll
        for (int tt = 0; tt < 2; ++tt) {
          xr[s][tt] = (xr[s][tt] + xr[s - 1][tt] * ra) - xi[s - 1][tt] * rbi;
          xi[s][tt] = (xi[s][tt] + xr[s - 1][tt] * rbi) + xi[s - 1][tt] * ra;
        }
      };
      auto mixv = [&]() {
#pragma unroll
        for (int i = 0; i < 16; ++i) { __builtin_amdgcn_sched_group_barrier(0x8, 1, 0); __builtin_amdgcn_sched_group_barrier(0x2 | 0x80 | 0x40, 2, 0); }
        SB();
      };
      BAR();
      ldx(0);
      {
        PIN(pf); PIN(nyq); PIN(nyq2);
        tag = TAG_OF(((u64)(unsigned)__builtin_amdgcn_readfirstlane(ev.y) << 32) | (unsigned)__builtin_amdgcn_readfirstlane(ev.x));
        LAS unsigned* sc = LP(unsigned, l_sc);
#pragma unroll
        for (int j = 0; j < 4; ++j) {
          sc[(2 * j) * 64] = __builtin_amdgcn_perm(wiv[j], wrv[j], 0x05040100u);
          sc[(2 * j + 1) * 64] = __builtin_amdgcn_perm(wiv[j], wrv[j], 0x07060302u);
        }
        if (ct == 0 && w == 0) ST32(nyq | (nyq2 << 16), WS_WT + 256 * KS + (64 * mt + lane) * 4);
        if (mt == 0 && w == 1 && lane < 4 * REP)
          put_flag(ws, LN_CZ + (lane >> 1) * 16 + 2 * ct + (lane & 1), tag);
      }
      BAR();
      const u32x4 wt0 = ld16(l_rd, 0), wt1 = ld16(l_rd ^ 64u, 4096);
      ldx(1); SB(); mmf(0);
      BAR();
      ldx(2); SB(); mmf(1);
      BAR();
      ldx(3); SB();
      mmf(2); horner(1);
      ST128(wt0, o_wt); ST128(wt1, o_wt + 16 * KS);
      mixv();
      mmf(3); horner(2);
      mixv();
      ldf(0); ldf(1);
      horner(3);
      ST128(pack8(xr[3][0], xr[3][1]), o_xt);
      ST128(pack8(xi[3][0], xi[3][1]), o_xt + 128);
    }
    ST(2);
    auto mix = [&]() {
#pragma unroll
      for (int i = 0; i < 8; ++i) { __builtin_amdgcn_sched_group_barrier(0x100, 1, 0); __builtin_amdgcn_sched_group_barrier(0x8, 1, 0); }
      SB();
    };
    {
      f32x4 acc[2][4];
#pragma unroll
      for (int t = 0; t < 2; ++t)
#pragma unroll
        for (int u = 0; u < 4; ++u) acc[t][u] = zero;
#pragma unroll
      for (int n = 0; n < 16; ++n) {
        if (n == FBN) DRAIN();
        if (n == 0 || n == 4 || n == 8) BAR();
        if (n == FBN) { if (tid == 0) put_flag(ws, LN_FB + ct * 32 + mt, tag); SB(); }
        if (n == 12) { SIG(E_G); SB(); }
        if (n < 14) ldf(n + 2);
#pragma unroll
        for (int u = 0; u < 4; ++u)
#pragma unroll
          for (int t = 0; t < 2; ++t) acc[t][u] = mfma32(xf[n >> 2][n & 3][t], fb[n % 3][u], acc[t][u]);
        mix();
      }
      ST(3);
      const u32x4 ones = {0x3f803f80u, 0x3f803f80u, 0x3f803f80u, 0x3f803f80u};
      f32x4 kv[2], zz[2];
#pragma unroll
      for (int n = 0; n < 2; ++n) {
        const u32x4 pk = pack8(phi4(acc[0][n]), phi4(acc[1][n]));
        kv[n] = mfma32(pk, pack8(acc[0][2 + n], acc[1][2 + n]), zero);
        zz[n] = mfma32(pk, ones, zero);
      }
      u32x4 kvo = pack8(kv[0], kv[1]), zo = pack8(zz[0], zz[1]);
#pragma unroll
      for (int e = 0; e < 4; ++e) {
        kvo[e] = min(kvo[e], 0xfffffffeu);
        zo[e] = min(zo[e], 0xfffffffeu);
      }
      ST128(kvo, o_kp);
      if (c == 0) ST128(zo, o_zp);
    }
    ST(4);
    {
      const int hp = 2 * ct + (mt >> 4), sub = mt & 15;
      u32x4 pv[4];
      unsigned pz;
      auto ldp = [&]() {
#pragma unroll
        for (int i = 0; i < 4; ++i)
          pv[i] = __builtin_amdgcn_raw_buffer_load_b128(rs, WS_KVP + (hp * 64 + (lane >> 2)) * RS + sub * 64 + (lane & 3) * 16, i * 16 * RS, 16);
        pz = LD32(WS_ZP + (hp * 64 + lane) * 64 + sub * 4);
      };
      f32x4 acc[2][2] = {{zero, zero}, {zero, zero}};
      ev_wait<15>(E_QL);
      SB();
      ldf(0); ldf(1);
      SB();
#pragma unroll
      for (int n = 0; n < 8; ++n) {
        if (n < 6) ldf(n + 2);
        if (n == 6) { SIG(E_QR); SB(); }
        if (n == LDPN) {
          if (w == 0) { ev_wait<1>(E_FB); ldp(); }
          SB();
        }
#pragma unroll
        for (int u = 0; u < 4; ++u)
#pragma unroll
          for (int t = 0; t < 2; ++t) acc[u & 1][t] = mfma32(fb[n % 3][u], xf[n >> 1][2 * (u >> 1) + (n & 1)][t], acc[u & 1][t]);
        mix();
      }
      auto st_pq = [&]() {
#pragma unroll
        for (int n = 0; n < 2; ++n)
          ST128(pack8(phi4(acc[n][0]), phi4(acc[n][1])), WS_PQ + ((2 * mt + rh) * 32 + 4 * ct + 2 * kt + n) * 1024 + lane * 16);
        SB();
      };
      st_pq();
      ST(5);
      if (w == 0) {
        for (unsigned spins = 0; spins < SPIN_CAP; ++spins) {
          unsigned m = pz;
#pragma unroll
          for (int i = 0; i < 4; ++i) m = max(max(m, max(pv[i][0], pv[i][1])), max(pv[i][2], pv[i][3]));
          if (__all(m != ~0u)) break;
          CFENCE();
          ldp();
        }
        ST(6);
        const unsigned one = (c & 1) ? 0x3f800000u : 0x00003f80u;
        const u32x4 sel = {(c >> 1) == 0 ? one : 0u, (c >> 1) == 1 ? one : 0u, (c >> 1) == 2 ? one : 0u, (c >> 1) == 3 ? one : 0u};
        f32x4 s = zero;
#pragma unroll
        for (int i = 0; i < 4; ++i) s = mfma32(sel, pv[i], s);
        const f32x4 zq = mfma32(sel, u32x4{pz, 0u, 0u, 0u}, zero);
#pragma unroll
        for (int e = 0; e < 4; ++e) s[e] = dpp_add<0x128>(dpp_add<0x124>(s[e]));
        const float z0 = row_sum16(zq[0]), z1 = row_sum16(zq[1]);
        if ((lane & 44) == 0) ST64((u32x2{pk2(s[0], s[1]), pk2(s[2], s[3])}), WS_KVF + hp * 1024 + (sub * 4 + c) * 16 + g * 8);
        if (lane == 0) ST32(pk2(z0, z1), WS_ZF + hp * 64 + sub * 4);
        SB();
      }
      if (w == 0) {
        VMW(2);
        ev_wait<14>(E_PQ);
        if (lane == 0) put_flag(ws, LN_FQ + mt * 8 + ct, tag);
        VMW(1);
        if (lane < REP) __hip_atomic_fetch_add((GAS u64*)(ws + (size_t)(LN_CK + lane * 16 + 2 * ct + (mt >> 4)) * 128), 1ull, RLX_AGENT);
        SIG(E_KA);
      } else {
        unsigned voff[4];
#pragma unroll
        for (int i = 0; i < 4; ++i) {
          const int r = 4 * i + g, sl = c ^ ((r & 7) << 1);
          voff[i] = (unsigned)(r * 1024 + (sl >> 3) * 512 + ((sl & 7) << 4) + 4096 - i * 1024);
        }
        ev_wait<15>(E_QR);
#pragma unroll 1
        for (int gj = 11 + w; gj < ((ct | mt) == 0 ? 36 : 32); gj += 3) {
          const int gi = gj & 31;
          if (gj >= 32 && (ct | mt) == 0) ev_wait<15>(E_X2);
          dma4(voff, wo - 4096 + cb * 128 + ((gi + 4 * i8) & 31) * 16384, gi * 4096);
          VMW(WO_THR);
          if (gj < 15) { VMW(4); SIG(E_PQ); }
        }
        DRAIN();
        SIG(E_WL);
      }
    }
    __builtin_amdgcn_s_setprio(APRIO);
    ST(7);
    u32x4 wa[16][2], pq[8], kf4[4], zf4[4];
    float gate[2];
    unsigned l_o[4];
#pragma unroll
    for (int hp = 0; hp < 4; ++hp) { l_o[hp] = 65536u + ((4 * w + hp - 2 * i8) & 15) * 2048 + lane * 16; PIN(l_o[hp]); }
    unsigned o_kf = (unsigned)(WS_KVF + 4 * w * 1024 + lane * 16), o_zf = (unsigned)(WS_ZF + 4 * w * 64 + g * 16), l_or = 65536u + lane * 16;
    unsigned o_pq = (unsigned)(WS_PQ + (rb * 32 + 8 * w) * 1024 + lane * 16), o_g = (unsigned)(WS_G + (32 * rb + c) * 4);
    unsigned o_fl = lane < 24 ? (unsigned)(lane < 16 ? LN_FQ + (rb >> 1) * 8 + (lane >> 1) : LN_CK + (bid % REP) * 16 + 4 * w + ((lane >> 1) & 3)) * 128u + (lane & 1) * 4
                              : (unsigned)(WS_END + 4096);
    unsigned want = (unsigned)((lane < 16 ? tag : tag + 16) >> ((lane & 1) * 32));
    PIN(o_kf); PIN(o_zf); PIN(l_or); PIN(o_pq); PIN(o_g); PIN(o_fl); PIN(want);
    {
      const unsigned f7 = (unsigned)((4 * g + (c >> 2)) & 7);
      unsigned pw[2][2];
#pragma unroll
      for (int hf = 0; hf < 2; ++hf) {
        pw[hf][0] = (4 * g + (c >> 2)) * 256 + 8 * (c & 3) + ((((unsigned)w + 4u * hf) ^ f7) << 5);
        pw[hf][1] = pw[hf][0] + 65536u;
        PIN(pw[hf][0]); PIN(pw[hf][1]);
      }
      ev_wait<15>(E_X1);
      ev_wait<14>(E_WL);
      unsigned fv = LOOK(o_fl);
      SB();
#pragma unroll
      for (int st = 0; st < 16; ++st)
#pragma unroll
        for (int hf = 0; hf < 2; ++hf) wa[st][hf] = trfrag(pw[hf][st >> 3], (st & 7) * 8192, 4096);
      SIG(E_WR);
      SB();
      ST(8);
      unsigned ok = (unsigned)__ballot(fv == want);
      for (unsigned spins = 0; (ok & 0xffffu) != 0xffffu && spins < SPIN_CAP; ++spins) {
        __builtin_amdgcn_s_sleep(POLL_SLEEP);
        ok = (unsigned)__ballot(LOOK(o_fl) == want);
      }
      CFENCE();
      fv = LOOK(((ok >> 16) & 0xffu) == 0xffu ? (unsigned)(WS_END + 4096) : o_fl);
      SB();
#pragma unroll
      for (int i = 0; i < 8; ++i) pq[i] = __builtin_amdgcn_raw_buffer_load_b128(rs, o_pq + (i & 3) * 1024, (i >> 2) * 4096, 16);
#pragma unroll
      for (int tt = 0; tt < 2; ++tt) gate[tt] = __uint_as_float(LD32(o_g + 64 * tt));
      SB();
      ST(9);
      if (((ok >> 16) & 0xffu) != 0xffu) {
        ok = (unsigned)__ballot(fv == want);
        for (unsigned spins = 0; ((ok >> 16) & 0xffu) != 0xffu && spins < SPIN_CAP; ++spins) ok = (unsigned)__ballot(LOOK(o_fl) == want);
      }
      CFENCE();
    }
    ST(10);
    {
#pragma unroll
      for (int i = 0; i < 4; ++i) { kf4[i] = LD128(o_kf + i * 1024); zf4[i] = LD128(o_zf + i * 64); }
      ev_wait<15>(E_WR);
      f32x4 num[2][2][2], den[2][2][2];
      auto mo = [&](int hp) {
#pragma unroll
        for (int tt = 0; tt < 2; ++tt)
#pragma unroll
          for (int n = 0; n < 2; ++n) {
            const u32x4 pp = pq[2 * hp + n];
            const u32x2 b = tt ? u32x2{pp.z, pp.w} : u32x2{pp.x, pp.y};
            const u32x2 ka = n ? u32x2{kf4[hp].z, kf4[hp].w} : u32x2{kf4[hp].x, kf4[hp].y};
            const u32x2 za = n ? u32x2{zf4[hp].z, zf4[hp].w} : u32x2{zf4[hp].x, zf4[hp].y};
            den[hp & 1][tt][n] = mfma16(za, b, f32x4{1e-6f, 1e-6f, 1e-6f, 1e-6f});
            num[hp & 1][tt][n] = mfma16(ka, b, zero);
          }
      };
      mo(0);
      SB();
#pragma unroll
      for (int hp = 0; hp < 4; ++hp) {
        if (hp < 3) mo(hp + 1);
        SB();
#pragma unroll
        for (int tt = 0; tt < 2; ++tt)
          *(LAS u32x4*)(LP(char, l_o[hp]) + tt * 1024) = pack8(num[hp & 1][tt][0] * __builtin_amdgcn_rcpf(den[hp & 1][tt][0][0]),
                                                               num[hp & 1][tt][1] * __builtin_amdgcn_rcpf(den[hp & 1][tt][1][0]));
        SB();
      }
    }
    SIG(E_O);
    ST(11);
    ev_wait<15>(E_O);
    f32x4 acc[2][2] = {{zero, zero}, {zero, zero}};
    {
#pragma unroll
      for (int st = 0; st < 16; ++st)
#pragma unroll
        for (int tt = 0; tt < 2; ++tt) {
          const u32x4 ob = ld16(l_or, (st * 2 + tt) * 1024);
          acc[0][tt] = mfma32(wa[st][0], ob, acc[0][tt]);
          acc[1][tt] = mfma32(wa[st][1], ob, acc[1][tt]);
        }
      __builtin_amdgcn_sched_group_barrier(0x100, 8, 0);
#pragma unroll
      for (int i = 0; i < 24; ++i) { __builtin_amdgcn_sched_group_barrier(0x8, 2, 0); __builtin_amdgcn_sched_group_barrier(0x100, 1, 0); }
      __builtin_amdgcn_sched_group_barrier(0x8, 16, 0);
    }
#pragma unroll
    for (int i = 0; i < 4; ++i) *(LAS f32x4*)(LP(char, l_or) + 32768 + w * 8192 + i * 1024) = acc[i >> 1][i & 1] * gate[i & 1];
    *(LAS f32x2*)(LP(char, l_or) + 32768 + w * 8192 + 4096) = f32x2{(1.f - gate[0]) * (2.f / (2048.f * 512.f)), (1.f - gate[1]) * (2.f / (2048.f * 512.f))};
    SIG(E_A);
    if (bid == 0 && tid == 0) *(u32x2*)ws = u32x2{ev.x + 1u, ev.y};
    ST(12);
  }
}

extern "C" void kernel_launch(void* const* d_in, const int* in_sizes, int n_in, void* d_out, int out_size,
                              void* d_ws, size_t ws_size, hipStream_t stream) {
  (void)hipFuncSetAttribute((const void*)fused_kernel, hipFuncAttributeMaxDynamicSharedMemorySize, LDS_BYTES);
  fused_kernel<<<256, 512, LDS_BYTES, stream>>>((const char*)d_in[0], (const char*)d_in[1], (const char*)d_in[2],
                                                (const char*)d_in[4], (const char*)d_in[5], (const char*)d_in[6],
                                                (char*)d_ws, (char*)d_out);
}
```

```cpp
#include <hip/hip_runtime.h>
#include <hip/hip_bf16.h>
#include <stdint.h>

typedef short s16x4 __attribute__((ext_vector_type(4)));
typedef short s16x8 __attribute__((ext_vector_type(8)));
typedef float f32x2 __attribute__((ext_vector_type(2)));
typedef float f32x4 __attribute__((ext_vector_type(4)));
typedef unsigned u32x2 __attribute__((ext_vector_type(2)));
typedef unsigned u32x4 __attribute__((ext_vector_type(4)));
typedef unsigned u32x4u __attribute__((ext_vector_type(4), aligned(2)));
typedef __bf16 bf16x2 __attribute__((ext_vector_type(2)));
typedef unsigned long long u64;
#define DI __device__ __forceinline__
#define LAS __attribute__((address_space(3)))
#define GAS __attribute__((address_space(1)))
#define RLX_AGENT __ATOMIC_RELAXED, __HIP_MEMORY_SCOPE_AGENT
#define CFENCE() asm volatile("" ::: "memory")
#define PIN(v) asm volatile("" : "+v"(v))
#define DRAIN() asm volatile("s_waitcnt vmcnt(0)" ::: "memory")
#define LGKM0() __builtin_amdgcn_s_waitcnt(0xC07F)
#define SB() __builtin_amdgcn_sched_barrier(0)
#define VMW_(n) asm volatile("s_waitcnt vmcnt(" #n ")" ::: "memory")
#define VMW(n) VMW_(n)
#define LP(T, a) ((LAS T*)(size_t)(a))
#define BAR() do { CFENCE(); SB(); LGKM0(); __builtin_amdgcn_s_barrier(); SB(); CFENCE(); } while (0)

DI unsigned pk2(float a, float b) {
  f32x2 v = {a, b};
  return __builtin_bit_cast(unsigned, __builtin_convertvector(v, bf16x2));
}
DI float bflo(unsigned u) { return __uint_as_float(u << 16); }
DI float bfhi(unsigned u) { return __uint_as_float(u & 0xffff0000u); }
DI f32x4 lo4(u32x4 u) { return f32x4{bflo(u.x), bflo(u.y), bflo(u.z), bflo(u.w)}; }
DI f32x4 hi4(u32x4 u) { return f32x4{bfhi(u.x), bfhi(u.y), bfhi(u.z), bfhi(u.w)}; }
DI f32x4 mfma32(u32x4 a, u32x4 b, f32x4 c) {
  return __builtin_amdgcn_mfma_f32_16x16x32_bf16(__builtin_bit_cast(s16x8, a), __builtin_bit_cast(s16x8, b), c, 0, 0, 0);
}
DI f32x4 mfma16(u32x2 a, u32x2 b, f32x4 c) {
  return __builtin_amdgcn_mfma_f32_16x16x16bf16_1k(__builtin_bit_cast(s16x4, a), __builtin_bit_cast(s16x4, b), c, 0, 0, 0);
}
DI u32x4 pack8(f32x4 a, f32x4 b) { return u32x4{pk2(a[0], a[1]), pk2(a[2], a[3]), pk2(b[0], b[1]), pk2(b[2], b[3])}; }
DI u32x4 neg8(u32x4 a) {
  PIN(a);
  a ^= 0x80008000u;
  PIN(a);
  return a;
}
DI f32x4 phi4(f32x4 t) {
  const f32x4 m = t * 1.4426950408889634f, r = t + 1e-6f;
  f32x4 e, q;
#pragma unroll
  for (int i = 0; i < 4; ++i) { e[i] = __builtin_amdgcn_exp2f(fminf(m[i], 0.f)); q[i] = fmaxf(r[i], 1e-6f); }
  return e + q;
}
DI void crot(f32x4& cr, f32x4& ci, float a, float b) {
  const f32x4 nr = cr * a - ci * b, ni = cr * b + ci * a;
  cr = nr; ci = ni;
}
DI u32x4 trfrag(unsigned a, int off, int hi_off) {
  s16x4 l = __builtin_amdgcn_ds_read_tr16_b64_v4i16((LAS s16x4*)(LP(char, a) + off));
  s16x4 h = __builtin_amdgcn_ds_read_tr16_b64_v4i16((LAS s16x4*)(LP(char, a) + off + hi_off));
  return __builtin_bit_cast(u32x4, __builtin_shufflevector(l, h, 0, 1, 2, 3, 4, 5, 6, 7));
}
DI u32x4 ld16(unsigned a, int off) { return *(const LAS u32x4*)(LP(char, a) + off); }
template <int CTRL> DI float dpp_add(float v) {
  return v + __int_as_float(__builtin_amdgcn_update_dpp(0, __float_as_int(v), CTRL, 0xf, 0xf, false));
}
DI float row_sum16(float v) { return dpp_add<0x128>(dpp_add<0x124>(dpp_add<0x4E>(dpp_add<0xB1>(v)))); }

constexpr int REP = 8;
constexpr int LN_FB = 8, LN_FQ = LN_FB + 256, LN_CZ = LN_FQ + 256, LN_CK = LN_CZ + 16 * REP;
constexpr int WS_XT = 160 * 1024;
constexpr int WS_WT = WS_XT + 256 * 8192;
constexpr int WS_KVP = WS_WT + 257 * 8192;
constexpr int WS_ZP = WS_KVP + 16 * 64 * 1024;
constexpr int WS_KVF = WS_ZP + 16 * 64 * 64;
constexpr int WS_ZF = WS_KVF + 16 * 1024;
constexpr int WS_G = WS_ZF + 2048;
constexpr int WS_PQ = WS_G + 8192;
constexpr int WS_Z = WS_PQ + 2048 * 1024;
constexpr int WS_END = WS_Z + 256 * 8192;
constexpr int L_SCR = 131072, L_SYNC = L_SCR + 32768 - 512, LDS_BYTES = 163840;
enum { E_QL, E_FB, E_X1, E_X2, E_ZD, E_FZ, E_ZI, E_QR, E_PQ, E_WL, E_WR, E_KA, E_FQ, E_FK, E_O, E_G, E_A, E_S1, E_XD };
constexpr unsigned SPIN_CAP = 1u << 16;
constexpr int POLL_SLEEP = 2;
constexpr int CODE_LINES = 8, CODE_MAX = 29952;
#define TAG_OF(e) ((e) * 0x9E3779B97F4A7C15ull + 0x632BE59BD9B4E019ull)

#define SIG(ev) do { CFENCE(); *(volatile LAS unsigned*)(LP(char, sigp) + (ev) * 16) = 1u; CFENCE(); } while (0)
template <unsigned MASK> DI void ev_wait(int ev) {
  const volatile LAS u32x4* p = (const volatile LAS u32x4*)(size_t)(L_SYNC + ev * 16);
  for (;;) {
    const u32x4 v = *p;
    const unsigned a = ((MASK & 1) ? v.x : 1u) & ((MASK & 2) ? v.y : 1u) & ((MASK & 4) ? v.z : 1u) & ((MASK & 8) ? v.w : 1u);
    if (__builtin_amdgcn_readfirstlane(a)) break;
    __builtin_amdgcn_s_sleep(1);
  }
  CFENCE();
}
DI void put_flag(char* ws, unsigned line, u64 v) { __hip_atomic_store((GAS u64*)(ws + (size_t)line * 128), v, RLX_AGENT); }

DI void dma4(const unsigned (&voff)[4], const char* sb, unsigned dst) {
  unsigned keep;
  asm volatile("s_mov_b32 %0, m0\n\ts_mov_b32 m0, %6\n\ts_nop 0\n\t"
               "global_load_lds_dwordx4 %1, %5\n\t"
               "global_load_lds_dwordx4 %2, %5 offset:1024\n\t"
               "global_load_lds_dwordx4 %3, %5 offset:2048\n\t"
               "global_load_lds_dwordx4 %4, %5 offset:3072\n\t"
               "s_mov_b32 m0, %0"
               : "=&s"(keep) : "v"(voff[0]), "v"(voff[1]), "v"(voff[2]), "v"(voff[3]), "s"(sb), "s"(dst) : "memory");
}
DI int wt_pos(int u) {
  const int k0 = u & 63, u1 = u >> 6;
  return (((k0 >> 4) * 64 + ((u1 >> 2) & 3) * 16 + (k0 & 15)) << 3) + (u1 >> 4) * 4 + (u1 & 3);
}
DI int wt_row(int pos) {
  const int bt = pos >> 3;
  return 16 * (bt >> 6) + (bt & 15) + 64 * (16 * ((pos >> 2) & 1) + 4 * ((bt >> 4) & 3) + (pos & 3));
}
DI void trig4(float f, float i0, f32x4& co, f32x4& si) {
#pragma unroll
  for (int e = 0; e < 4; ++e) {
    const float a = f * (i0 + (float)e);
    co[e] = __builtin_amdgcn_cosf(a); si[e] = __builtin_amdgcn_sinf(a);
  }
}
#define LOOK(off) ((unsigned)__builtin_amdgcn_raw_ptr_buffer_atomic_add_i32(0, rs, (off), 0, 16))
#define LD128(off) __builtin_amdgcn_raw_buffer_load_b128(rs, (off), 0, 16)
#define LD32(off) __builtin_amdgcn_raw_buffer_load_b32(rs, (off), 0, 16)
#define ST128(v, off) __builtin_amdgcn_raw_buffer_store_b128((v), rs, (off), 0, 16)
#define ST64(v, off) __builtin_amdgcn_raw_buffer_store_b64((v), rs, (off), 0, 16)
#define ST32(v, off) __builtin_amdgcn_raw_buffer_store_b32((v), rs, (off), 0, 16)

#define POLLN(line0, n, val) do { \
    const unsigned o_ = lane < 2 * (n) ? (unsigned)((line0) + (lane >> 1)) * 128u + (lane & 1) * 4 : (unsigned)(WS_END + 4096); \
    const unsigned w_ = lane < 2 * (n) ? (unsigned)((val) >> ((lane & 1) * 32)) : 0u; \
    for (unsigned spins_ = 0; spins_ < SPIN_CAP; ++spins_) { \
      if ((__ballot(LOOK(o_) == w_) & ((1ull << (2 * (n))) - 1)) == ((1ull << (2 * (n))) - 1)) break; \
      __builtin_amdgcn_s_sleep(POLL_SLEEP); \
    } \
    CFENCE(); } while (0)

__global__ __launch_bounds__(512, 2) void fused_kernel(const char* __restrict__ x, const char* __restrict__ wqkv,
                                                       const char* __restrict__ wo, const char* __restrict__ w_r,
                                                       const char* __restrict__ w_i, const char* __restrict__ gate_w,
                                                       char* __restrict__ ws, char* __restrict__ out) {
  extern __shared__ __attribute__((aligned(16))) char smem[];
  LAS char* sm = (LAS char*)smem;
  asm volatile("" :: "s"(x), "s"(wqkv), "s"(wo), "s"(w_r), "s"(w_i), "s"(gate_w), "s"(ws), "s"(out), "s"(sm));
  u64 rt0;
  asm volatile("s_memrealtime %0\n\ts_waitcnt lgkmcnt(0)" : "=s"(rt0) :: "memory");
  u64 pc0;
  asm volatile("s_getpc_b64 %0" : "=s"(pc0));
  pc0 &= ~127ull;
  const int tid = threadIdx.x, lane = tid & 63;
  const int w = __builtin_amdgcn_readfirstlane(tid >> 6);
  const int bid = blockIdx.x, xcd = bid & 7, idx = bid >> 3;
  const int mt = (xcd & 3) * 8 + (idx & 7), ct = (xcd >> 2) * 4 + (idx >> 3);
  const int rb = xcd * 8 + (idx & 7), cb = idx >> 3;
  const int c = lane & 15, g = lane >> 4;
  const __amdgpu_buffer_rsrc_t rs = __builtin_amdgcn_make_buffer_rsrc(ws, 0, WS_END, 0x00020000);
  const f32x4 zero = {0, 0, 0, 0};
  unsigned sigp = L_SYNC + (w & 3) * 4;
  PIN(sigp);
  const int i8 = idx & 7, c0 = (idx + (idx >> 3)) & 3, flip = (idx >> 2) & 1;
  if (w >= 4) {
    const int wb = w - 4, bt = tid - 256;
    unsigned vx[4], vk[4], vq[4];
    const unsigned dst0 = wb * 4096;
#pragma unroll
    for (int i = 0; i < 4; ++i) {
      const int r = 16 * wb + 4 * i + g;
      vx[i] = (unsigned)((mt + 32 * r) * 1024 + ((c ^ (r & 15)) << 4) + 4096 - i * 1024);
    }
    auto issue_x = [&](int i) { dma4(vx, x - 4096 + ((c0 - i) & 3) * 256, dst0 + i * 16384); };
    issue_x(0);
#pragma unroll
    for (int i = 0; i < 4; ++i) {
      const int r = 16 * wb + 4 * i + g, sl = c ^ ((g | ((i >> 1) << 2)) << 1);
      vk[i] = (unsigned)(r * 3072 + (sl >> 3) * 1024 + ((sl & 7) << 4) + 4096 - i * 1024);
    }
    auto issue_kv = [&](int j) {
      const int blk = 2 * ((c0 - (j >> 1)) & 3) + ((j & 1) ^ flip);
      dma4(vk, wqkv - 4096 + 1024 + ct * 128 + (unsigned)blk * (64 * 3072), dst0 + ((j + 4) & 7) * 16384);
    };
    u32x4 gw[4];
#pragma unroll
    for (int i = 0; i < 4; ++i)
      asm volatile("global_load_dwordx4 %0, %1, %2" : "=v"(gw[i]) : "v"(c * 16), "s"(gate_w + ((c0 - i) & 3) * 256) : "memory");
    issue_kv(0);
    issue_x(1);
    if (wb == 0) *(LAS u32x2*)(sm + L_SYNC + lane * 8) = u32x2{0u, 0u};
#pragma unroll
    for (int i = 0; i < 4; ++i) {
      const int r = 16 * wb + 4 * i + g, sl = c ^ ((g | ((i >> 1) << 2)) << 1);
      vq[i] = (unsigned)(r * 3072 + ((sl >> 3) ^ flip) * (64 * 3072) + ((sl & 7) << 4) + 4096 - i * 1024);
      PIN(vq[i]);
    }
    auto issue_q = [&](int j) { dma4(vq, wqkv - 4096 + ct * 128 + (unsigned)((c0 - j) & 3) * (128 * 3072), dst0 + (j + 4) * 16384); };
    const float g4 = (float)(4 * g);
    u32x4 m0c[2], m0s[2], m0n[2], mac[2], mas[2], mcc[2][2], mcs[2][2];
    f32x4 w1c[2], w1s[2], w2c[2], w2s[2];
    auto tab = [&](int row) { return (unsigned)(7 * 16384 + row * 256 + ((g ^ (row & 15)) << 4)); };
    {
      f32x4 c0v, s0v;
      trig4((float)(16 * wb + c) * (1.f / 64.f), g4, c0v, s0v);
      f32x4 c1v = c0v, s1v = s0v;
      crot(c1v, s1v, (c & 1) ? 0.f : ((c & 2) ? -1.f : 1.f), (c & 1) ? ((c & 2) ? -1.f : 1.f) : 0.f);
      const unsigned sg = (c & 1) ? 0x80008000u : 0u;
      m0c[0] = pack8(c0v, c1v); m0s[0] = pack8(s0v, s1v);
      PIN(m0c[0]); PIN(m0s[0]);
      m0c[1] = m0c[0] ^ sg; m0s[1] = m0s[0] ^ sg;
#pragma unroll
      for (int i = 0; i < 2; ++i) {
        m0n[i] = neg8(m0s[i]);
        *LP(u32x4, tab(16 * wb + c) ^ (64u * i)) = m0c[i];
        *LP(u32x4, tab(16 * wb + c) ^ (64u * i + 128u)) = m0s[i];
      }
    }
#pragma unroll
    for (int tt = 0; tt < 2; ++tt) {
      trig4((float)(16 * wb + c) * (1.f / 2048.f), g4 + 16.f * tt, w1c[tt], w1s[tt]);
      trig4((float)(16 * tt + c) * (1.f / 2048.f), g4 + 16.f * wb, w2c[tt], w2s[tt]);
      PIN(w1c[tt]); PIN(w1s[tt]); PIN(w2c[tt]); PIN(w2s[tt]);
    }
    u32x4 ga[16];
    auto make_g = [&]() {
      const int d = 64 * cb + 16 * wb + c;
      const float df = (float)d * (1.f / 512.f);
      f32x4 ec, es, oc, os;
      trig4(-2.f * df, g4, ec, es);
      trig4(-2.f * df, g4 + 0.5f, oc, os);
      const float ra = __builtin_amdgcn_cosf(-32.f * df), rb_ = __builtin_amdgcn_sinf(-32.f * df);
#pragma unroll
      for (int ss = 0; ss < 4; ++ss) {
        ga[ss] = u32x4{pk2(ec[0], es[0]), pk2(ec[1], es[1]), pk2(ec[2], es[2]), pk2(ec[3], es[3])};
        ga[8 + ss] = u32x4{pk2(oc[0], os[0]), pk2(oc[1], os[1]), pk2(oc[2], os[2]), pk2(oc[3], os[3])};
        PIN(ga[ss]); PIN(ga[8 + ss]);
        if (ss < 3) { crot(ec, es, ra, rb_); crot(oc, os, ra, rb_); }
      }
    };
    const int gr = 8 * ct + 2 * wb + (g & 1);
    unsigned gxp = gr * 256 + ((c ^ (gr & 15)) << 4);
    PIN(gxp);
    u32x4 gx[4];
    u64 epoch;
    asm volatile("s_load_dwordx2 %0, %1, 0x0\n\ts_waitcnt lgkmcnt(0)" : "=s"(epoch) : "s"(ws) : "memory");
    const u64 tag = TAG_OF(epoch);
    VMW(12); BAR();
    __builtin_amdgcn_s_setprio(3);
    gx[0] = ld16(gxp, 0);
    issue_x(2); issue_x(3);
#pragma unroll
    for (int i = 0; i < 2; ++i) {
      const unsigned ta = tab(2 * (16 * i + c)), tc = tab(32 * (wb >> 1) + 8 * (c >> 2) + 4 * i + (c & 3));
      mac[i] = *LP(u32x4, ta); mas[i] = *LP(u32x4, ta ^ 128u);
      mcc[i][0] = *LP(u32x4, tc); mcc[i][1] = *LP(u32x4, tc ^ 64u); mcs[i][0] = *LP(u32x4, tc ^ 128u); mcs[i][1] = *LP(u32x4, tc ^ 192u);
    }
    asm volatile("s_waitcnt vmcnt(8)" : "+v"(gw[0]), "+v"(gw[1]), "+v"(gw[2]), "+v"(gw[3]) :: "memory");
    BAR();
    gx[1] = ld16(gxp, 16384);
    issue_kv(1); issue_kv(2);
    VMW(12); BAR();
    gx[2] = ld16(gxp, 32768);
    issue_kv(3); issue_kv(4);
    VMW(16); BAR();
    gx[3] = ld16(gxp, 49152);
    issue_kv(5); issue_kv(6);
    VMW(16); BAR();
    issue_kv(7); issue_q(0);
    unsigned pfw;
    asm volatile("global_load_dword %0, %1, %2" : "=v"(pfw) : "v"((lane & 31) * 128), "s"(wo + idx * 16384 + wb * 4096) : "memory");
    VMW(17); BAR();
    issue_q(1); issue_q(2);
    VMW(13); BAR();
    issue_q(3);
#pragma unroll
    for (int i = 0; i < 4; ++i) {
      const int r = 4 * i + g, sl = c ^ ((r & 7) << 1);
      vx[i] = (unsigned)((16 * wb + r) * 1024 + (sl >> 3) * 512 + ((sl & 7) << 4) + 4096 - i * 1024);
    }
    if ((ct | mt) != 0) dma4(vx, wo - 4096 + cb * 128 + (i8 & 7) * 65536, dst0);
    {
      float s = 0.f;
#pragma unroll
      for (int i = 0; i < 4; ++i)
#pragma unroll
        for (int j = 0; j < 4; ++j) s += bflo(gx[i][j]) * bflo(gw[i][j]) + bfhi(gx[i][j]) * bfhi(gw[i][j]);
      s = row_sum16(s);
      if ((lane & 47) == 0) ST32(__float_as_uint(__builtin_amdgcn_rcpf(1.f + __expf(-s))), WS_G + (64 * mt + gr) * 4);
    }
    asm volatile("s_waitcnt vmcnt(0)" : "+v"(pfw) :: "memory");
    SIG(E_QL);
    ev_wait<15>(E_G);
    dma4(vx, wo - 4096 + cb * 128 + ((1 + i8) & 7) * 65536, dst0 + 16384);
    dma4(vx, wo - 4096 + cb * 128 + ((2 + i8) & 7) * 65536, dst0 + 32768);
    const int k = 32 * ct + mt;
    unsigned o_x = (unsigned)(WS_XT + k * 8192 + wb * 2048 + lane * 16), o_w = (unsigned)(WS_WT + k * 8192 + bt * 32);
    unsigned l_xw = L_SCR + (8 * wb + g) * 256 + ((c ^ ((8 * wb + g) & 15)) << 4);
    unsigned l_ca[4];
#pragma unroll
    for (int i = 0; i < 4; ++i) { l_ca[i] = L_SCR + c * 256 + (((4 * i + g) ^ c) << 4); PIN(l_ca[i]); }
    unsigned l_ew = L_SCR + 8192 + (wb >> 1) * 4096 + lane * 16 + (wb & 1) * 8, l_er = L_SCR + 8192 + (wb & 1) * 2048 + lane * 16;
    unsigned o_z = (unsigned)(WS_Z + k * 8192 + (2 * (16 * (wb & 1) + c) + (wb >> 1)) * 128 + g * 16);
    PIN(o_x); PIN(o_w); PIN(l_xw); PIN(l_ew); PIN(l_er); PIN(o_z);
    POLLN(LN_FB + ct * 32 + 8 * wb, 8, tag);
    SIG(E_FB);
    __builtin_amdgcn_s_setprio(2);
    {
      const u32x4 x0 = LD128(o_x), x1 = LD128(o_x + 1024);
      const u32x4 wv[2] = {LD128(o_w), LD128(o_w + 16)};
      u32x4 ar, ai, an;
      u32x4 ca[2][2][2];
      auto twiddle_dft32 = [&](const f32x4 (&yr)[2], const f32x4 (&yi)[2], f32x4 (&xr)[2], f32x4 (&xi)[2]) {
        const f32x4 r0 = yr[0] * w1c[0] + yi[0] * w1s[0], r1 = yr[1] * w1c[1] + yi[1] * w1s[1];
        const u32x4 br = pack8(r0, r1), bn = pack8(-r0, -r1);
        const u32x4 bi = pack8(yi[0] * w1c[0] - yr[0] * w1s[0], yi[1] * w1c[1] - yr[1] * w1s[1]);
#pragma unroll
        for (int ut = 0; ut < 2; ++ut) {
          xr[ut] = mfma32(mas[ut], bi, mfma32(mac[ut], br, zero));
          xi[ut] = mfma32(mas[ut], bn, mfma32(mac[ut], bi, zero));
        }
      };
      if (k != 0) {
        *LP(u32x4, l_xw) = x0; *(LAS u32x4*)(LP(char, l_xw ^ 64u) + 1024) = x1;
        SIG(E_X1);
        f32x4 fr[2] = {lo4(wv[0]), lo4(wv[1])}, fi[2] = {hi4(wv[0]), hi4(wv[1])};
        PIN(fr[0]); PIN(fr[1]); PIN(fi[0]); PIN(fi[1]);
        ev_wait<15>(E_X1);
#pragma unroll
        for (int i = 0; i < 8; ++i) ca[i >> 2][(i >> 1) & 1][i & 1] = ld16(l_ca[i >> 1], (i & 1) * 4096);
        f32x4 yr[2] = {zero, zero}, yi[2] = {zero, zero}, xr[2], xi[2];
#pragma unroll
        for (int ks = 0; ks < 2; ++ks)
#pragma unroll
          for (int tt = 0; tt < 2; ++tt) { yr[tt] = mfma32(ca[0][ks][tt], m0c[ks], yr[tt]); yi[tt] = mfma32(ca[0][ks][tt], m0n[ks], yi[tt]); }
#pragma unroll
        for (int ks = 0; ks < 2; ++ks)
#pragma unroll
          for (int tt = 0; tt < 2; ++tt) { yr[tt] = mfma32(ca[1][ks][tt], m0s[ks], yr[tt]); yi[tt] = mfma32(ca[1][ks][tt], m0c[ks], yi[tt]); }
        twiddle_dft32(yr, yi, xr, xi);
        const f32x4 i0 = xr[0] * fi[0] + xi[0] * fr[0], i1 = xr[1] * fi[1] + xi[1] * fr[1];
        ar = pack8(xr[0] * fr[0] - xi[0] * fi[0], xr[1] * fr[1] - xi[1] * fi[1]);
        ai = pack8(i0, i1); an = pack8(-i0, -i1);
      } else {
        const u32x4 wn[2] = {LD128(o_w + 256 * 8192), LD128(o_w + 256 * 8192 + 16)};
        unsigned mp[8];
#pragma unroll
        for (int e = 0; e < 8; ++e) { mp[e] = 4 * wt_pos((2048 - (16 * wb + c + 64 * (16 * (e >> 2) + 4 * g + (e & 3)))) & 2047); PIN(mp[e]); }
        *LP(u32x4, l_xw) = x0; *(LAS u32x4*)(LP(char, l_xw ^ 64u) + 1024) = x1;
        LAS u32x4* d = (LAS u32x4*)sm + bt * 2;
        d[0] = wv[0]; d[1] = wv[1]; d[512] = wn[0]; d[513] = wn[1];
        SIG(E_X1);
        ev_wait<15>(E_X1);
#pragma unroll
        for (int i = 0; i < 8; ++i) ca[i >> 2][(i >> 1) & 1][i & 1] = ld16(l_ca[i >> 1], (i & 1) * 4096);
        u32x4 m0[2], m1[2];
#pragma unroll
        for (int e = 0; e < 8; ++e) { m0[e >> 2][e & 3] = *LP(unsigned, mp[e]); m1[e >> 2][e & 3] = *(LAS unsigned*)(LP(char, mp[e]) + 8192); }
        f32x4 yr[2] = {zero, zero}, yi[2] = {zero, zero}, xr[2], xi[2], dr[2], di[2];
#pragma unroll
        for (int ks = 0; ks < 2; ++ks)
#pragma unroll
          for (int tt = 0; tt < 2; ++tt) { yr[tt] = mfma32(ca[0][ks][tt], m0c[ks], yr[tt]); yi[tt] = mfma32(ca[0][ks][tt], m0n[ks], yi[tt]); }
        twiddle_dft32(yr, yi, xr, xi);
#pragma unroll
        for (int ut = 0; ut < 2; ++ut) {
          const f32x4 fr = lo4(wv[ut]) + lo4(m0[ut]), fi = hi4(wv[ut]) - hi4(m0[ut]);
          dr[ut] = xr[ut] * fr - xi[ut] * fi; di[ut] = xr[ut] * fi + xi[ut] * fr;
          yr[ut] = zero; yi[ut] = zero;
        }
#pragma unroll
        for (int ks = 0; ks < 2; ++ks)
#pragma unroll
          for (int tt = 0; tt < 2; ++tt) { yr[tt] = mfma32(ca[1][ks][tt], m0s[ks], yr[tt]); yi[tt] = mfma32(ca[1][ks][tt], m0c[ks], yi[tt]); }
        twiddle_dft32(yr, yi, xr, xi);
#pragma unroll
        for (int ut = 0; ut < 2; ++ut) {
          const f32x4 fr = lo4(wn[ut]) + lo4(m1[ut]), fi = hi4(wn[ut]) - hi4(m1[ut]);
          dr[ut] += xr[ut] * fr - xi[ut] * fi; di[ut] += xr[ut] * fi + xi[ut] * fr;
        }
        ar = pack8(dr[0], dr[1]); ai = pack8(di[0], di[1]); an = pack8(-di[0], -di[1]);
      }
#pragma unroll
      for (int tt = 0; tt < 2; ++tt) {
        const f32x4 vr = mfma32(an, mas[tt], mfma32(ar, mac[tt], zero));
        const f32x4 vi = mfma32(ar, mas[tt], mfma32(ai, mac[tt], zero));
        const f32x4 pr = vr * w2c[tt] - vi * w2s[tt], pi = vi * w2c[tt] + vr * w2s[tt];
        *(LAS u32x2*)(LP(char, l_ew) + tt * 2048) = u32x2{pk2(pr[0], pr[1]), pk2(pr[2], pr[3])};
        *(LAS u32x2*)(LP(char, l_ew) + tt * 2048 + 1024) = u32x2{pk2(pi[0], pi[1]), pk2(pi[2], pi[3])};
      }
      SIG(E_X2);
      ev_wait<15>(E_X2);
      f32x4 zr[2] = {zero, zero}, zi[2] = {zero, zero}, zs[2] = {zero, zero};
#pragma unroll
      for (int ks = 0; ks < 2; ++ks) {
        const u32x4 vr = ld16(l_er, ks * 4096), vi = ld16(l_er, ks * 4096 + 1024);
#pragma unroll
        for (int jt = 0; jt < 2; ++jt) {
          zr[jt] = mfma32(mcc[jt][ks], vr, zr[jt]); zi[jt] = mfma32(mcs[jt][ks], vr, zi[jt]);
          zs[jt] = mfma32(mcs[jt][ks], vi, zs[jt]); zi[jt] = mfma32(mcc[jt][ks], vi, zi[jt]);
        }
      }
      ST128(pack8(zr[0] - zs[0], zr[1] - zs[1]), o_z);
      ST128(pack8(zi[0], zi[1]), o_z + 64);
    }
    __builtin_amdgcn_s_setprio(0);
    {
      make_g();
      const unsigned sel = (c & 1) ? 0x01000302u : 0x03020100u;
      const unsigned sgn = (c & 3) == 0 ? 0u : (c & 3) == 1 ? 0x80000000u : (c & 3) == 2 ? 0x80008000u : 0x00008000u;
      auto g_rest = [&]() {
#pragma unroll
        for (int f = 0; f < 8; ++f) {
          const int src = (f >> 2) * 8 + (f & 3);
#pragma unroll
          for (int i = 0; i < 4; ++i) ga[src + 4][i] = __builtin_amdgcn_perm(0u, ga[src][i], sel) ^ sgn;
          PIN(ga[src + 4]);
        }
        if (g == 0) ga[0][0] = (c & 1) ? 0xBE803E80u : 0x3E803E80u;
        PIN(ga[0]);
      };
      SB();
      DRAIN();
      SIG(E_ZD);
      if (wb == 0) {
        ev_wait<15>(E_ZD);
        if (lane < REP) __hip_atomic_fetch_add((GAS u64*)(ws + (size_t)(LN_CZ + lane * 16 + 2 * ct + (mt >> 4)) * 128), 1ull, RLX_AGENT);
      }
      g_rest();
    }
    unsigned o_zl = (unsigned)(WS_Z + (64 * wb + (lane >> 3)) * 8192 + rb * 128 + (lane & 7) * 16);
    unsigned l_zw, l_z0, l_z1;
    {
      const int kk = 64 * wb + (lane >> 3), R = (((kk & 1) * 128 + (kk >> 1)) << 1) + ((lane >> 2) & 1);
      l_zw = R * 64 + ((lane & 3) << 4);
      l_z0 = (8 * g + (c >> 2)) * 64 + 8 * (c & 3) + ((g & 1) << 5);
      l_z1 = l_z0 ^ 32u;
    }
    unsigned l_zw1 = l_zw ^ 32u, o_out[2], l_a = 98304u + wb * 8192 + lane * 16;
#pragma unroll
    for (int tt = 0; tt < 2; ++tt) { o_out[tt] = (unsigned)(((rb >> 1) + 32 * (32 * (rb & 1) + 16 * tt + c)) * 1024 + (64 * cb + 16 * wb + 4 * g) * 2); PIN(o_out[tt]); }
    PIN(l_a);
    PIN(o_zl); PIN(l_zw); PIN(l_zw1); PIN(l_z0); PIN(l_z1);
    __builtin_amdgcn_s_setprio(0);
    POLLN(LN_CZ + (bid % REP) * 16 + 4 * wb, 4, tag + 16);
    {
      u32x4 z[8];
#pragma unroll
      for (int i = 0; i < 8; ++i) z[i] = __builtin_amdgcn_raw_buffer_load_b128(rs, o_zl, i * 8 * 8192, 16);
      ev_wait<15>(E_WR);
#pragma unroll
      for (int i = 0; i < 8; ++i) *(LAS u32x4*)(LP(char, (i & 1) ? l_zw1 : l_zw) + i * 512) = z[i];
    }
    SIG(E_ZI);
    ev_wait<15>(E_ZI);
    f32x4 acc[2][2] = {{zero, zero}, {zero, zero}};
    {
      u32x4 zf[3][2][2];
      auto ldz = [&](int n) {
#pragma unroll
        for (int i = 0; i < 2; ++i) { zf[n % 3][i][0] = trfrag(l_z0, (2 * n + i) * 2048, 256); zf[n % 3][i][1] = trfrag(l_z1, (2 * n + i) * 2048, 256); }
      };
      ldz(0); ldz(1);
      SB();
#pragma unroll
      for (int n = 0; n < 8; ++n) {
        if (n < 6) ldz(n + 2);
#pragma unroll
        for (int i = 0; i < 2; ++i) {
          acc[n >> 2][0] = mfma32(ga[2 * n + i], zf[n % 3][i][0], acc[n >> 2][0]);
          acc[n >> 2][1] = mfma32(ga[2 * n + i], zf[n % 3][i][1], acc[n >> 2][1]);
        }
#pragma unroll
        for (int i = 0; i < 4; ++i) { __builtin_amdgcn_sched_group_barrier(0x100, 2, 0); __builtin_amdgcn_sched_group_barrier(0x8, 1, 0); }
        SB();
      }
    }
    {
      const volatile LAS unsigned* fp = (const volatile LAS unsigned*)(size_t)(L_SYNC + E_A * 16 + wb * 4);
      while (!__builtin_amdgcn_readfirstlane(*fp)) __builtin_amdgcn_s_sleep(1);
      CFENCE();
      const f32x2 sc = *(const LAS f32x2*)(LP(char, l_a) + 4096);
#pragma unroll
      for (int tt = 0; tt < 2; ++tt)
#pragma unroll
        for (int hf = 0; hf < 2; ++hf) {
          const f32x4 av = __builtin_bit_cast(f32x4, ld16(l_a, (hf * 2 + tt) * 1024));
          const f32x4 r = av + (hf ? acc[0][tt] - acc[1][tt] : acc[0][tt] + acc[1][tt]) * sc[tt];
          *(u32x2*)(out + o_out[tt] + 512 * hf) = u32x2{pk2(r[0], r[1]), pk2(r[2], r[3])};
        }
    }
  } else {
    __builtin_amdgcn_s_setprio(1);
    u32x4 wrv, wiv;
    unsigned nyq = 0, nyq2 = 0, pf = 0;
    {
      const unsigned o_w = (unsigned)(wt_row(64 * mt + (tid >> 2)) * 257 + 32 * ct + 8 * (tid & 3)) * 2u;
      wrv = *(const u32x4u*)(w_r + o_w);
      wiv = *(const u32x4u*)(w_i + o_w);
      if (ct == 0 && w == 0) {
        const unsigned o_ny = (unsigned)(wt_row(64 * mt + lane) * 257 + 256) * 2u;
        nyq = *(const unsigned short*)(w_r + o_ny); nyq2 = *(const unsigned short*)(w_i + o_ny);
      }
    }
    if (w == 2 && lane < CODE_LINES) pf = *(const GAS unsigned*)(pc0 + min((idx * CODE_LINES + lane) * 128, CODE_MAX));
    const u32x2 ev = *(const u32x2*)ws;
    const int rec = (2 * ct + (w >> 1)) * 64 + 2 * mt + (w & 1);
    unsigned o_kp = (unsigned)(WS_KVP + rec * 1024 + lane * 16), o_zp = (unsigned)(WS_ZP + rec * 64 + g * 16);
    ST128((u32x4{~0u, ~0u, ~0u, ~0u}), o_kp);
    if (c == 0) ST128((u32x4{~0u, ~0u, ~0u, ~0u}), o_zp);
    SB();
    const int rh = w & 1, kt = w >> 1;
    const int kf = 32 * ct + 16 * kt + c;
    u32x4 xf[4][4][2];
    unsigned bb[2][4];
    {
      const unsigned fl = (unsigned)((c >> 2) | ((g & 1) << 2));
      const unsigned bu = (unsigned)((8 * g + (c >> 2)) * 256 + 8 * (c & 3)) + (((unsigned)(2 * kt) ^ fl) << 5);
#pragma unroll
      for (int u = 0; u < 4; ++u) {
        bb[0][u] = bu ^ (unsigned)((u & 1) * 32 + (u >> 1) * 128);
        bb[1][u] = bb[0][u] + 65536u;
      }
    }
    u32x4 fb[3][4];
    auto ldf = [&](int n) {
#pragma unroll
      for (int u = 0; u < 4; ++u) {
        const int slot = (4 + (n >> 1)) & 7;
        fb[n % 3][u] = trfrag(bb[slot >> 2][u], (slot & 3) * 16384 + (n & 1) * 8192, 1024);
      }
    };
    u64 tag;
    {
      u32x4 fc[4], fs[4];
      {
        const float kk = (float)kf * (1.f / 512.f);
        const float a0 = __builtin_amdgcn_fractf(kk * (float)(8 * g + 128 * ((c0 + 1) & 3)));
#pragma unroll
        for (int ks = 0; ks < 4; ++ks) {
          const float ak = __builtin_fmaf(kk, (float)(((ks & 2) ^ (flip << 1)) * 32), a0);
#pragma unroll
          for (int i = 0; i < 4; ++i) {
            const float p0 = __builtin_fmaf(kk, (float)(32 * (ks & 1) + 2 * i), ak), p1 = __builtin_fmaf(kk, (float)(32 * (ks & 1) + 2 * i + 1), ak);
            fc[ks][i] = pk2(__builtin_amdgcn_cosf(p0), __builtin_amdgcn_cosf(p1));
            fs[ks][i] = kf == 0 ? 0xBF803F80u
                                : pk2(__builtin_amdgcn_sinf(-p0), __builtin_amdgcn_sinf(-p1));
          }
          PIN(fc[ks]); PIN(fs[ks]);
        }
      }
      float ra = (c & 1) ? 0.f : ((c & 2) ? -1.f : 1.f), rbi = (c & 1) ? ((c & 2) ? 1.f : -1.f) : 0.f;
      unsigned l_sc = L_SCR + ((tid & 3) * 512 + ((tid >> 2) ^ ((tid & 3) << 3))) * 4;
      unsigned l_rd = L_SCR + (tid >> 4) * 256 + (((tid & 15) ^ ((tid >> 7) << 1)) << 4);
      unsigned o_wt = (unsigned)(WS_WT + (32 * ct + (tid >> 4)) * 8192 + (64 * mt + 4 * (tid & 15)) * 4);
      unsigned o_xt = (unsigned)(WS_XT + kf * 8192 + mt * 256 + rh * 64 + g * 16);
      unsigned xa[4];
#pragma unroll
      for (int ks = 0; ks < 4; ++ks) { xa[ks] = rh * 8192 + c * 256 + (((4 * ks + g) ^ (flip << 3) ^ c) << 4); PIN(xa[ks]); }
#pragma unroll
      for (int u = 0; u < 4; ++u) { PIN(bb[0][u]); PIN(bb[1][u]); }
      PIN(l_sc); PIN(l_rd); PIN(o_wt); PIN(o_xt); PIN(ra); PIN(rbi); PIN(o_kp); PIN(o_zp);
      f32x4 xr[4][2], xi[4][2];
      auto ldx = [&](int s) {
#pragma unroll
        for (int ks = 0; ks < 4; ++ks)
#pragma unroll
          for (int tt = 0; tt < 2; ++tt) xf[s][ks][tt] = ld16(xa[ks], s * 16384 + tt * 4096);
      };
      auto mmf = [&](int s) {
#pragma unroll
        for (int tt = 0; tt < 2; ++tt) { xr[s][tt] = zero; xi[s][tt] = zero; }
#pragma unroll
        for (int ks = 0; ks < 4; ++ks)
#pragma unroll
          for (int tt = 0; tt < 2; ++tt) {
            xr[s][tt] = mfma32(xf[s][ks][tt], fc[ks], xr[s][tt]);
            xi[s][tt] = mfma32(xf[s][ks][tt], fs[ks], xi[s][tt]);
          }
      };
      auto horner = [&](int s) {
#pragma unroll
        for (int tt = 0; tt < 2; ++tt) {
          xr[s][tt] = (xr[s][tt] + xr[s - 1][tt] * ra) - xi[s - 1][tt] * rbi;
          xi[s][tt] = (xi[s][tt] + xr[s - 1][tt] * rbi) + xi[s - 1][tt] * ra;
        }
      };
      auto mixv = [&]() {
#pragma unroll
        for (int i = 0; i < 16; ++i) { __builtin_amdgcn_sched_group_barrier(0x8, 1, 0); __builtin_amdgcn_sched_group_barrier(0x2 | 0x80 | 0x40, 2, 0); }
        SB();
      };
      for (;;) { u64 r; asm volatile("s_memrealtime %0\n\ts_waitcnt lgkmcnt(0)" : "=s"(r) :: "memory"); if (r - rt0 >= 200) break; __builtin_amdgcn_s_sleep(1); }
      BAR();
      ldx(0);
      {
        PIN(pf); PIN(nyq); PIN(nyq2);
        tag = TAG_OF(((u64)(unsigned)__builtin_amdgcn_readfirstlane(ev.y) << 32) | (unsigned)__builtin_amdgcn_readfirstlane(ev.x));
        LAS unsigned* sc = LP(unsigned, l_sc);
#pragma unroll
        for (int j = 0; j < 4; ++j) {
          sc[(2 * j) * 64] = __builtin_amdgcn_perm(wiv[j], wrv[j], 0x05040100u);
          sc[(2 * j + 1) * 64] = __builtin_amdgcn_perm(wiv[j], wrv[j], 0x07060302u);
        }
        if (ct == 0 && w == 0) ST32(nyq | (nyq2 << 16), WS_WT + 256 * 8192 + (64 * mt + lane) * 4);
        if (mt == 0 && w == 1 && lane < 4 * REP)
          put_flag(ws, LN_CZ + (lane >> 1) * 16 + 2 * ct + (lane & 1), tag);
      }
      BAR();
      const u32x4 wt0 = ld16(l_rd, 0), wt1 = ld16(l_rd ^ 64u, 4096);
      ldx(1); SB(); mmf(0);
      BAR();
      ldx(2); SB(); mmf(1);
      BAR();
      ldx(3); SB();
      mmf(2); horner(1);
      ST128(wt0, o_wt); ST128(wt1, o_wt + 16 * 8192);
      mixv();
      mmf(3); horner(2);
      mixv();
      ldf(0); ldf(1);
      horner(3);
      ST128(pack8(xr[3][0], xr[3][1]), o_xt);
      ST128(pack8(xi[3][0], xi[3][1]), o_xt + 128);
    }
    auto mix = [&]() {
#pragma unroll
      for (int i = 0; i < 8; ++i) { __builtin_amdgcn_sched_group_barrier(0x100, 1, 0); __builtin_amdgcn_sched_group_barrier(0x8, 1, 0); }
      SB();
    };
    {
      f32x4 acc[2][4];
#pragma unroll
      for (int t = 0; t < 2; ++t)
#pragma unroll
        for (int u = 0; u < 4; ++u) acc[t][u] = zero;
#pragma unroll
      for (int n = 0; n < 16; ++n) {
        if (n == 8) DRAIN();
        if (n == 0 || n == 4 || n == 8) BAR();
        if (n == 8) { if (tid == 0) put_flag(ws, LN_FB + ct * 32 + mt, tag); SB(); }
        if (n == 12) { SIG(E_G); SB(); }
        if (n < 14) ldf(n + 2);
#pragma unroll
        for (int u = 0; u < 4; ++u)
#pragma unroll
          for (int t = 0; t < 2; ++t) acc[t][u] = mfma32(xf[n >> 2][n & 3][t], fb[n % 3][u], acc[t][u]);
        mix();
      }
      const u32x4 ones = {0x3f803f80u, 0x3f803f80u, 0x3f803f80u, 0x3f803f80u};
      f32x4 kv[2], zz[2];
#pragma unroll
      for (int n = 0; n < 2; ++n) {
        const u32x4 pk = pack8(phi4(acc[0][n]), phi4(acc[1][n]));
        kv[n] = mfma32(pk, pack8(acc[0][2 + n], acc[1][2 + n]), zero);
        zz[n] = mfma32(pk, ones, zero);
      }
      u32x4 kvo = pack8(kv[0], kv[1]), zo = pack8(zz[0], zz[1]);
#pragma unroll
      for (int e = 0; e < 4; ++e) {
        kvo[e] = min(kvo[e], 0xfffffffeu);
        zo[e] = min(zo[e], 0xfffffffeu);
      }
      ST128(kvo, o_kp);
      if (c == 0) ST128(zo, o_zp);
    }
    {
      const int hp = 2 * ct + (mt >> 4), sub = mt & 15;
      u32x4 pv[4];
      unsigned pz;
      auto ldp = [&]() {
#pragma unroll
        for (int i = 0; i < 4; ++i)
          pv[i] = __builtin_amdgcn_raw_buffer_load_b128(rs, WS_KVP + (hp * 64 + (lane >> 2)) * 1024 + sub * 64 + (lane & 3) * 16, i * 16 * 1024, 16);
        pz = LD32(WS_ZP + (hp * 64 + lane) * 64 + sub * 4);
      };
      f32x4 acc[2][2] = {{zero, zero}, {zero, zero}};
      ev_wait<15>(E_QL);
      SB();
      ldf(0); ldf(1);
      SB();
#pragma unroll
      for (int n = 0; n < 8; ++n) {
        if (n < 6) ldf(n + 2);
        if (n == 6) { SIG(E_QR); SB(); }
        if (n == 5) {
          if (w == 0) { ev_wait<15>(E_FB); ldp(); __builtin_amdgcn_s_setprio(3); }
          SB();
        }
#pragma unroll
        for (int u = 0; u < 4; ++u)
#pragma unroll
          for (int t = 0; t < 2; ++t) acc[u & 1][t] = mfma32(fb[n % 3][u], xf[n >> 1][2 * (u >> 1) + (n & 1)][t], acc[u & 1][t]);
        mix();
      }
      auto st_pq = [&]() {
#pragma unroll
        for (int n = 0; n < 2; ++n)
          ST128(pack8(phi4(acc[n][0]), phi4(acc[n][1])), WS_PQ + ((2 * mt + rh) * 32 + 4 * ct + 2 * kt + n) * 1024 + lane * 16);
        SB();
      };
      st_pq();
      if (w == 0) {
        for (unsigned spins = 0; spins < SPIN_CAP; ++spins) {
          unsigned m = pz;
#pragma unroll
          for (int i = 0; i < 4; ++i) m = max(max(m, max(pv[i][0], pv[i][1])), max(pv[i][2], pv[i][3]));
          if (__all(m != ~0u)) break;
          CFENCE();
          ldp();
        }
        const unsigned one = (c & 1) ? 0x3f800000u : 0x00003f80u;
        const u32x4 sel = {(c >> 1) == 0 ? one : 0u, (c >> 1) == 1 ? one : 0u, (c >> 1) == 2 ? one : 0u, (c >> 1) == 3 ? one : 0u};
        f32x4 s = zero;
#pragma unroll
        for (int i = 0; i < 4; ++i) s = mfma32(sel, pv[i], s);
        const f32x4 zq = mfma32(sel, u32x4{pz, 0u, 0u, 0u}, zero);
#pragma unroll
        for (int e = 0; e < 4; ++e) s[e] = dpp_add<0x128>(dpp_add<0x124>(s[e]));
        const float z0 = row_sum16(zq[0]), z1 = row_sum16(zq[1]);
        if ((lane & 44) == 0) ST64((u32x2{pk2(s[0], s[1]), pk2(s[2], s[3])}), WS_KVF + hp * 1024 + (sub * 4 + c) * 16 + g * 8);
        if (lane == 0) ST32(pk2(z0, z1), WS_ZF + hp * 64 + sub * 4);
        SB();
      }
      if (w == 0) {
        VMW(2);
        ev_wait<14>(E_PQ);
        if (lane == 0) put_flag(ws, LN_FQ + mt * 8 + ct, tag);
        VMW(1);
        if (lane < REP) __hip_atomic_fetch_add((GAS u64*)(ws + (size_t)(LN_CK + lane * 16 + 2 * ct + (mt >> 4)) * 128), 1ull, RLX_AGENT);
        SIG(E_KA);
      } else {
        unsigned voff[4];
#pragma unroll
        for (int i = 0; i < 4; ++i) {
          const int r = 4 * i + g, sl = c ^ ((r & 7) << 1);
          voff[i] = (unsigned)(r * 1024 + (sl >> 3) * 512 + ((sl & 7) << 4) + 4096 - i * 1024);
        }
        ev_wait<15>(E_QR);
#pragma unroll 1
        for (int gj = 11 + w; gj < ((ct | mt) == 0 ? 36 : 32); gj += 3) {
          const int gi = gj & 31;
          if (gj >= 32 && (ct | mt) == 0) ev_wait<15>(E_X2);
          dma4(voff, wo - 4096 + cb * 128 + ((gi + 4 * i8) & 31) * 16384, gi * 4096);
          VMW(4);
          if (gj < 15) { VMW(4); SIG(E_PQ); }
        }
        DRAIN();
        SIG(E_WL);
      }
    }
    __builtin_amdgcn_s_setprio(1);
    u32x4 wa[16][2], pq[8], kf4[4], zf4[4];
    float gate[2];
    unsigned l_o[4];
#pragma unroll
    for (int hp = 0; hp < 4; ++hp) { l_o[hp] = 65536u + ((4 * w + hp - 2 * i8) & 15) * 2048 + lane * 16; PIN(l_o[hp]); }
    unsigned o_kf = (unsigned)(WS_KVF + 4 * w * 1024 + lane * 16), o_zf = (unsigned)(WS_ZF + 4 * w * 64 + g * 16), l_or = 65536u + lane * 16;
    unsigned o_pq = (unsigned)(WS_PQ + (rb * 32 + 8 * w) * 1024 + lane * 16), o_g = (unsigned)(WS_G + (32 * rb + c) * 4);
    unsigned o_fl = lane < 24 ? (unsigned)(lane < 16 ? LN_FQ + (rb >> 1) * 8 + (lane >> 1) : LN_CK + (bid % REP) * 16 + 4 * w + ((lane >> 1) & 3)) * 128u + (lane & 1) * 4
                              : (unsigned)(WS_END + 4096);
    unsigned want = (unsigned)((lane < 16 ? tag : tag + 16) >> ((lane & 1) * 32));
    PIN(o_kf); PIN(o_zf); PIN(l_or); PIN(o_pq); PIN(o_g); PIN(o_fl); PIN(want);
    {
      const unsigned f7 = (unsigned)((4 * g + (c >> 2)) & 7);
      unsigned pw[2][2];
#pragma unroll
      for (int hf = 0; hf < 2; ++hf) {
        pw[hf][0] = (4 * g + (c >> 2)) * 256 + 8 * (c & 3) + ((((unsigned)w + 4u * hf) ^ f7) << 5);
        pw[hf][1] = pw[hf][0] + 65536u;
        PIN(pw[hf][0]); PIN(pw[hf][1]);
      }
      ev_wait<15>(E_X1);
      ev_wait<14>(E_WL);
      unsigned fv = LOOK(o_fl);
      SB();
#pragma unroll
      for (int st = 0; st < 16; ++st)
#pragma unroll
        for (int hf = 0; hf < 2; ++hf) wa[st][hf] = trfrag(pw[hf][st >> 3], (st & 7) * 8192, 4096);
      SIG(E_WR);
      SB();
      unsigned ok = (unsigned)__ballot(fv == want);
      for (unsigned spins = 0; (ok & 0xffffu) != 0xffffu && spins < SPIN_CAP; ++spins) {
        __builtin_amdgcn_s_sleep(POLL_SLEEP);
        ok = (unsigned)__ballot(LOOK(o_fl) == want);
      }
      CFENCE();
      fv = LOOK(((ok >> 16) & 0xffu) == 0xffu ? (unsigned)(WS_END + 4096) : o_fl);
      SB();
#pragma unroll
      for (int i = 0; i < 8; ++i) pq[i] = __builtin_amdgcn_raw_buffer_load_b128(rs, o_pq + (i & 3) * 1024, (i >> 2) * 4096, 16);
#pragma unroll
      for (int tt = 0; tt < 2; ++tt) gate[tt] = __uint_as_float(LD32(o_g + 64 * tt));
      SB();
      if (((ok >> 16) & 0xffu) != 0xffu) {
        ok = (unsigned)__ballot(fv == want);
        for (unsigned spins = 0; ((ok >> 16) & 0xffu) != 0xffu && spins < SPIN_CAP; ++spins) ok = (unsigned)__ballot(LOOK(o_fl) == want);
      }
      CFENCE();
    }
    {
#pragma unroll
      for (int i = 0; i < 4; ++i) { kf4[i] = LD128(o_kf + i * 1024); zf4[i] = LD128(o_zf + i * 64); }
      ev_wait<15>(E_WR);
      f32x4 num[2][2][2], den[2][2][2];
      auto mo = [&](int hp) {
#pragma unroll
        for (int tt = 0; tt < 2; ++tt)
#pragma unroll
          for (int n = 0; n < 2; ++n) {
            const u32x4 pp = pq[2 * hp + n];
            const u32x2 b = tt ? u32x2{pp.z, pp.w} : u32x2{pp.x, pp.y};
            const u32x2 ka = n ? u32x2{kf4[hp].z, kf4[hp].w} : u32x2{kf4[hp].x, kf4[hp].y};
            const u32x2 za = n ? u32x2{zf4[hp].z, zf4[hp].w} : u32x2{zf4[hp].x, zf4[hp].y};
            den[hp & 1][tt][n] = mfma16(za, b, f32x4{1e-6f, 1e-6f, 1e-6f, 1e-6f});
            num[hp & 1][tt][n] = mfma16(ka, b, zero);
          }
      };
      mo(0);
      SB();
#pragma unroll
      for (int hp = 0; hp < 4; ++hp) {
        if (hp < 3) mo(hp + 1);
        SB();
#pragma unroll
        for (int tt = 0; tt < 2; ++tt)
          *(LAS u32x4*)(LP(char, l_o[hp]) + tt * 1024) = pack8(num[hp & 1][tt][0] * __builtin_amdgcn_rcpf(den[hp & 1][tt][0][0]),
                                                               num[hp & 1][tt][1] * __builtin_amdgcn_rcpf(den[hp & 1][tt][1][0]));
        SB();
      }
    }
    SIG(E_O);
    ev_wait<15>(E_O);
    f32x4 acc[2][2] = {{zero, zero}, {zero, zero}};
    {
#pragma unroll
      for (int st = 0; st < 16; ++st)
#pragma unroll
        for (int tt = 0; tt < 2; ++tt) {
          const u32x4 ob = ld16(l_or, (st * 2 + tt) * 1024);
          acc[0][tt] = mfma32(wa[st][0], ob, acc[0][tt]);
          acc[1][tt] = mfma32(wa[st][1], ob, acc[1][tt]);
        }
      __builtin_amdgcn_sched_group_barrier(0x100, 8, 0);
#pragma unroll
      for (int i = 0; i < 24; ++i) { __builtin_amdgcn_sched_group_barrier(0x8, 2, 0); __builtin_amdgcn_sched_group_barrier(0x100, 1, 0); }
      __builtin_amdgcn_sched_group_barrier(0x8, 16, 0);
    }
#pragma unroll
    for (int i = 0; i < 4; ++i) *(LAS f32x4*)(LP(char, l_or) + 32768 + w * 8192 + i * 1024) = acc[i >> 1][i & 1] * gate[i & 1];
    *(LAS f32x2*)(LP(char, l_or) + 32768 + w * 8192 + 4096) = f32x2{(1.f - gate[0]) * (2.f / (2048.f * 512.f)), (1.f - gate[1]) * (2.f / (2048.f * 512.f))};
    SIG(E_A);
    if (bid == 0 && tid == 0) *(u32x2*)ws = u32x2{ev.x + 1u, ev.y};
  }
}

extern "C" void kernel_launch(void* const* d_in, const int* in_sizes, int n_in, void* d_out, int out_size,
                              void* d_ws, size_t ws_size, hipStream_t stream) {
  (void)hipFuncSetAttribute((const void*)fused_kernel, hipFuncAttributeMaxDynamicSharedMemorySize, LDS_BYTES);
  fused_kernel<<<256, 512, LDS_BYTES, stream>>>((const char*)d_in[0], (const char*)d_in[1], (const char*)d_in[2],
                                                (const char*)d_in[4], (const char*)d_in[5], (const char*)d_in[6],
                                                (char*)d_ws, (char*)d_out);
}
```

```cpp
#define OPT_RP
#define LDPN 5
#define CODE_MAX 29952
#define OPT_AF
#define BWN 4
#include <hip/hip_runtime.h>
#include <hip/hip_bf16.h>
#include <stdint.h>

typedef short s16x4 __attribute__((ext_vector_type(4)));
typedef short s16x8 __attribute__((ext_vector_type(8)));
typedef float f32x2 __attribute__((ext_vector_type(2)));
typedef float f32x4 __attribute__((ext_vector_type(4)));
typedef unsigned u32x2 __attribute__((ext_vector_type(2)));
typedef unsigned u32x4 __attribute__((ext_vector_type(4)));
typedef unsigned u32x4u __attribute__((ext_vector_type(4), aligned(2)));
typedef __bf16 bf16x2 __attribute__((ext_vector_type(2)));
typedef unsigned long long u64;
#define DI __device__ __forceinline__
#define LAS __attribute__((address_space(3)))
#define GAS __attribute__((address_space(1)))
#define RLX_AGENT __ATOMIC_RELAXED, __HIP_MEMORY_SCOPE_AGENT
#define CFENCE() asm volatile("" ::: "memory")
#define PIN(v) asm volatile("" : "+v"(v))
#define DRAIN() asm volatile("s_waitcnt vmcnt(0)" ::: "memory")
#define LGKM0() __builtin_amdgcn_s_waitcnt(0xC07F)
#define SB() __builtin_amdgcn_sched_barrier(0)
#define VMW_(n) asm volatile("s_waitcnt vmcnt(" #n ")" ::: "memory")
#define VMW(n) VMW_(n)
#define LP(T, a) ((LAS T*)(size_t)(a))
#define WS_DBG (64u << 20)
#define BAR() do { CFENCE(); SB(); LGKM0(); __builtin_amdgcn_s_barrier(); SB(); CFENCE(); } while (0)
#define ST(i)

DI unsigned pk2(float a, float b) {
  f32x2 v = {a, b};
  return __builtin_bit_cast(unsigned, __builtin_convertvector(v, bf16x2));
}
DI float bflo(unsigned u) { return __uint_as_float(u << 16); }
DI float bfhi(unsigned u) { return __uint_as_float(u & 0xffff0000u); }
DI f32x4 lo4(u32x4 u) { return f32x4{bflo(u.x), bflo(u.y), bflo(u.z), bflo(u.w)}; }
DI f32x4 hi4(u32x4 u) { return f32x4{bfhi(u.x), bfhi(u.y), bfhi(u.z), bfhi(u.w)}; }
DI f32x4 mfma32(u32x4 a, u32x4 b, f32x4 c) {
  return __builtin_amdgcn_mfma_f32_16x16x32_bf16(__builtin_bit_cast(s16x8, a), __builtin_bit_cast(s16x8, b), c, 0, 0, 0);
}
DI f32x4 mfma16(u32x2 a, u32x2 b, f32x4 c) {
  return __builtin_amdgcn_mfma_f32_16x16x16bf16_1k(__builtin_bit_cast(s16x4, a), __builtin_bit_cast(s16x4, b), c, 0, 0, 0);
}
DI u32x4 pack8(f32x4 a, f32x4 b) { return u32x4{pk2(a[0], a[1]), pk2(a[2], a[3]), pk2(b[0], b[1]), pk2(b[2], b[3])}; }
DI u32x4 neg8(u32x4 a) {
  PIN(a);
  a ^= 0x80008000u;
  PIN(a);
  return a;
}
DI f32x4 phi4(f32x4 t) {
  const f32x4 m = t * 1.4426950408889634f, r = t + 1e-6f;
  f32x4 e, q;
#pragma unroll
  for (int i = 0; i < 4; ++i) { e[i] = __builtin_amdgcn_exp2f(fminf(m[i], 0.f)); q[i] = fmaxf(r[i], 1e-6f); }
  return e + q;
}
DI void crot(f32x4& cr, f32x4& ci, float a, float b) {
  const f32x4 nr = cr * a - ci * b, ni = cr * b + ci * a;
  cr = nr; ci = ni;
}
DI u32x4 trfrag(unsigned a, int off, int hi_off) {
  s16x4 l = __builtin_amdgcn_ds_read_tr16_b64_v4i16((LAS s16x4*)(LP(char, a) + off));
  s16x4 h = __builtin_amdgcn_ds_read_tr16_b64_v4i16((LAS s16x4*)(LP(char, a) + off + hi_off));
  return __builtin_bit_cast(u32x4, __builtin_shufflevector(l, h, 0, 1, 2, 3, 4, 5, 6, 7));
}
DI u32x4 ld16(unsigned a, int off) { return *(const LAS u32x4*)(LP(char, a) + off); }
template <int CTRL> DI float dpp_add(float v) {
  return v + __int_as_float(__builtin_amdgcn_update_dpp(0, __float_as_int(v), CTRL, 0xf, 0xf, false));
}
DI float row_sum16(float v) { return dpp_add<0x128>(dpp_add<0x124>(dpp_add<0x4E>(dpp_add<0xB1>(v)))); }

#define KS 8192
#define RS 1024
#define REPV 8
constexpr int REP = REPV;
constexpr int LN_FB = 8, LN_FQ = LN_FB + 256, LN_CZ = LN_FQ + 256, LN_CK = LN_CZ + 16 * REP;
constexpr int WS_XT = 160 * 1024;
constexpr int WS_WT = WS_XT + 256 * KS;
constexpr int WS_KVP = WS_WT + 257 * KS;
constexpr int WS_ZP = WS_KVP + 16 * 64 * RS;
constexpr int WS_KVF = WS_ZP + 16 * 64 * 64;
constexpr int WS_ZF = WS_KVF + 16 * 1024;
constexpr int WS_G = WS_ZF + 2048;
constexpr int WS_PQ = WS_G + 8192;
constexpr int WS_Z = WS_PQ + 2048 * 1024;
constexpr int WS_WOF = WS_Z;
constexpr int WS_END = WS_Z + 256 * KS;
constexpr int L_SCR = 131072, L_ST = L_SCR + 30720, L_SYNC = L_SCR + 32768 - 512, LDS_BYTES = 163840;
enum { E_QL, E_FB, E_X1, E_X2, E_ZD, E_FZ, E_ZI, E_QR, E_PQ, E_WL, E_WR, E_KA, E_FQ, E_FK, E_O, E_G, E_A, E_S1, E_XD };
constexpr unsigned SPIN_CAP = 1u << 16;
#define POLL_SLEEP 2
#define WO_THR 4
#define FBN 8
#define BPRIO 0
#define APRIO 1
#define POLLK 1
#define ZT 16
#define RPRIO() __builtin_amdgcn_s_setprio(3)
#define FPRIO 2
#define CODE_LINES 8
#define TAG_OF(e) ((e) * 0x9E3779B97F4A7C15ull + 0x632BE59BD9B4E019ull)

#define SIG(ev) do { CFENCE(); *(volatile LAS unsigned*)(LP(char, sigp) + (ev) * 16) = 1u; CFENCE(); } while (0)
template <unsigned MASK> DI void ev_wait(int ev) {
  const volatile LAS u32x4* p = (const volatile LAS u32x4*)(size_t)(L_SYNC + ev * 16);
  for (;;) {
    const u32x4 v = *p;
    const unsigned a = ((MASK & 1) ? v.x : 1u) & ((MASK & 2) ? v.y : 1u) & ((MASK & 4) ? v.z : 1u) & ((MASK & 8) ? v.w : 1u);
    if (__builtin_amdgcn_readfirstlane(a)) break;
    __builtin_amdgcn_s_sleep(1);
  }
  CFENCE();
}
DI void poll_all_(const char* ws, unsigned line, u64 want) {
  const GAS u64* p = (const GAS u64*)(ws + (size_t)line * 128);
  for (unsigned spins = 0; spins < SPIN_CAP; ++spins) {
    u64 v; const u64 z = 0;
    asm volatile("global_atomic_add_x2 %0, %1, %2, off sc0 sc1\n\ts_waitcnt vmcnt(0)" : "=&v"(v) : "v"(p), "v"(z) : "memory");
    if (__all(v == want)) break;
    __builtin_amdgcn_s_sleep(POLL_SLEEP);
  }
  __builtin_amdgcn_fence(__ATOMIC_ACQUIRE, "wavefront");
  CFENCE();
}
#define poll_all(ws, line, want, n) do { if (lane < (n)) poll_all_(ws, line, want); CFENCE(); } while (0)
#define TGATE(kind)
#define EV_WAIT_READ(ev, body) do { \
    for (;;) { \
      CFENCE(); \
      const u32x4 v_ = *(const volatile LAS u32x4*)(size_t)(L_SYNC + (ev) * 16); \
      CFENCE(); \
      body; \
      SB(); \
      if (__builtin_amdgcn_readfirstlane(v_.x & v_.y & v_.z & v_.w)) break; \
      __builtin_amdgcn_s_sleep(1); \
    } \
    CFENCE(); } while (0)
DI void put_flag(char* ws, unsigned line, u64 v) { __hip_atomic_store((GAS u64*)(ws + (size_t)line * 128), v, RLX_AGENT); }

DI void dma4(const unsigned (&voff)[4], const char* sb, unsigned dst) {
  unsigned keep;
  asm volatile("s_mov_b32 %0, m0\n\ts_mov_b32 m0, %6\n\ts_nop 0\n\t"
               "global_load_lds_dwordx4 %1, %5\n\t"
               "global_load_lds_dwordx4 %2, %5 offset:1024\n\t"
               "global_load_lds_dwordx4 %3, %5 offset:2048\n\t"
               "global_load_lds_dwordx4 %4, %5 offset:3072\n\t"
               "s_mov_b32 m0, %0"
               : "=&s"(keep) : "v"(voff[0]), "v"(voff[1]), "v"(voff[2]), "v"(voff[3]), "s"(sb), "s"(dst) : "memory");
}
DI int wt_pos(int u) {
  const int k0 = u & 63, u1 = u >> 6;
  return (((k0 >> 4) * 64 + ((u1 >> 2) & 3) * 16 + (k0 & 15)) << 3) + (u1 >> 4) * 4 + (u1 & 3);
}
DI int wt_row(int pos) {
  const int bt = pos >> 3;
  return 16 * (bt >> 6) + (bt & 15) + 64 * (16 * ((pos >> 2) & 1) + 4 * ((bt >> 4) & 3) + (pos & 3));
}
DI void trig4(float f, float i0, f32x4& co, f32x4& si) {
#pragma unroll
  for (int e = 0; e < 4; ++e) {
    const float a = f * (i0 + (float)e);
    co[e] = __builtin_amdgcn_cosf(a); si[e] = __builtin_amdgcn_sinf(a);
  }
}
DI void trig_frag(float f, float i0, u32x4& co, u32x4& si) {
  f32x4 c0, s0, c1, s1;
  trig4(f, i0, c0, s0); trig4(f, i0 + 16.f, c1, s1);
  co = pack8(c0, c1); si = pack8(s0, s1);
  PIN(co); PIN(si);
}

#define LOOK(off) ((unsigned)__builtin_amdgcn_raw_ptr_buffer_atomic_add_i32(0, rs, (off), 0, 16))
#define LD128(off) __builtin_amdgcn_raw_buffer_load_b128(rs, (off), 0, 16)
#define LD32(off) __builtin_amdgcn_raw_buffer_load_b32(rs, (off), 0, 16)
#define ST128(v, off) __builtin_amdgcn_raw_buffer_store_b128((v), rs, (off), 0, 16)
#define ST64(v, off) __builtin_amdgcn_raw_buffer_store_b64((v), rs, (off), 0, 16)
#define ST32(v, off) __builtin_amdgcn_raw_buffer_store_b32((v), rs, (off), 0, 16)

#define POLLN(line0, n, val) do { \
    const unsigned o_ = lane < 2 * (n) ? (unsigned)((line0) + (lane >> 1)) * 128u + (lane & 1) * 4 : (unsigned)(WS_END + 4096); \
    const unsigned w_ = lane < 2 * (n) ? (unsigned)((val) >> ((lane & 1) * 32)) : 0u; \
    _Pragma("unroll 1") for (unsigned spins_ = 0; spins_ < SPIN_CAP; ++spins_) { \
      if ((__ballot(LOOK(o_) == w_) & ((1ull << (2 * (n))) - 1)) == ((1ull << (2 * (n))) - 1)) break; \
      __builtin_amdgcn_s_sleep(POLL_SLEEP); \
    } \
    CFENCE(); } while (0)

__global__ __launch_bounds__(512, 2) void fused_kernel(const char* __restrict__ x, const char* __restrict__ wqkv,
                                                       const char* __restrict__ wo, const char* __restrict__ w_r,
                                                       const char* __restrict__ w_i, const char* __restrict__ gate_w,
                                                       char* __restrict__ ws, char* __restrict__ out) {
  extern __shared__ __attribute__((aligned(16))) char smem[];
  LAS char* sm = (LAS char*)smem;
  asm volatile("" :: "s"(x), "s"(wqkv), "s"(wo), "s"(w_r), "s"(w_i), "s"(gate_w), "s"(ws), "s"(out), "s"(sm));
  u64 pc0;
  asm volatile("s_getpc_b64 %0" : "=s"(pc0));
  pc0 &= ~127ull;
  const int tid = threadIdx.x, lane = tid & 63;
  const int w = __builtin_amdgcn_readfirstlane(tid >> 6);
  const int bid = blockIdx.x, xcd = bid & 7, idx = bid >> 3;
  const int mt = (xcd & 3) * 8 + (idx & 7), ct = (xcd >> 2) * 4 + (idx >> 3);
  const int rb = xcd * 8 + (idx & 7), cb = idx >> 3;
  const int c = lane & 15, g = lane >> 4;
  const __amdgpu_buffer_rsrc_t rs = __builtin_amdgcn_make_buffer_rsrc(ws, 0, WS_END, 0x00020000);
  const f32x4 zero = {0, 0, 0, 0};
  unsigned sigp = L_SYNC + (w & 3) * 4;
  PIN(sigp);
  ST(0);
  const int i8 = idx & 7, c0 = (idx + (idx >> 3)) & 3, flip = (idx >> 2) & 1;
  if (w >= 4) {
    const int wb = w - 4, bt = tid - 256;
    unsigned vx[4], vk[4], vq[4];
    const unsigned dst0 = wb * 4096;
#pragma unroll
    for (int i = 0; i < 4; ++i) {
      const int r = 16 * wb + 4 * i + g;
      vx[i] = (unsigned)((mt + 32 * r) * 1024 + ((c ^ (r & 15)) << 4) + 4096 - i * 1024);
    }
    auto issue_x = [&](int i) { dma4(vx, x - 4096 + ((c0 - i) & 3) * 256, dst0 + i * 16384); };
    issue_x(0);
#pragma unroll
    for (int i = 0; i < 4; ++i) {
      const int r = 16 * wb + 4 * i + g, sl = c ^ ((g | ((i >> 1) << 2)) << 1);
      vk[i] = (unsigned)(r * 3072 + (sl >> 3) * 1024 + ((sl & 7) << 4) + 4096 - i * 1024);
    }
    auto issue_kv = [&](int j) {
      const int blk = 2 * ((c0 - (j >> 1)) & 3) + ((j & 1) ^ flip);
      dma4(vk, wqkv - 4096 + 1024 + ct * 128 + (unsigned)blk * (64 * 3072), dst0 + ((j + 4) & 7) * 16384);
    };
    u32x4 gw[4];
#pragma unroll
    for (int i = 0; i < 4; ++i)
      asm volatile("global_load_dwordx4 %0, %1, %2" : "=v"(gw[i]) : "v"(c * 16), "s"(gate_w + ((c0 - i) & 3) * 256) : "memory");
    issue_kv(0);
    issue_x(1);
    if (wb == 0) *(LAS u32x2*)(sm + L_SYNC + lane * 8) = u32x2{0u, 0u};
#pragma unroll
    for (int i = 0; i < 4; ++i) {
      const int r = 16 * wb + 4 * i + g, sl = c ^ ((g | ((i >> 1) << 2)) << 1);
      vq[i] = (unsigned)(r * 3072 + ((sl >> 3) ^ flip) * (64 * 3072) + ((sl & 7) << 4) + 4096 - i * 1024);
      PIN(vq[i]);
    }
    auto issue_q = [&](int j) { dma4(vq, wqkv - 4096 + ct * 128 + (unsigned)((c0 - j) & 3) * (128 * 3072), dst0 + (j + 4) * 16384); };
    const float g4 = (float)(4 * g);
    u32x4 m0c[2], m0s[2], m0n[2], mac[2], mas[2], man[2], mcc[2][2], mcs[2][2], mcn[2][2];
    f32x4 w1c[2], w1s[2], w2c[2], w2s[2];
    auto tab = [&](int row) { return (unsigned)(7 * 16384 + row * 256 + ((g ^ (row & 15)) << 4)); };
    {
      f32x4 c0v, s0v;
      trig4((float)(16 * wb + c) * (1.f / 64.f), g4, c0v, s0v);
      f32x4 c1v = c0v, s1v = s0v;
      crot(c1v, s1v, (c & 1) ? 0.f : ((c & 2) ? -1.f : 1.f), (c & 1) ? ((c & 2) ? -1.f : 1.f) : 0.f);
      const unsigned sg = (c & 1) ? 0x80008000u : 0u;
      m0c[0] = pack8(c0v, c1v); m0s[0] = pack8(s0v, s1v);
      PIN(m0c[0]); PIN(m0s[0]);
      m0c[1] = m0c[0] ^ sg; m0s[1] = m0s[0] ^ sg;
#pragma unroll
      for (int i = 0; i < 2; ++i) {
        m0n[i] = neg8(m0s[i]);
        *LP(u32x4, tab(16 * wb + c) ^ (64u * i)) = m0c[i];
        *LP(u32x4, tab(16 * wb + c) ^ (64u * i + 128u)) = m0s[i];
      }
    }
#pragma unroll
    for (int tt = 0; tt < 2; ++tt) {
      trig4((float)(16 * wb + c) * (1.f / 2048.f), g4 + 16.f * tt, w1c[tt], w1s[tt]);
      trig4((float)(16 * tt + c) * (1.f / 2048.f), g4 + 16.f * wb, w2c[tt], w2s[tt]);
      PIN(w1c[tt]); PIN(w1s[tt]); PIN(w2c[tt]); PIN(w2s[tt]);
    }
    u32x4 ga[16];
    auto make_g = [&]() {
      const int d = 64 * cb + 16 * wb + c;
      const float df = (float)d * (1.f / 512.f);
      f32x4 ec, es, oc, os;
      trig4(-2.f * df, g4, ec, es);
      trig4(-2.f * df, g4 + 0.5f, oc, os);
      const float ra = __builtin_amdgcn_cosf(-32.f * df), rb_ = __builtin_amdgcn_sinf(-32.f * df);
#pragma unroll
      for (int ss = 0; ss < 4; ++ss) {
        ga[ss] = u32x4{pk2(ec[0], es[0]), pk2(ec[1], es[1]), pk2(ec[2], es[2]), pk2(ec[3], es[3])};
        ga[8 + ss] = u32x4{pk2(oc[0], os[0]), pk2(oc[1], os[1]), pk2(oc[2], os[2]), pk2(oc[3], os[3])};
        PIN(ga[ss]); PIN(ga[8 + ss]);
        if (ss < 3) { crot(ec, es, ra, rb_); crot(oc, os, ra, rb_); }
      }
    };
    const int gr = 8 * ct + 2 * wb + (g & 1);
    unsigned gxp = gr * 256 + ((c ^ (gr & 15)) << 4);
    PIN(gxp);
    const int k = 32 * ct + mt;
    unsigned o_x = (unsigned)(WS_XT + k * KS + wb * 2048 + lane * 16), o_w = (unsigned)(WS_WT + k * KS + bt * 32);
    unsigned l_xw = L_SCR + (8 * wb + g) * 256 + ((c ^ ((8 * wb + g) & 15)) << 4);
    unsigned l_ca[4], vw[4];
#pragma unroll
    for (int i = 0; i < 4; ++i) {
      const int r = 4 * i + g, sl = c ^ ((r & 7) << 1);
      vw[i] = (unsigned)((16 * wb + r) * 1024 + (sl >> 3) * 512 + ((sl & 7) << 4) + 4096 - i * 1024);
      l_ca[i] = L_SCR + c * 256 + (((4 * i + g) ^ c) << 4);
      PIN(l_ca[i]); PIN(vw[i]);
    }
    unsigned l_ew = L_SCR + 8192 + (wb >> 1) * 4096 + lane * 16 + (wb & 1) * 8, l_er = L_SCR + 8192 + (wb & 1) * 2048 + lane * 16;
    unsigned o_z = (unsigned)(WS_Z + k * KS + (2 * (16 * (wb & 1) + c) + (wb >> 1)) * 128 + g * 16);
    PIN(o_x); PIN(o_w); PIN(l_xw); PIN(l_ew); PIN(l_er); PIN(o_z);
    u32x4 gx[4];
    u64 epoch;
    asm volatile("s_load_dwordx2 %0, %1, 0x0\n\ts_waitcnt lgkmcnt(0)" : "=s"(epoch) : "s"(ws) : "memory");
    const u64 tag = TAG_OF(epoch);
    VMW(12); BAR();
    __builtin_amdgcn_s_setprio(3);
    gx[0] = ld16(gxp, 0);
    issue_x(2); issue_x(3);
#pragma unroll
    for (int i = 0; i < 2; ++i) {
      const unsigned ta = tab(2 * (16 * i + c)), tc = tab(32 * (wb >> 1) + 8 * (c >> 2) + 4 * i + (c & 3));
      mac[i] = *LP(u32x4, ta); mas[i] = *LP(u32x4, ta ^ 128u);
      mcc[i][0] = *LP(u32x4, tc); mcc[i][1] = *LP(u32x4, tc ^ 64u); mcs[i][0] = *LP(u32x4, tc ^ 128u); mcs[i][1] = *LP(u32x4, tc ^ 192u);
    }
    asm volatile("s_waitcnt vmcnt(8)" : "+v"(gw[0]), "+v"(gw[1]), "+v"(gw[2]), "+v"(gw[3]) :: "memory");
    BAR();
    gx[1] = ld16(gxp, 16384);
    issue_kv(1); issue_kv(2);
#pragma unroll
    for (int i = 0; i < 2; ++i) { man[i] = neg8(mas[i]); mcn[i][0] = neg8(mcs[i][0]); mcn[i][1] = neg8(mcs[i][1]); }
    VMW(12); BAR();
    gx[2] = ld16(gxp, 32768);
    issue_kv(3); issue_kv(4);
    VMW(16); BAR();
    gx[3] = ld16(gxp, 49152);
    issue_kv(5); issue_kv(6);
    VMW(16); BAR();
    issue_kv(7); issue_q(0);
    unsigned pfw;
    asm volatile("global_load_dword %0, %1, %2" : "=v"(pfw) : "v"((lane & 31) * 128), "s"(wo + idx * 16384 + wb * 4096) : "memory");
    VMW(17); BAR();
    issue_q(1); issue_q(2);
    VMW(13); BAR();
    issue_q(3);
    {
      float s = 0.f;
#pragma unroll
      for (int i = 0; i < 4; ++i)
#pragma unroll
        for (int j = 0; j < 4; ++j) s += bflo(gx[i][j]) * bflo(gw[i][j]) + bfhi(gx[i][j]) * bfhi(gw[i][j]);
      s = row_sum16(s);
      if ((lane & 47) == 0) ST32(__float_as_uint(__builtin_amdgcn_rcpf(1.f + __expf(-s))), WS_G + (64 * mt + gr) * 4);
    }
    ST(1);
    ev_wait<15>(E_G);
    dma4(vw, wo - 4096 + cb * 128 + ((1 + i8) & 7) * 65536, dst0 + 16384);
    dma4(vw, wo - 4096 + cb * 128 + ((2 + i8) & 7) * 65536, dst0 + 32768);
    asm volatile("s_waitcnt vmcnt(9)" : "+v"(pfw) :: "memory");
    SIG(E_QL);
    if (k != 0) dma4(vw, wo - 4096 + cb * 128 + (i8 & 7) * 65536, dst0);
    ev_wait<15>(E_S1);
    dma4(vw, wo - 4096 + cb * 128 + ((3 + i8) & 7) * 65536, dst0 + 49152);
    unsigned mp[8] = {0, 0, 0, 0, 0, 0, 0, 0};
    if (k == 0) {
#pragma unroll
      for (int e = 0; e < 8; ++e) { mp[e] = 4 * wt_pos((2048 - (16 * wb + c + 64 * (16 * (e >> 2) + 4 * g + (e & 3)))) & 2047); PIN(mp[e]); }
    }
    ST(2);
    POLLN(LN_FB + ct * 32 + 8 * wb, 8, tag);
    SIG(E_FB);
    __builtin_amdgcn_s_setprio(FPRIO);
    ST(3);
    {
      const u32x4 x0 = LD128(o_x), x1 = LD128(o_x + 1024);
      SB();
      const u32x4 wv[2] = {LD128(o_w), LD128(o_w + 16)};
      const unsigned o_wn = k == 0 ? o_w + 256 * KS : (unsigned)(WS_END + 4096);
      const u32x4 wn[2] = {LD128(o_wn), LD128(o_wn + 16)};
      SB();
      *LP(u32x4, l_xw) = x0; *(LAS u32x4*)(LP(char, l_xw ^ 64u) + 1024) = x1;
      u32x4 ar, ai;
      u32x4 ca[2][2][2];
      auto rd_ca = [&]() {
#pragma unroll
        for (int i = 0; i < 8; ++i) ca[i >> 2][(i >> 1) & 1][i & 1] = ld16(l_ca[i >> 1], (i & 1) * 4096);
      };
      auto twiddle_dft32 = [&](const f32x4 (&yr)[2], const f32x4 (&yi)[2], f32x4 (&xr)[2], f32x4 (&xi)[2]) {
        const u32x4 br = pack8(yr[0] * w1c[0] + yi[0] * w1s[0], yr[1] * w1c[1] + yi[1] * w1s[1]);
        const u32x4 bi = pack8(yi[0] * w1c[0] - yr[0] * w1s[0], yi[1] * w1c[1] - yr[1] * w1s[1]);
#pragma unroll
        for (int ut = 0; ut < 2; ++ut) { xr[ut] = mfma32(mac[ut], br, zero); xi[ut] = mfma32(man[ut], br, zero); }
#pragma unroll
        for (int ut = 0; ut < 2; ++ut) { xr[ut] = mfma32(mas[ut], bi, xr[ut]); xi[ut] = mfma32(mac[ut], bi, xi[ut]); }
      };
      if (k != 0) {
        SIG(E_X1);
        ST(4);
        EV_WAIT_READ(E_X1, rd_ca());
        f32x4 yr[2] = {zero, zero}, yi[2] = {zero, zero}, xr[2], xi[2];
#pragma unroll
        for (int ks = 0; ks < 2; ++ks)
#pragma unroll
          for (int tt = 0; tt < 2; ++tt) { yr[tt] = mfma32(ca[0][ks][tt], m0c[ks], yr[tt]); yi[tt] = mfma32(ca[0][ks][tt], m0n[ks], yi[tt]); }
#pragma unroll
        for (int ks = 0; ks < 2; ++ks)
#pragma unroll
          for (int tt = 0; tt < 2; ++tt) { yr[tt] = mfma32(ca[1][ks][tt], m0s[ks], yr[tt]); yi[tt] = mfma32(ca[1][ks][tt], m0c[ks], yi[tt]); }
        f32x4 fr[2] = {lo4(wv[0]), lo4(wv[1])}, fi[2] = {hi4(wv[0]), hi4(wv[1])};
        PIN(fr[0]); PIN(fr[1]); PIN(fi[0]); PIN(fi[1]);
#pragma unroll
        for (int i = 0; i < 16; ++i) { __builtin_amdgcn_sched_group_barrier(0x8, 1, 0); __builtin_amdgcn_sched_group_barrier(0x2, 1, 0); }
        SB();
        twiddle_dft32(yr, yi, xr, xi);
        ar = pack8(xr[0] * fr[0] - xi[0] * fi[0], xr[1] * fr[1] - xi[1] * fi[1]);
        ai = pack8(xr[0] * fi[0] + xi[0] * fr[0], xr[1] * fi[1] + xi[1] * fr[1]);
      } else {
        LAS u32x4* d = (LAS u32x4*)sm + bt * 2;
        d[0] = wv[0]; d[1] = wv[1]; d[512] = wn[0]; d[513] = wn[1];
        SIG(E_X1);
        ev_wait<15>(E_X1);
        rd_ca();
        u32x4 m0[2], m1[2];
#pragma unroll
        for (int e = 0; e < 8; ++e) { m0[e >> 2][e & 3] = *LP(unsigned, mp[e]); m1[e >> 2][e & 3] = *(LAS unsigned*)(LP(char, mp[e]) + 8192); }
        SB();
        f32x4 yr[2] = {zero, zero}, yi[2] = {zero, zero}, zr[2] = {zero, zero}, zi[2] = {zero, zero}, xr[2], xi[2], ur[2], ui[2];
#pragma unroll
        for (int ks = 0; ks < 2; ++ks)
#pragma unroll
          for (int tt = 0; tt < 2; ++tt) { yr[tt] = mfma32(ca[0][ks][tt], m0c[ks], yr[tt]); yi[tt] = mfma32(ca[0][ks][tt], m0n[ks], yi[tt]); }
#pragma unroll
        for (int ks = 0; ks < 2; ++ks)
#pragma unroll
          for (int tt = 0; tt < 2; ++tt) { zr[tt] = mfma32(ca[1][ks][tt], m0s[ks], zr[tt]); zi[tt] = mfma32(ca[1][ks][tt], m0c[ks], zi[tt]); }
        twiddle_dft32(yr, yi, xr, xi);
        twiddle_dft32(zr, zi, ur, ui);
        f32x4 dr[2], di[2];
#pragma unroll
        for (int ut = 0; ut < 2; ++ut) {
          const f32x4 fr = lo4(wv[ut]) + lo4(m0[ut]), fi = hi4(wv[ut]) - hi4(m0[ut]);
          const f32x4 hr = lo4(wn[ut]) + lo4(m1[ut]), hi = hi4(wn[ut]) - hi4(m1[ut]);
          dr[ut] = (xr[ut] * fr - xi[ut] * fi) + (ur[ut] * hr - ui[ut] * hi);
          di[ut] = (xr[ut] * fi + xi[ut] * fr) + (ur[ut] * hi + ui[ut] * hr);
        }
        ar = pack8(dr[0], dr[1]); ai = pack8(di[0], di[1]);
      }
      ST(5);
      {
        f32x4 vr[2], vi[2];
#pragma unroll
        for (int tt = 0; tt < 2; ++tt) { vr[tt] = mfma32(ar, mac[tt], zero); vi[tt] = mfma32(ar, mas[tt], zero); }
#pragma unroll
        for (int tt = 0; tt < 2; ++tt) { vr[tt] = mfma32(ai, man[tt], vr[tt]); vi[tt] = mfma32(ai, mac[tt], vi[tt]); }
        SB();
#pragma unroll
        for (int tt = 0; tt < 2; ++tt) {
          const f32x4 pr = vr[tt] * w2c[tt] - vi[tt] * w2s[tt], pi = vi[tt] * w2c[tt] + vr[tt] * w2s[tt];
          *(LAS u32x2*)(LP(char, l_ew) + tt * 2048) = u32x2{pk2(pr[0], pr[1]), pk2(pr[2], pr[3])};
          *(LAS u32x2*)(LP(char, l_ew) + tt * 2048 + 1024) = u32x2{pk2(pi[0], pi[1]), pk2(pi[2], pi[3])};
        }
      }
      SIG(E_X2);
      u32x4 ve[2][2];
      EV_WAIT_READ(E_X2, { ve[0][0] = ld16(l_er, 0); ve[0][1] = ld16(l_er, 1024); ve[1][0] = ld16(l_er, 4096); ve[1][1] = ld16(l_er, 5120); });
      f32x4 zr[2] = {zero, zero}, zi[2] = {zero, zero};
#pragma unroll
      for (int ks = 0; ks < 2; ++ks) {
#pragma unroll
        for (int jt = 0; jt < 2; ++jt) { zr[jt] = mfma32(mcc[jt][ks], ve[ks][0], zr[jt]); zi[jt] = mfma32(mcs[jt][ks], ve[ks][0], zi[jt]); }
#pragma unroll
        for (int jt = 0; jt < 2; ++jt) { zr[jt] = mfma32(mcn[jt][ks], ve[ks][1], zr[jt]); zi[jt] = mfma32(mcc[jt][ks], ve[ks][1], zi[jt]); }
      }
      SB();
      ST128(pack8(zr[0], zr[1]), o_z);
      ST128(pack8(zi[0], zi[1]), o_z + 64);
    }
    ST(6);
    {
      make_g();
      const unsigned sel = (c & 1) ? 0x01000302u : 0x03020100u;
      const unsigned sgn = (c & 3) == 0 ? 0u : (c & 3) == 1 ? 0x80000000u : (c & 3) == 2 ? 0x80008000u : 0x00008000u;
      auto g_rest = [&]() {
#pragma unroll
        for (int f = 0; f < 8; ++f) {
          const int src = (f >> 2) * 8 + (f & 3);
#pragma unroll
          for (int i = 0; i < 4; ++i) ga[src + 4][i] = __builtin_amdgcn_perm(0u, ga[src][i], sel) ^ sgn;
          PIN(ga[src + 4]);
        }
        if (g == 0) ga[0][0] = (c & 1) ? 0xBE803E80u : 0x3E803E80u;
        PIN(ga[0]);
      };
      SB();
      DRAIN();
      SIG(E_ZD);
      if (wb == 0) {
        ev_wait<15>(E_ZD);
        if (lane < REP) __hip_atomic_fetch_add((GAS u64*)(ws + (size_t)(LN_CZ + lane * 16 + 2 * ct + (mt >> 4)) * 128), 1ull, RLX_AGENT);
      }
      __builtin_amdgcn_s_setprio(0);
      ST(7);
      g_rest();
    }
    unsigned o_zl = (unsigned)(WS_Z + (64 * wb + (lane >> 3)) * KS + rb * 128 + (lane & 7) * 16);
    unsigned l_zw, l_z0, l_z1;
    {
      const int kk = 64 * wb + (lane >> 3), R = (((kk & 1) * 128 + (kk >> 1)) << 1) + ((lane >> 2) & 1);
      l_zw = R * 64 + ((lane & 3) << 4);
      l_z0 = (8 * g + (c >> 2)) * 64 + 8 * (c & 3) + ((g & 1) << 5);
      l_z1 = l_z0 ^ 32u;
    }
    unsigned l_zw1 = l_zw ^ 32u, o_out[2], l_a = 98304u + wb * 8192 + lane * 16;
#pragma unroll
    for (int tt = 0; tt < 2; ++tt) { o_out[tt] = (unsigned)(((rb >> 1) + 32 * (32 * (rb & 1) + 16 * tt + c)) * 1024 + (64 * cb + 16 * wb + 4 * g) * 2); PIN(o_out[tt]); }
    PIN(l_a);
    PIN(o_zl); PIN(l_zw); PIN(l_zw1); PIN(l_z0); PIN(l_z1);
    __builtin_amdgcn_s_setprio(BPRIO);
    ST(11);
    POLLN(LN_CZ + (bid % REP) * 16 + 4 * wb, 4, tag + ZT);
    ST(8);
    {
      u32x4 z[8];
#pragma unroll
      for (int i = 0; i < 8; ++i) z[i] = __builtin_amdgcn_raw_buffer_load_b128(rs, o_zl, i * 8 * KS, 16);
      ev_wait<15>(E_WR);
#pragma unroll
      for (int i = 0; i < 8; ++i) *(LAS u32x4*)(LP(char, (i & 1) ? l_zw1 : l_zw) + i * 512) = z[i];
    }
    SIG(E_ZI);
    ST(9);
    f32x4 acc[2][2] = {{zero, zero}, {zero, zero}};
    {
      u32x4 zf[3][2][2];
      auto ldz = [&](int n) {
#pragma unroll
        for (int i = 0; i < 2; ++i) { zf[n % 3][i][0] = trfrag(l_z0, (2 * n + i) * 2048, 256); zf[n % 3][i][1] = trfrag(l_z1, (2 * n + i) * 2048, 256); }
      };
      EV_WAIT_READ(E_ZI, { ldz(0); ldz(1); });
#pragma unroll
      for (int n = 0; n < 8; ++n) {
        if (n < 6) ldz(n + 2);
#pragma unroll
        for (int i = 0; i < 2; ++i) {
          acc[n >> 2][0] = mfma32(ga[2 * n + i], zf[n % 3][i][0], acc[n >> 2][0]);
          acc[n >> 2][1] = mfma32(ga[2 * n + i], zf[n % 3][i][1], acc[n >> 2][1]);
        }
#pragma unroll
        for (int i = 0; i < 4; ++i) { __builtin_amdgcn_sched_group_barrier(0x100, 2, 0); __builtin_amdgcn_sched_group_barrier(0x8, 1, 0); }
        SB();
      }
    }
    ST(10);
#pragma unroll
    for (int tt = 0; tt < 2; ++tt) {
      *(LAS f32x4*)(LP(char, l_a) + tt * 1024) = acc[0][tt] + acc[1][tt];
      *(LAS f32x4*)(LP(char, l_a) + 2048 + tt * 1024) = acc[0][tt] - acc[1][tt];
    }
    SIG(E_A);
    ST(13);
  } else {
    __builtin_amdgcn_s_setprio(1);
    u32x4 wrv, wiv;
    unsigned nyq = 0, nyq2 = 0, pf = 0;
    {
      const unsigned o_w = (unsigned)(wt_row(64 * mt + (tid >> 2)) * 257 + 32 * ct + 8 * (tid & 3)) * 2u;
      wrv = *(const u32x4u*)(w_r + o_w);
      wiv = *(const u32x4u*)(w_i + o_w);
      if (ct == 0 && w == 0) {
        const unsigned o_ny = (unsigned)(wt_row(64 * mt + lane) * 257 + 256) * 2u;
        nyq = *(const unsigned short*)(w_r + o_ny); nyq2 = *(const unsigned short*)(w_i + o_ny);
      }
    }
    if (w == 2 && lane < CODE_LINES) pf = *(const GAS unsigned*)(pc0 + min((idx * CODE_LINES + lane) * 128, CODE_MAX));
    const u32x2 ev = *(const u32x2*)ws;
    const int rec = (2 * ct + (w >> 1)) * 64 + 2 * mt + (w & 1);
    unsigned o_kp = (unsigned)(WS_KVP + rec * RS + lane * 16), o_zp = (unsigned)(WS_ZP + rec * 64 + g * 16);
    ST128((u32x4{~0u, ~0u, ~0u, ~0u}), o_kp);
    if (c == 0) ST128((u32x4{~0u, ~0u, ~0u, ~0u}), o_zp);
    SB();
    const int rh = w & 1, kt = w >> 1;
    const int kf = 32 * ct + 16 * kt + c;
    u32x4 xf[4][4][2];
    unsigned bb[2][4];
    {
      const unsigned fl = (unsigned)((c >> 2) | ((g & 1) << 2));
      const unsigned bu = (unsigned)((8 * g + (c >> 2)) * 256 + 8 * (c & 3)) + (((unsigned)(2 * kt) ^ fl) << 5);
#pragma unroll
      for (int u = 0; u < 4; ++u) {
        bb[0][u] = bu ^ (unsigned)((u & 1) * 32 + (u >> 1) * 128);
        bb[1][u] = bb[0][u] + 65536u;
      }
    }
    u32x4 fb[3][4];
    auto ldf = [&](int n) {
#pragma unroll
      for (int u = 0; u < 4; ++u) {
        const int slot = (4 + (n >> 1)) & 7;
        fb[n % 3][u] = trfrag(bb[slot >> 2][u], (slot & 3) * 16384 + (n & 1) * 8192, 1024);
      }
    };
    u64 tag;
    {
      u32x4 fc[4], fs[4];
      {
        const float kk = (float)kf * (1.f / 512.f);
        const float a0 = __builtin_amdgcn_fractf(kk * (float)(8 * g + 128 * ((c0 + 1) & 3)));
#pragma unroll
        for (int ks = 0; ks < 4; ++ks) {
          const float ak = __builtin_fmaf(kk, (float)(((ks & 2) ^ (flip << 1)) * 32), a0);
#pragma unroll
          for (int i = 0; i < 4; ++i) {
            const float p0 = __builtin_fmaf(kk, (float)(32 * (ks & 1) + 2 * i), ak), p1 = __builtin_fmaf(kk, (float)(32 * (ks & 1) + 2 * i + 1), ak);
            fc[ks][i] = pk2(__builtin_amdgcn_cosf(p0), __builtin_amdgcn_cosf(p1));
            fs[ks][i] = kf == 0 ? 0xBF803F80u
                                : pk2(__builtin_amdgcn_sinf(-p0), __builtin_amdgcn_sinf(-p1));
          }
          PIN(fc[ks]); PIN(fs[ks]);
        }
      }
      float ra = (c & 1) ? 0.f : ((c & 2) ? -1.f : 1.f), rbi = (c & 1) ? ((c & 2) ? 1.f : -1.f) : 0.f;
      unsigned l_sc = L_SCR + ((tid & 3) * 512 + ((tid >> 2) ^ ((tid & 3) << 3))) * 4;
      unsigned l_rd = L_SCR + (tid >> 4) * 256 + (((tid & 15) ^ ((tid >> 7) << 1)) << 4);
      unsigned o_wt = (unsigned)(WS_WT + (32 * ct + (tid >> 4)) * KS + (64 * mt + 4 * (tid & 15)) * 4);
      unsigned o_xt = (unsigned)(WS_XT + kf * KS + mt * 256 + rh * 64 + g * 16);
      unsigned xa[4];
#pragma unroll
      for (int ks = 0; ks < 4; ++ks) { xa[ks] = rh * 8192 + c * 256 + (((4 * ks + g) ^ (flip << 3) ^ c) << 4); PIN(xa[ks]); }
#pragma unroll
      for (int u = 0; u < 4; ++u) { PIN(bb[0][u]); PIN(bb[1][u]); }
      PIN(l_sc); PIN(l_rd); PIN(o_wt); PIN(o_xt); PIN(ra); PIN(rbi); PIN(o_kp); PIN(o_zp);
      ST(1);
      f32x4 xr[4][2], xi[4][2];
      auto ldx = [&](int s) {
#pragma unroll
        for (int ks = 0; ks < 4; ++ks)
#pragma unroll
          for (int tt = 0; tt < 2; ++tt) xf[s][ks][tt] = ld16(xa[ks], s * 16384 + tt * 4096);
      };
      auto mmf = [&](int s) {
#pragma unroll
        for (int tt = 0; tt < 2; ++tt) { xr[s][tt] = zero; xi[s][tt] = zero; }
#pragma unroll
        for (int ks = 0; ks < 4; ++ks)
#pragma unroll
          for (int tt = 0; tt < 2; ++tt) {
            xr[s][tt] = mfma32(xf[s][ks][tt], fc[ks], xr[s][tt]);
            xi[s][tt] = mfma32(xf[s][ks][tt], fs[ks], xi[s][tt]);
          }
      };
      auto horner = [&](int s) {
#pragma unroll
        for (int tt = 0; tt < 2; ++tt) {
          xr[s][tt] = (xr[s][tt] + xr[s - 1][tt] * ra) - xi[s - 1][tt] * rbi;
          xi[s][tt] = (xi[s][tt] + xr[s - 1][tt] * rbi) + xi[s - 1][tt] * ra;
        }
      };
      auto mixv = [&]() {
#pragma unroll
        for (int i = 0; i < 16; ++i) { __builtin_amdgcn_sched_group_barrier(0x8, 1, 0); __builtin_amdgcn_sched_group_barrier(0x2 | 0x80 | 0x40, 2, 0); }
        SB();
      };
      BAR();
      ldx(0);
      {
        PIN(pf); PIN(nyq); PIN(nyq2);
        tag = TAG_OF(((u64)(unsigned)__builtin_amdgcn_readfirstlane(ev.y) << 32) | (unsigned)__builtin_amdgcn_readfirstlane(ev.x));
        LAS unsigned* sc = LP(unsigned, l_sc);
#pragma unroll
        for (int j = 0; j < 4; ++j) {
          sc[(2 * j) * 64] = __builtin_amdgcn_perm(wiv[j], wrv[j], 0x05040100u);
          sc[(2 * j + 1) * 64] = __builtin_amdgcn_perm(wiv[j], wrv[j], 0x07060302u);
        }
        if (ct == 0 && w == 0) ST32(nyq | (nyq2 << 16), WS_WT + 256 * KS + (64 * mt + lane) * 4);
        if (mt == 0 && w == 1 && lane < 4 * REP)
          put_flag(ws, LN_CZ + (lane >> 1) * 16 + 2 * ct + (lane & 1), tag);
      }
      BAR();
      const u32x4 wt0 = ld16(l_rd, 0), wt1 = ld16(l_rd ^ 64u, 4096);
      ldx(1); SB(); mmf(0);
      BAR();
      ldx(2); SB(); mmf(1);
      BAR();
      ldx(3); SB();
      mmf(2); horner(1);
      ST128(wt0, o_wt); ST128(wt1, o_wt + 16 * KS);
      mixv();
      mmf(3); horner(2);
      mixv();
      ldf(0); ldf(1);
      horner(3);
      ST128(pack8(xr[3][0], xr[3][1]), o_xt);
      ST128(pack8(xi[3][0], xi[3][1]), o_xt + 128);
    }
    ST(2);
    auto mix = [&]() {
#pragma unroll
      for (int i = 0; i < 8; ++i) { __builtin_amdgcn_sched_group_barrier(0x100, 1, 0); __builtin_amdgcn_sched_group_barrier(0x8, 1, 0); }
      SB();
    };
    {
      f32x4 acc[2][4];
#pragma unroll
      for (int t = 0; t < 2; ++t)
#pragma unroll
        for (int u = 0; u < 4; ++u) acc[t][u] = zero;
#pragma unroll
      for (int n = 0; n < 16; ++n) {
        if (n == FBN) DRAIN();
        if (n == 0 || n == 4 || n == 8) BAR();
        if (n == FBN) { if (tid == 0) put_flag(ws, LN_FB + ct * 32 + mt, tag); SB(); }
        if (n == 12) { SIG(E_G); SB(); }
        if (n == 14 && BWN > 3) { SIG(E_S1); SB(); }
        if (n < 14) ldf(n + 2);
#pragma unroll
        for (int u = 0; u < 4; ++u)
#pragma unroll
          for (int t = 0; t < 2; ++t) acc[t][u] = mfma32(xf[n >> 2][n & 3][t], fb[n % 3][u], acc[t][u]);
        mix();
      }
      ST(3);
      const u32x4 ones = {0x3f803f80u, 0x3f803f80u, 0x3f803f80u, 0x3f803f80u};
      f32x4 kv[2], zz[2];
#pragma unroll
      for (int n = 0; n < 2; ++n) {
        const u32x4 pk = pack8(phi4(acc[0][n]), phi4(acc[1][n]));
        kv[n] = mfma32(pk, pack8(acc[0][2 + n], acc[1][2 + n]), zero);
        zz[n] = mfma32(pk, ones, zero);
      }
      u32x4 kvo = pack8(kv[0], kv[1]), zo = pack8(zz[0], zz[1]);
#pragma unroll
      for (int e = 0; e < 4; ++e) {
        kvo[e] = min(kvo[e], 0xfffffffeu);
        zo[e] = min(zo[e], 0xfffffffeu);
      }
      ST128(kvo, o_kp);
      if (c == 0) ST128(zo, o_zp);
    }
    ST(4);
    {
      const int hp = 2 * ct + (mt >> 4), sub = mt & 15;
      u32x4 pv[4];
      unsigned pz;
      auto ldp = [&]() {
#pragma unroll
        for (int i = 0; i < 4; ++i)
          pv[i] = __builtin_amdgcn_raw_buffer_load_b128(rs, WS_KVP + (hp * 64 + (lane >> 2)) * RS + sub * 64 + (lane & 3) * 16, i * 16 * RS, 16);
        pz = LD32(WS_ZP + (hp * 64 + lane) * 64 + sub * 4);
      };
      f32x4 acc[2][2] = {{zero, zero}, {zero, zero}};
      ev_wait<15>(E_QL);
      SB();
      ldf(0); ldf(1);
      SB();
#pragma unroll
      for (int n = 0; n < 8; ++n) {
        if (n < 6) ldf(n + 2);
        if (n == 6) { SIG(E_QR); SB(); }
        if (n == LDPN) {
          if (w == 0) { ev_wait<15>(E_FB); ldp(); RPRIO(); }
          SB();
        }
#pragma unroll
        for (int u = 0; u < 4; ++u)
#pragma unroll
          for (int t = 0; t < 2; ++t) acc[u & 1][t] = mfma32(fb[n % 3][u], xf[n >> 1][2 * (u >> 1) + (n & 1)][t], acc[u & 1][t]);
        mix();
      }
      auto st_pq = [&]() {
#pragma unroll
        for (int n = 0; n < 2; ++n)
          ST128(pack8(phi4(acc[n][0]), phi4(acc[n][1])), WS_PQ + ((2 * mt + rh) * 32 + 4 * ct + 2 * kt + n) * 1024 + lane * 16);
        SB();
      };
      st_pq();
      ST(5);
      if (w == 0) {
        for (unsigned spins = 0; spins < SPIN_CAP; ++spins) {
          unsigned m = pz;
#pragma unroll
          for (int i = 0; i < 4; ++i) m = max(max(m, max(pv[i][0], pv[i][1])), max(pv[i][2], pv[i][3]));
          if (__all(m != ~0u)) break;
          CFENCE();
          ldp();
        }
        ST(6);
        const unsigned one = (c & 1) ? 0x3f800000u : 0x00003f80u;
        const u32x4 sel = {(c >> 1) == 0 ? one : 0u, (c >> 1) == 1 ? one : 0u, (c >> 1) == 2 ? one : 0u, (c >> 1) == 3 ? one : 0u};
        f32x4 s = zero;
#pragma unroll
        for (int i = 0; i < 4; ++i) s = mfma32(sel, pv[i], s);
        const f32x4 zq = mfma32(sel, u32x4{pz, 0u, 0u, 0u}, zero);
#pragma unroll
        for (int e = 0; e < 4; ++e) s[e] = dpp_add<0x128>(dpp_add<0x124>(s[e]));
        const float z0 = row_sum16(zq[0]), z1 = row_sum16(zq[1]);
        if ((lane & 44) == 0) ST64((u32x2{pk2(s[0], s[1]), pk2(s[2], s[3])}), WS_KVF + hp * 1024 + (sub * 4 + c) * 16 + g * 8);
        if (lane == 0) ST32(pk2(z0, z1), WS_ZF + hp * 64 + sub * 4);
        SB();
      }
      if (w == 0) {
        VMW(2);
        ev_wait<14>(E_PQ);
        if (lane == 0) put_flag(ws, LN_FQ + mt * 8 + ct, tag);
        VMW(1);
        if (lane < REP) __hip_atomic_fetch_add((GAS u64*)(ws + (size_t)(LN_CK + lane * 16 + 2 * ct + (mt >> 4)) * 128), 1ull, RLX_AGENT);
        SIG(E_KA);
      } else {
        unsigned voff[4];
#pragma unroll
        for (int i = 0; i < 4; ++i) {
          const int r = 4 * i + g, sl = c ^ ((r & 7) << 1);
          voff[i] = (unsigned)(r * 1024 + (sl >> 3) * 512 + ((sl & 7) << 4) + 4096 - i * 1024);
        }
        ev_wait<15>(E_QR);
#pragma unroll 1
        for (int gj = (BWN > 3 ? 15 : 11) + w; gj < (BWN == 0 ? 44 : (BWN == 2 || (ct | mt) == 0) ? 36 : 32); gj += 3) {
          const int gi = gj & 31;
          if (gj >= 32 && (ct | mt) == 0) ev_wait<15>(E_X2);
          dma4(voff, wo - 4096 + cb * 128 + ((gi + 4 * i8) & 31) * 16384, gi * 4096);
          VMW(WO_THR);
          if (gj < (BWN > 3 ? 19 : 15)) { VMW(4); SIG(E_PQ); }
        }
        DRAIN();
        SIG(E_WL);
      }
    }
    __builtin_amdgcn_s_setprio(APRIO);
    ST(7);
    u32x4 wa[16][2], pq[8], kf4[4], zf4[4];
    float gate[2];
    unsigned l_o[4];
#pragma unroll
    for (int q = 0; q < 4; ++q) { l_o[q] = 65536u + ((w + q) & 3) * 8192 + lane * 16; PIN(l_o[q]); }
    unsigned o_kf = (unsigned)(WS_KVF + 4 * w * 1024 + lane * 16), o_zf = (unsigned)(WS_ZF + 4 * w * 64 + g * 16), l_or = 65536u + lane * 16;
    unsigned o_pq = (unsigned)(WS_PQ + (rb * 32 + 8 * w) * 1024 + lane * 16), o_g = (unsigned)(WS_G + (32 * rb + c) * 4);
    unsigned o_fl = lane < 24 ? (unsigned)(lane < 16 ? LN_FQ + (rb >> 1) * 8 + (lane >> 1) : LN_CK + (bid % REP) * 16 + 4 * w + ((lane >> 1) & 3)) * 128u + (lane & 1) * 4
                              : (unsigned)(WS_END + 4096);
    unsigned want = (unsigned)((lane < 16 ? tag : tag + 16) >> ((lane & 1) * 32));
    PIN(o_kf); PIN(o_zf); PIN(l_or); PIN(o_pq); PIN(o_g); PIN(o_fl); PIN(want);
    {
      const unsigned f7 = (unsigned)((4 * g + (c >> 2)) & 7);
      const unsigned pw0 = (4 * g + (c >> 2)) * 256 + 8 * (c & 3) + (((unsigned)w ^ f7) << 5);
      ev_wait<15>(E_X1);
      ev_wait<14>(E_WL);
      unsigned fv = LOOK(o_fl);
      SB();
#pragma unroll
      for (int sp = 0; sp < 8; ++sp) {
        const unsigned p0 = pw0 + (unsigned)((2 * w + sp - i8) & 7) * 16384u, p1 = p0 ^ 128u;
#pragma unroll
        for (int i = 0; i < 2; ++i) { wa[2 * sp + i][0] = trfrag(p0, i * 8192, 4096); wa[2 * sp + i][1] = trfrag(p1, i * 8192, 4096); }
      }
      SIG(E_WR);
      SB();
      ST(8);
      unsigned ok = (unsigned)__ballot(fv == want);
      for (unsigned spins = 0; (ok & 0xffffu) != 0xffffu && spins < SPIN_CAP; ++spins) {
        __builtin_amdgcn_s_sleep(POLL_SLEEP);
        ok = (unsigned)__ballot(LOOK(o_fl) == want);
      }
      CFENCE();
      fv = LOOK(((ok >> 16) & 0xffu) == 0xffu ? (unsigned)(WS_END + 4096) : o_fl);
      SB();
#pragma unroll
      for (int i = 0; i < 8; ++i) pq[i] = __builtin_amdgcn_raw_buffer_load_b128(rs, o_pq + (i & 3) * 1024, (i >> 2) * 4096, 16);
#pragma unroll
      for (int tt = 0; tt < 2; ++tt) gate[tt] = __uint_as_float(LD32(o_g + 64 * tt));
      SB();
      ST(9);
      if (((ok >> 16) & 0xffu) != 0xffu) {
        ok = (unsigned)__ballot(fv == want);
        for (unsigned spins = 0; ((ok >> 16) & 0xffu) != 0xffu && spins < SPIN_CAP; ++spins) ok = (unsigned)__ballot(LOOK(o_fl) == want);
      }
      CFENCE();
    }
    ST(10);
    u32x4 ob[2][4][2];
    unsigned o_out = (unsigned)(((rb >> 1) + 32 * (32 * (rb & 1) + c)) * 1024 + (64 * cb + 16 * w + 4 * g) * 2);
    {
#pragma unroll
      for (int i = 0; i < 4; ++i) { kf4[i] = LD128(o_kf + i * 1024); zf4[i] = LD128(o_zf + i * 64); }
      PIN(o_out);
      ev_wait<15>(E_WR);
      f32x4 num[2][2][2], den[2][2][2];
      auto mo = [&](int hp) {
#pragma unroll
        for (int tt = 0; tt < 2; ++tt)
#pragma unroll
          for (int n = 0; n < 2; ++n) {
            const u32x4 pp = pq[2 * hp + n];
            const u32x2 b = tt ? u32x2{pp.z, pp.w} : u32x2{pp.x, pp.y};
            const u32x2 ka = n ? u32x2{kf4[hp].z, kf4[hp].w} : u32x2{kf4[hp].x, kf4[hp].y};
            const u32x2 za = n ? u32x2{zf4[hp].z, zf4[hp].w} : u32x2{zf4[hp].x, zf4[hp].y};
            den[hp & 1][tt][n] = mfma16(za, b, f32x4{1e-6f, 1e-6f, 1e-6f, 1e-6f});
            num[hp & 1][tt][n] = mfma16(ka, b, zero);
          }
      };
      mo(0);
      SB();
#pragma unroll
      for (int hp = 0; hp < 4; ++hp) {
        if (hp < 3) mo(hp + 1);
        SB();
#pragma unroll
        for (int tt = 0; tt < 2; ++tt) {
          ob[0][hp][tt] = pack8(num[hp & 1][tt][0] * __builtin_amdgcn_rcpf(den[hp & 1][tt][0][0]),
                                num[hp & 1][tt][1] * __builtin_amdgcn_rcpf(den[hp & 1][tt][1][0]));
          *(LAS u32x4*)(LP(char, l_o[0]) + (hp * 2 + tt) * 1024) = ob[0][hp][tt];
        }
        SB();
      }
    }
    SIG(E_O);
    ST(11);
    f32x4 acc[2][2] = {{zero, zero}, {zero, zero}};
#pragma unroll
    for (int q = 0; q < 4; ++q) {
      const volatile LAS unsigned* fp = (const volatile LAS unsigned*)(size_t)(L_SYNC + E_O * 16 + ((w + q + 1) & 3) * 4);
      unsigned up = 1u;
      if (q < 3) {
        CFENCE();
        up = *fp;
        CFENCE();
#pragma unroll
        for (int i = 0; i < 8; ++i) ob[(q + 1) & 1][i >> 1][i & 1] = ld16(l_o[q + 1], i * 1024);
        SB();
      }
#pragma unroll
      for (int i = 0; i < 8; ++i) {
        acc[0][i & 1] = mfma32(wa[4 * q + (i >> 1)][0], ob[q & 1][i >> 1][i & 1], acc[0][i & 1]);
        acc[1][i & 1] = mfma32(wa[4 * q + (i >> 1)][1], ob[q & 1][i >> 1][i & 1], acc[1][i & 1]);
      }
      SB();
      if (q < 3 && !__builtin_amdgcn_readfirstlane(up)) {
        while (!__builtin_amdgcn_readfirstlane(*fp)) __builtin_amdgcn_s_sleep(1);
        CFENCE();
#pragma unroll
        for (int i = 0; i < 8; ++i) ob[(q + 1) & 1][i >> 1][i & 1] = ld16(l_o[q + 1], i * 1024);
      }
    }
    ST(12);
    {
      const volatile LAS unsigned* fp = (const volatile LAS unsigned*)(size_t)(L_SYNC + E_A * 16 + w * 4);
      while (!__builtin_amdgcn_readfirstlane(*fp)) __builtin_amdgcn_s_sleep(1);
      CFENCE();
#pragma unroll
      for (int tt = 0; tt < 2; ++tt) {
        const float sc = (1.f - gate[tt]) * (2.f / (2048.f * 512.f));
#pragma unroll
        for (int hf = 0; hf < 2; ++hf) {
          const f32x4 bv = __builtin_bit_cast(f32x4, ld16(l_or + w * 8192, 32768 + (hf * 2 + tt) * 1024));
          const f32x4 r = acc[hf][tt] * gate[tt] + bv * sc;
          *(u32x2*)(out + o_out + tt * (16 * 32768) + 512 * hf) = u32x2{pk2(r[0], r[1]), pk2(r[2], r[3])};
        }
      }
    }
    if (bid == 0 && tid == 0) *(u32x2*)ws = u32x2{ev.x + 1u, ev.y};
    ST(13);
  }
}

extern "C" void kernel_launch(void* const* d_in, const int* in_sizes, int n_in, void* d_out, int out_size,
                              void* d_ws, size_t ws_size, hipStream_t stream) {
  (void)hipFuncSetAttribute((const void*)fused_kernel, hipFuncAttributeMaxDynamicSharedMemorySize, LDS_BYTES);
  fused_kernel<<<256, 512, LDS_BYTES, stream>>>((const char*)d_in[0], (const char*)d_in[1], (const char*)d_in[2],
                                                (const char*)d_in[4], (const char*)d_in[5], (const char*)d_in[6],
                                                (char*)d_ws, (char*)d_out);
}
```

```cpp
#include <hip/hip_runtime.h>
#include <hip/hip_bf16.h>
#include <stdint.h>

typedef short s16x4 __attribute__((ext_vector_type(4)));
typedef short s16x8 __attribute__((ext_vector_type(8)));
typedef float f32x2 __attribute__((ext_vector_type(2)));
typedef float f32x4 __attribute__((ext_vector_type(4)));
typedef unsigned u32x2 __attribute__((ext_vector_type(2)));
typedef unsigned u32x4 __attribute__((ext_vector_type(4)));
typedef unsigned u32x4u __attribute__((ext_vector_type(4), aligned(2)));
typedef __bf16 bf16x2 __attribute__((ext_vector_type(2)));
typedef unsigned long long u64;
#define DI __device__ __forceinline__
#define LAS __attribute__((address_space(3)))
#define GAS __attribute__((address_space(1)))
#define RLX_AGENT __ATOMIC_RELAXED, __HIP_MEMORY_SCOPE_AGENT
#define CFENCE() asm volatile("" ::: "memory")
#define PIN(v) asm volatile("" : "+v"(v))
#define DRAIN() asm volatile("s_waitcnt vmcnt(0)" ::: "memory")
#define LGKM0() __builtin_amdgcn_s_waitcnt(0xC07F)
#define SB() __builtin_amdgcn_sched_barrier(0)
#define VMW_(n) asm volatile("s_waitcnt vmcnt(" #n ")" ::: "memory")
#define VMW(n) VMW_(n)
#define LP(T, a) ((LAS T*)(size_t)(a))
#define BAR() do { CFENCE(); SB(); LGKM0(); __builtin_amdgcn_s_barrier(); SB(); CFENCE(); } while (0)

DI unsigned pk2(float a, float b) {
  f32x2 v = {a, b};
  return __builtin_bit_cast(unsigned, __builtin_convertvector(v, bf16x2));
}
DI float bflo(unsigned u) { return __uint_as_float(u << 16); }
DI float bfhi(unsigned u) { return __uint_as_float(u & 0xffff0000u); }
DI f32x4 lo4(u32x4 u) { return f32x4{bflo(u.x), bflo(u.y), bflo(u.z), bflo(u.w)}; }
DI f32x4 hi4(u32x4 u) { return f32x4{bfhi(u.x), bfhi(u.y), bfhi(u.z), bfhi(u.w)}; }
DI f32x4 mfma32(u32x4 a, u32x4 b, f32x4 c) {
  return __builtin_amdgcn_mfma_f32_16x16x32_bf16(__builtin_bit_cast(s16x8, a), __builtin_bit_cast(s16x8, b), c, 0, 0, 0);
}
DI f32x4 mfma16(u32x2 a, u32x2 b, f32x4 c) {
  return __builtin_amdgcn_mfma_f32_16x16x16bf16_1k(__builtin_bit_cast(s16x4, a), __builtin_bit_cast(s16x4, b), c, 0, 0, 0);
}
DI u32x4 pack8(f32x4 a, f32x4 b) { return u32x4{pk2(a[0], a[1]), pk2(a[2], a[3]), pk2(b[0], b[1]), pk2(b[2], b[3])}; }
DI u32x4 neg8(u32x4 a) {
  PIN(a);
  a ^= 0x80008000u;
  PIN(a);
  return a;
}
DI f32x4 phi4(f32x4 r) {
  f32x4 m = r * 1.4426950408889634f - 1.4426950408889634e-6f;
  PIN(m);
  f32x4 e, q;
#pragma unroll
  for (int i = 0; i < 4; ++i) {
    e[i] = __builtin_amdgcn_fmed3f(__builtin_amdgcn_exp2f(m[i]), 0.f, 1.f);
    q[i] = __int_as_float(max(__float_as_int(r[i]), 0x358637bd));
  }
  return e + q;
}
DI void crot(f32x4& cr, f32x4& ci, float a, float b) {
  const f32x4 nr = cr * a - ci * b, ni = cr * b + ci * a;
  cr = nr; ci = ni;
}
DI u32x4 trfrag(unsigned a, int off, int hi_off) {
  s16x4 l = __builtin_amdgcn_ds_read_tr16_b64_v4i16((LAS s16x4*)(LP(char, a) + off));
  s16x4 h = __builtin_amdgcn_ds_read_tr16_b64_v4i16((LAS s16x4*)(LP(char, a) + off + hi_off));
  return __builtin_bit_cast(u32x4, __builtin_shufflevector(l, h, 0, 1, 2, 3, 4, 5, 6, 7));
}
DI u32x4 ld16(unsigned a, int off) { return *(const LAS u32x4*)(LP(char, a) + off); }
template <int CTRL> DI float dpp_add(float v) {
  return v + __int_as_float(__builtin_amdgcn_update_dpp(0, __float_as_int(v), CTRL, 0xf, 0xf, false));
}
DI float row_sum16(float v) { return dpp_add<0x128>(dpp_add<0x124>(dpp_add<0x4E>(dpp_add<0xB1>(v)))); }

constexpr int REP = 8;
constexpr int LN_FB = 8, LN_FQ = LN_FB + 256, LN_CZ = LN_FQ + 256, LN_CK = LN_CZ + 16 * REP;
constexpr int WS_XT = 160 * 1024;
constexpr int WS_WT = WS_XT + 256 * 8192;
constexpr int WS_KVP = WS_WT + 257 * 8192;
constexpr int WS_ZP = WS_KVP + 16 * 64 * 1024;
constexpr int WS_KVF = WS_ZP + 16 * 64 * 64;
constexpr int WS_ZF = WS_KVF + 16 * 1024;
constexpr int WS_G = WS_ZF + 2048;
constexpr int WS_PQ = WS_G + 8192;
constexpr int WS_Z = WS_PQ + 2048 * 1024;
constexpr int WS_END = WS_Z + 256 * 8192;
constexpr int L_SCR = 131072, L_SYNC = L_SCR + 32768 - 512, L_TAG = L_SYNC - 16, LDS_BYTES = 163840;
enum { E_QL, E_FB, E_X1, E_X2, E_ZD, E_ZI, E_QR, E_PQ, E_WL, E_WR, E_O, E_G, E_A, E_S1 };
constexpr unsigned SPIN_CAP = 1u << 16;
constexpr int POLL_SLEEP = 2;
constexpr int CODE_LINES = 8, CODE_MAX = 29824;
#define TAG_OF(e) ((e) * 0x9E3779B97F4A7C15ull + 0x632BE59BD9B4E019ull)

#define SIG(ev) do { CFENCE(); *(volatile LAS unsigned*)(LP(char, sigp) + (ev) * 16) = 1u; CFENCE(); } while (0)
template <unsigned MASK> DI void ev_wait(int ev) {
  const volatile LAS u32x4* p = (const volatile LAS u32x4*)(size_t)(L_SYNC + ev * 16);
  for (;;) {
    const u32x4 v = *p;
    const unsigned a = ((MASK & 1) ? v.x : 1u) & ((MASK & 2) ? v.y : 1u) & ((MASK & 4) ? v.z : 1u) & ((MASK & 8) ? v.w : 1u);
    if (__builtin_amdgcn_readfirstlane(a)) break;
    __builtin_amdgcn_s_sleep(1);
  }
  CFENCE();
}
#define EV_WAIT_READ(ev, body) do { \
    for (;;) { \
      CFENCE(); \
      const u32x4 v_ = *(const volatile LAS u32x4*)(size_t)(L_SYNC + (ev) * 16); \
      CFENCE(); \
      body; \
      SB(); \
      if (__builtin_amdgcn_readfirstlane(v_.x & v_.y & v_.z & v_.w)) break; \
      __builtin_amdgcn_s_sleep(1); \
    } \
    CFENCE(); } while (0)
DI void put_flag(char* ws, unsigned line, u64 v) { __hip_atomic_store((GAS u64*)(ws + (size_t)line * 128), v, RLX_AGENT); }

DI void dma4(const unsigned (&voff)[4], const char* sb, unsigned dst) {
  unsigned keep;
  asm volatile("s_mov_b32 %0, m0\n\ts_mov_b32 m0, %6\n\ts_nop 0\n\t"
               "global_load_lds_dwordx4 %1, %5\n\t"
               "global_load_lds_dwordx4 %2, %5 offset:1024\n\t"
               "global_load_lds_dwordx4 %3, %5 offset:2048\n\t"
               "global_load_lds_dwordx4 %4, %5 offset:3072\n\t"
               "s_mov_b32 m0, %0"
               : "=&s"(keep) : "v"(voff[0]), "v"(voff[1]), "v"(voff[2]), "v"(voff[3]), "s"(sb), "s"(dst) : "memory");
}
DI int wt_pos(int u) {
  const int k0 = u & 63, u1 = u >> 6;
  return (((k0 >> 4) * 64 + ((u1 >> 2) & 3) * 16 + (k0 & 15)) << 3) + (u1 >> 4) * 4 + (u1 & 3);
}
DI int wt_row(int pos) {
  const int bt = pos >> 3;
  return 16 * (bt >> 6) + (bt & 15) + 64 * (16 * ((pos >> 2) & 1) + 4 * ((bt >> 4) & 3) + (pos & 3));
}
DI void trig4(float f, float i0, f32x4& co, f32x4& si) {
#pragma unroll
  for (int e = 0; e < 4; ++e) {
    const float a = f * (i0 + (float)e);
    co[e] = __builtin_amdgcn_cosf(a); si[e] = __builtin_amdgcn_sinf(a);
  }
}
#define LOOK(off) ((unsigned)__builtin_amdgcn_raw_ptr_buffer_atomic_add_i32(0, rs, (off), 0, 16))
#define LD128(off) __builtin_amdgcn_raw_buffer_load_b128(rs, (off), 0, 16)
#define LD32(off) __builtin_amdgcn_raw_buffer_load_b32(rs, (off), 0, 16)
#define ST128(v, off) __builtin_amdgcn_raw_buffer_store_b128((v), rs, (off), 0, 16)
#define ST64(v, off) __builtin_amdgcn_raw_buffer_store_b64((v), rs, (off), 0, 16)
#define ST32(v, off) __builtin_amdgcn_raw_buffer_store_b32((v), rs, (off), 0, 16)

#define POLLN(line0, n, val) do { \
    const unsigned o_ = lane < 2 * (n) ? (unsigned)((line0) + (lane >> 1)) * 128u + (lane & 1) * 4 : (unsigned)(WS_END + 4096); \
    const unsigned w_ = lane < 2 * (n) ? (unsigned)((val) >> ((lane & 1) * 32)) : 0u; \
    _Pragma("unroll 1") for (unsigned spins_ = 0; spins_ < SPIN_CAP; ++spins_) { \
      if ((__ballot(LOOK(o_) == w_) & ((1ull << (2 * (n))) - 1)) == ((1ull << (2 * (n))) - 1)) break; \
      __builtin_amdgcn_s_sleep(POLL_SLEEP); \
    } \
    CFENCE(); } while (0)

__global__ __launch_bounds__(512, 2) void fused_kernel(const char* __restrict__ x, const char* __restrict__ wqkv,
                                                       const char* __restrict__ wo, const char* __restrict__ w_r,
                                                       const char* __restrict__ w_i, const char* __restrict__ gate_w,
                                                       char* __restrict__ ws, char* __restrict__ out) {
  extern __shared__ __attribute__((aligned(16))) char smem[];
  LAS char* sm = (LAS char*)smem;
  asm volatile("" :: "s"(x), "s"(wqkv), "s"(wo), "s"(w_r), "s"(w_i), "s"(gate_w), "s"(ws), "s"(out), "s"(sm));
  u64 pc0;
  asm volatile("s_getpc_b64 %0" : "=s"(pc0));
  pc0 &= ~127ull;
  const int tid = threadIdx.x, lane = tid & 63;
  const int w = __builtin_amdgcn_readfirstlane(tid >> 6);
  const int bid = blockIdx.x, xcd = bid & 7, idx = bid >> 3;
  const int mt = (xcd & 3) * 8 + (idx & 7), ct = (xcd >> 2) * 4 + (idx >> 3);
  const int rb = xcd * 8 + (idx & 7), cb = idx >> 3;
  const int c = lane & 15, g = lane >> 4;
  const __amdgpu_buffer_rsrc_t rs = __builtin_amdgcn_make_buffer_rsrc(ws, 0, WS_END, 0x00020000);
  const f32x4 zero = {0, 0, 0, 0}, eps4 = {1e-6f, 1e-6f, 1e-6f, 1e-6f};
  unsigned sigp = L_SYNC + (w & 3) * 4;
  PIN(sigp);
  const int i8 = idx & 7, c0 = (idx + (idx >> 3)) & 3, flip = (idx >> 2) & 1;
  if (w >= 4) {
    const int wb = w - 4, bt = tid - 256;
    unsigned vx[4], vk[4], vq[4];
    const unsigned dst0 = wb * 4096;
#pragma unroll
    for (int i = 0; i < 4; ++i) {
      const int r = 16 * wb + 4 * i + g;
      vx[i] = (unsigned)((mt + 32 * r) * 1024 + ((c ^ (r & 15)) << 4) + 4096 - i * 1024);
    }
    auto issue_x = [&](int i) { dma4(vx, x - 4096 + ((c0 - i) & 3) * 256, dst0 + i * 16384); };
    issue_x(0);
#pragma unroll
    for (int i = 0; i < 4; ++i) {
      const int r = 16 * wb + 4 * i + g, sl = c ^ ((g | ((i >> 1) << 2)) << 1);
      vk[i] = (unsigned)(r * 3072 + (sl >> 3) * 1024 + ((sl & 7) << 4) + 4096 - i * 1024);
    }
    auto issue_kv = [&](int j) {
      const int blk = 2 * ((c0 - (j >> 1)) & 3) + ((j & 1) ^ flip);
      dma4(vk, wqkv - 4096 + 1024 + ct * 128 + (unsigned)blk * (64 * 3072), dst0 + ((j + 4) & 7) * 16384);
    };
    u32x4 gw[4];
#pragma unroll
    for (int i = 0; i < 4; ++i)
      asm volatile("global_load_dwordx4 %0, %1, %2" : "=v"(gw[i]) : "v"(c * 16), "s"(gate_w + ((c0 - i) & 3) * 256) : "memory");
    issue_kv(0);
    issue_x(1);
    if (wb == 0) *(LAS u32x2*)(sm + L_SYNC + lane * 8) = u32x2{0u, 0u};
#pragma unroll
    for (int i = 0; i < 4; ++i) {
      const int r = 16 * wb + 4 * i + g, sl = c ^ ((g | ((i >> 1) << 2)) << 1);
      vq[i] = (unsigned)(r * 3072 + ((sl >> 3) ^ flip) * (64 * 3072) + ((sl & 7) << 4) + 4096 - i * 1024);
      PIN(vq[i]);
    }
    auto issue_q = [&](int j) { dma4(vq, wqkv - 4096 + ct * 128 + (unsigned)((c0 - j) & 3) * (128 * 3072), dst0 + (j + 4) * 16384); };
    const float g4 = (float)(4 * g);
    u32x4 m0c[2], m0s[2], m0n[2], mac[2], mas[2], man[2], mcc[2][2], mcs[2][2], mcn[2][2];
    f32x4 w1c[2], w1s[2], w2c[2], w2s[2];
    auto tab = [&](int row) { return (unsigned)(7 * 16384 + row * 256 + ((g ^ (row & 15)) << 4)); };
    {
      f32x4 c0v, s0v;
      trig4((float)(16 * wb + c) * (1.f / 64.f), g4, c0v, s0v);
      f32x4 c1v = c0v, s1v = s0v;
      crot(c1v, s1v, (c & 1) ? 0.f : ((c & 2) ? -1.f : 1.f), (c & 1) ? ((c & 2) ? -1.f : 1.f) : 0.f);
      const unsigned sg = (c & 1) ? 0x80008000u : 0u;
      m0c[0] = pack8(c0v, c1v); m0s[0] = pack8(s0v, s1v);
      PIN(m0c[0]); PIN(m0s[0]);
      m0c[1] = m0c[0] ^ sg; m0s[1] = m0s[0] ^ sg;
#pragma unroll
      for (int i = 0; i < 2; ++i) {
        m0n[i] = neg8(m0s[i]);
        *LP(u32x4, tab(16 * wb + c) ^ (64u * i)) = m0c[i];
        *LP(u32x4, tab(16 * wb + c) ^ (64u * i + 128u)) = m0s[i];
      }
    }
#pragma unroll
    for (int tt = 0; tt < 2; ++tt) {
      trig4((float)(16 * wb + c) * (1.f / 2048.f), g4 + 16.f * tt, w1c[tt], w1s[tt]);
      trig4((float)(16 * tt + c) * (1.f / 2048.f), g4 + 16.f * wb, w2c[tt], w2s[tt]);
      PIN(w1c[tt]); PIN(w1s[tt]); PIN(w2c[tt]); PIN(w2s[tt]);
    }
    u32x4 ga[16];
    auto make_g = [&]() {
      const int d = 64 * cb + 16 * wb + c;
      const float df = (float)d * (1.f / 512.f);
      f32x4 ec, es, oc, os;
      trig4(-2.f * df, g4, ec, es);
      trig4(-2.f * df, g4 + 0.5f, oc, os);
      const float ra = __builtin_amdgcn_cosf(-32.f * df), rb_ = __builtin_amdgcn_sinf(-32.f * df);
#pragma unroll
      for (int ss = 0; ss < 4; ++ss) {
        ga[ss] = u32x4{pk2(ec[0], es[0]), pk2(ec[1], es[1]), pk2(ec[2], es[2]), pk2(ec[3], es[3])};
        ga[8 + ss] = u32x4{pk2(oc[0], os[0]), pk2(oc[1], os[1]), pk2(oc[2], os[2]), pk2(oc[3], os[3])};
        PIN(ga[ss]); PIN(ga[8 + ss]);
        if (ss < 3) { crot(ec, es, ra, rb_); crot(oc, os, ra, rb_); }
      }
    };
    const int gr = 8 * ct + 2 * wb + (g & 1);
    unsigned gxp = gr * 256 + ((c ^ (gr & 15)) << 4);
    PIN(gxp);
    const int k = 32 * ct + mt;
    unsigned o_x = (unsigned)(WS_XT + k * 8192 + wb * 2048 + lane * 16), o_w = (unsigned)(WS_WT + k * 8192 + bt * 32);
    unsigned l_xw = L_SCR + (8 * wb + g) * 256 + ((c ^ ((8 * wb + g) & 15)) << 4);
    unsigned l_ca[4], vw[4];
#pragma unroll
    for (int i = 0; i < 4; ++i) {
      const int r = 4 * i + g, sl = c ^ ((r & 7) << 1);
      vw[i] = (unsigned)((16 * wb + r) * 1024 + (sl >> 3) * 512 + ((sl & 7) << 4) + 4096 - i * 1024);
      l_ca[i] = L_SCR + c * 256 + (((4 * i + g) ^ c) << 4);
      PIN(l_ca[i]); PIN(vw[i]);
    }
    unsigned l_ew = L_SCR + 8192 + (wb >> 1) * 4096 + lane * 16 + (wb & 1) * 8, l_er = L_SCR + 8192 + (wb & 1) * 2048 + lane * 16;
    unsigned o_z = (unsigned)(WS_Z + k * 8192 + (2 * (16 * (wb & 1) + c) + (wb >> 1)) * 128 + g * 16);
    PIN(o_x); PIN(o_w); PIN(l_xw); PIN(l_ew); PIN(l_er); PIN(o_z);
    u32x4 gx[4];
    VMW(12); BAR();
    __builtin_amdgcn_s_setprio(3);
    gx[0] = ld16(gxp, 0);
    issue_x(2); issue_x(3);
#pragma unroll
    for (int i = 0; i < 2; ++i) {
      const unsigned ta = tab(2 * (16 * i + c)), tc = tab(32 * (wb >> 1) + 8 * (c >> 2) + 4 * i + (c & 3));
      mac[i] = *LP(u32x4, ta); mas[i] = *LP(u32x4, ta ^ 128u);
      mcc[i][0] = *LP(u32x4, tc); mcc[i][1] = *LP(u32x4, tc ^ 64u); mcs[i][0] = *LP(u32x4, tc ^ 128u); mcs[i][1] = *LP(u32x4, tc ^ 192u);
    }
    asm volatile("s_waitcnt vmcnt(8)" : "+v"(gw[0]), "+v"(gw[1]), "+v"(gw[2]), "+v"(gw[3]) :: "memory");
    BAR();
    gx[1] = ld16(gxp, 16384);
    issue_kv(1); issue_kv(2);
#pragma unroll
    for (int i = 0; i < 2; ++i) { man[i] = neg8(mas[i]); mcn[i][0] = neg8(mcs[i][0]); mcn[i][1] = neg8(mcs[i][1]); }
    VMW(12); BAR();
    const u32x2 tv = *LP(u32x2, L_TAG);
    gx[2] = ld16(gxp, 32768);
    issue_kv(3); issue_kv(4);
    VMW(16); BAR();
    const u64 tag = ((u64)(unsigned)__builtin_amdgcn_readfirstlane(tv.y) << 32) | (unsigned)__builtin_amdgcn_readfirstlane(tv.x);
    gx[3] = ld16(gxp, 49152);
    issue_kv(5); issue_kv(6);
    VMW(16); BAR();
    issue_kv(7); issue_q(0);
    unsigned pfw;
    asm volatile("global_load_dword %0, %1, %2" : "=v"(pfw) : "v"((lane & 31) * 128), "s"(wo + idx * 16384 + wb * 4096) : "memory");
    VMW(17); BAR();
    issue_q(1); issue_q(2);
    VMW(13); BAR();
    issue_q(3);
    {
      float s = 0.f;
#pragma unroll
      for (int i = 0; i < 4; ++i)
#pragma unroll
        for (int j = 0; j < 4; ++j) s += bflo(gx[i][j]) * bflo(gw[i][j]) + bfhi(gx[i][j]) * bfhi(gw[i][j]);
      s = row_sum16(s);
      if ((lane & 47) == 0) ST32(__float_as_uint(__builtin_amdgcn_rcpf(1.f + __expf(-s))), WS_G + (64 * mt + gr) * 4);
    }
    ev_wait<15>(E_G);
    dma4(vw, wo - 4096 + cb * 128 + ((1 + i8) & 7) * 65536, dst0 + 16384);
    dma4(vw, wo - 4096 + cb * 128 + ((2 + i8) & 7) * 65536, dst0 + 32768);
    asm volatile("s_waitcnt vmcnt(9)" : "+v"(pfw) :: "memory");
    SIG(E_QL);
    if (k != 0) dma4(vw, wo - 4096 + cb * 128 + (i8 & 7) * 65536, dst0);
    ev_wait<15>(E_S1);
    dma4(vw, wo - 4096 + cb * 128 + ((3 + i8) & 7) * 65536, dst0 + 49152);
    unsigned mp[8] = {0, 0, 0, 0, 0, 0, 0, 0};
    if (k == 0) {
#pragma unroll
      for (int e = 0; e < 8; ++e) { mp[e] = 4 * wt_pos((2048 - (16 * wb + c + 64 * (16 * (e >> 2) + 4 * g + (e & 3)))) & 2047); PIN(mp[e]); }
    }
    POLLN(LN_FB + ct * 32 + 8 * wb, 8, tag);
    SIG(E_FB);
    __builtin_amdgcn_s_setprio(2);
    {
      const u32x4 x0 = LD128(o_x), x1 = LD128(o_x + 1024);
      SB();
      const u32x4 wv[2] = {LD128(o_w), LD128(o_w + 16)};
      const unsigned o_wn = k == 0 ? o_w + 256 * 8192 : (unsigned)(WS_END + 4096);
      const u32x4 wn[2] = {LD128(o_wn), LD128(o_wn + 16)};
      SB();
      *LP(u32x4, l_xw) = x0; *(LAS u32x4*)(LP(char, l_xw ^ 64u) + 1024) = x1;
      u32x4 ar, ai;
      u32x4 ca[2][2][2];
      auto rd_ca = [&]() {
#pragma unroll
        for (int i = 0; i < 8; ++i) ca[i >> 2][(i >> 1) & 1][i & 1] = ld16(l_ca[i >> 1], (i & 1) * 4096);
      };
      auto twiddle_dft32 = [&](const f32x4 (&yr)[2], const f32x4 (&yi)[2], f32x4 (&xr)[2], f32x4 (&xi)[2]) {
        const u32x4 br = pack8(yr[0] * w1c[0] + yi[0] * w1s[0], yr[1] * w1c[1] + yi[1] * w1s[1]);
        const u32x4 bi = pack8(yi[0] * w1c[0] - yr[0] * w1s[0], yi[1] * w1c[1] - yr[1] * w1s[1]);
#pragma unroll
        for (int ut = 0; ut < 2; ++ut) { xr[ut] = mfma32(mac[ut], br, zero); xi[ut] = mfma32(man[ut], br, zero); }
#pragma unroll
        for (int ut = 0; ut < 2; ++ut) { xr[ut] = mfma32(mas[ut], bi, xr[ut]); xi[ut] = mfma32(mac[ut], bi, xi[ut]); }
      };
      if (k != 0) {
        SIG(E_X1);
        EV_WAIT_READ(E_X1, rd_ca());
        f32x4 yr[2] = {zero, zero}, yi[2] = {zero, zero}, xr[2], xi[2];
#pragma unroll
        for (int ks = 0; ks < 2; ++ks)
#pragma unroll
          for (int tt = 0; tt < 2; ++tt) { yr[tt] = mfma32(ca[0][ks][tt], m0c[ks], yr[tt]); yi[tt] = mfma32(ca[0][ks][tt], m0n[ks], yi[tt]); }
#pragma unroll
        for (int ks = 0; ks < 2; ++ks)
#pragma unroll
          for (int tt = 0; tt < 2; ++tt) { yr[tt] = mfma32(ca[1][ks][tt], m0s[ks], yr[tt]); yi[tt] = mfma32(ca[1][ks][tt], m0c[ks], yi[tt]); }
        f32x4 fr[2] = {lo4(wv[0]), lo4(wv[1])}, fi[2] = {hi4(wv[0]), hi4(wv[1])};
        PIN(fr[0]); PIN(fr[1]); PIN(fi[0]); PIN(fi[1]);
#pragma unroll
        for (int i = 0; i < 16; ++i) { __builtin_amdgcn_sched_group_barrier(0x8, 1, 0); __builtin_amdgcn_sched_group_barrier(0x2, 1, 0); }
        SB();
        twiddle_dft32(yr, yi, xr, xi);
        ar = pack8(xr[0] * fr[0] - xi[0] * fi[0], xr[1] * fr[1] - xi[1] * fi[1]);
        ai = pack8(xr[0] * fi[0] + xi[0] * fr[0], xr[1] * fi[1] + xi[1] * fr[1]);
      } else {
        LAS u32x4* d = (LAS u32x4*)sm + bt * 2;
        d[0] = wv[0]; d[1] = wv[1]; d[512] = wn[0]; d[513] = wn[1];
        SIG(E_X1);
        ev_wait<15>(E_X1);
        rd_ca();
        u32x4 m0[2], m1[2];
#pragma unroll
        for (int e = 0; e < 8; ++e) { m0[e >> 2][e & 3] = *LP(unsigned, mp[e]); m1[e >> 2][e & 3] = *(LAS unsigned*)(LP(char, mp[e]) + 8192); }
        SB();
        f32x4 yr[2] = {zero, zero}, yi[2] = {zero, zero}, zr[2] = {zero, zero}, zi[2] = {zero, zero}, xr[2], xi[2], ur[2], ui[2];
#pragma unroll
        for (int ks = 0; ks < 2; ++ks)
#pragma unroll
          for (int tt = 0; tt < 2; ++tt) { yr[tt] = mfma32(ca[0][ks][tt], m0c[ks], yr[tt]); yi[tt] = mfma32(ca[0][ks][tt], m0n[ks], yi[tt]); }
#pragma unroll
        for (int ks = 0; ks < 2; ++ks)
#pragma unroll
          for (int tt = 0; tt < 2; ++tt) { zr[tt] = mfma32(ca[1][ks][tt], m0s[ks], zr[tt]); zi[tt] = mfma32(ca[1][ks][tt], m0c[ks], zi[tt]); }
        twiddle_dft32(yr, yi, xr, xi);
        twiddle_dft32(zr, zi, ur, ui);
        f32x4 dr[2], di[2];
#pragma unroll
        for (int ut = 0; ut < 2; ++ut) {
          const f32x4 fr = lo4(wv[ut]) + lo4(m0[ut]), fi = hi4(wv[ut]) - hi4(m0[ut]);
          const f32x4 hr = lo4(wn[ut]) + lo4(m1[ut]), hi = hi4(wn[ut]) - hi4(m1[ut]);
          dr[ut] = (xr[ut] * fr - xi[ut] * fi) + (ur[ut] * hr - ui[ut] * hi);
          di[ut] = (xr[ut] * fi + xi[ut] * fr) + (ur[ut] * hi + ui[ut] * hr);
        }
        ar = pack8(dr[0], dr[1]); ai = pack8(di[0], di[1]);
      }
      {
        f32x4 vr[2], vi[2];
#pragma unroll
        for (int tt = 0; tt < 2; ++tt) { vr[tt] = mfma32(ar, mac[tt], zero); vi[tt] = mfma32(ar, mas[tt], zero); }
#pragma unroll
        for (int tt = 0; tt < 2; ++tt) { vr[tt] = mfma32(ai, man[tt], vr[tt]); vi[tt] = mfma32(ai, mac[tt], vi[tt]); }
        SB();
#pragma unroll
        for (int tt = 0; tt < 2; ++tt) {
          const f32x4 pr = vr[tt] * w2c[tt] - vi[tt] * w2s[tt], pi = vi[tt] * w2c[tt] + vr[tt] * w2s[tt];
          *(LAS u32x2*)(LP(char, l_ew) + tt * 2048) = u32x2{pk2(pr[0], pr[1]), pk2(pr[2], pr[3])};
          *(LAS u32x2*)(LP(char, l_ew) + tt * 2048 + 1024) = u32x2{pk2(pi[0], pi[1]), pk2(pi[2], pi[3])};
        }
      }
      SIG(E_X2);
      u32x4 ve[2][2];
      EV_WAIT_READ(E_X2, { ve[0][0] = ld16(l_er, 0); ve[0][1] = ld16(l_er, 1024); ve[1][0] = ld16(l_er, 4096); ve[1][1] = ld16(l_er, 5120); });
      f32x4 zr[2] = {zero, zero}, zi[2] = {zero, zero};
#pragma unroll
      for (int ks = 0; ks < 2; ++ks) {
#pragma unroll
        for (int jt = 0; jt < 2; ++jt) { zr[jt] = mfma32(mcc[jt][ks], ve[ks][0], zr[jt]); zi[jt] = mfma32(mcs[jt][ks], ve[ks][0], zi[jt]); }
#pragma unroll
        for (int jt = 0; jt < 2; ++jt) { zr[jt] = mfma32(mcn[jt][ks], ve[ks][1], zr[jt]); zi[jt] = mfma32(mcc[jt][ks], ve[ks][1], zi[jt]); }
      }
      SB();
      ST128(pack8(zr[0], zr[1]), o_z);
      ST128(pack8(zi[0], zi[1]), o_z + 64);
    }
    {
      make_g();
      const unsigned sel = (c & 1) ? 0x01000302u : 0x03020100u;
      const unsigned sgn = (c & 3) == 0 ? 0u : (c & 3) == 1 ? 0x80000000u : (c & 3) == 2 ? 0x80008000u : 0x00008000u;
      auto g_rest = [&]() {
#pragma unroll
        for (int f = 0; f < 8; ++f) {
          const int src = (f >> 2) * 8 + (f & 3);
#pragma unroll
          for (int i = 0; i < 4; ++i) ga[src + 4][i] = __builtin_amdgcn_perm(0u, ga[src][i], sel) ^ sgn;
          PIN(ga[src + 4]);
        }
        if (g == 0) ga[0][0] = (c & 1) ? 0xBE803E80u : 0x3E803E80u;
        PIN(ga[0]);
      };
      SB();
      DRAIN();
      SIG(E_ZD);
      if (wb == 0) {
        ev_wait<15>(E_ZD);
        if (lane < REP) __hip_atomic_fetch_add((GAS u64*)(ws + (size_t)(LN_CZ + lane * 16 + 2 * ct + (mt >> 4)) * 128), 1ull, RLX_AGENT);
      }
      __builtin_amdgcn_s_setprio(0);
      g_rest();
    }
    unsigned o_zl = (unsigned)(WS_Z + (64 * wb + (lane >> 3)) * 8192 + rb * 128 + (lane & 7) * 16);
    unsigned l_zw, l_z0, l_z1;
    {
      const int kk = 64 * wb + (lane >> 3), R = (((kk & 1) * 128 + (kk >> 1)) << 1) + ((lane >> 2) & 1);
      l_zw = R * 64 + ((lane & 3) << 4);
      l_z0 = (8 * g + (c >> 2)) * 64 + 8 * (c & 3) + ((g & 1) << 5);
      l_z1 = l_z0 ^ 32u;
    }
    unsigned l_zw1 = l_zw ^ 32u, l_a = 98304u + wb * 8192 + lane * 16;
    PIN(l_a);
    PIN(o_zl); PIN(l_zw); PIN(l_zw1); PIN(l_z0); PIN(l_z1);
    __builtin_amdgcn_s_setprio(0);
    POLLN(LN_CZ + (bid % REP) * 16 + 4 * wb, 4, tag + 16);
    {
      u32x4 z[8];
#pragma unroll
      for (int i = 0; i < 8; ++i) z[i] = __builtin_amdgcn_raw_buffer_load_b128(rs, o_zl, i * 8 * 8192, 16);
      ev_wait<15>(E_WR);
#pragma unroll
      for (int i = 0; i < 8; ++i) *(LAS u32x4*)(LP(char, (i & 1) ? l_zw1 : l_zw) + i * 512) = z[i];
    }
    SIG(E_ZI);
    f32x4 acc[2][2] = {{zero, zero}, {zero, zero}};
    {
      u32x4 zf[3][2][2];
      auto ldz = [&](int n) {
#pragma unroll
        for (int i = 0; i < 2; ++i) { zf[n % 3][i][0] = trfrag(l_z0, (2 * n + i) * 2048, 256); zf[n % 3][i][1] = trfrag(l_z1, (2 * n + i) * 2048, 256); }
      };
      EV_WAIT_READ(E_ZI, { ldz(0); ldz(1); });
#pragma unroll
      for (int n = 0; n < 8; ++n) {
        if (n < 6) ldz(n + 2);
#pragma unroll
        for (int i = 0; i < 2; ++i) {
          acc[n >> 2][0] = mfma32(ga[2 * n + i], zf[n % 3][i][0], acc[n >> 2][0]);
          acc[n >> 2][1] = mfma32(ga[2 * n + i], zf[n % 3][i][1], acc[n >> 2][1]);
        }
#pragma unroll
        for (int i = 0; i < 4; ++i) { __builtin_amdgcn_sched_group_barrier(0x100, 2, 0); __builtin_amdgcn_sched_group_barrier(0x8, 1, 0); }
        SB();
      }
    }
#pragma unroll
    for (int tt = 0; tt < 2; ++tt) {
      *(LAS f32x4*)(LP(char, l_a) + tt * 1024) = acc[0][tt] + acc[1][tt];
      *(LAS f32x4*)(LP(char, l_a) + 2048 + tt * 1024) = acc[0][tt] - acc[1][tt];
    }
    SIG(E_A);
  } else {
    __builtin_amdgcn_s_setprio(1);
    u32x4 wrv, wiv;
    unsigned nyq = 0, nyq2 = 0, pf = 0;
    {
      const unsigned o_w = (unsigned)(wt_row(64 * mt + (tid >> 2)) * 257 + 32 * ct + 8 * (tid & 3)) * 2u;
      wrv = *(const u32x4u*)(w_r + o_w);
      wiv = *(const u32x4u*)(w_i + o_w);
      if (ct == 0 && w == 0) {
        const unsigned o_ny = (unsigned)(wt_row(64 * mt + lane) * 257 + 256) * 2u;
        nyq = *(const unsigned short*)(w_r + o_ny); nyq2 = *(const unsigned short*)(w_i + o_ny);
      }
    }
    if (w == 2 && lane < CODE_LINES) pf = *(const GAS unsigned*)(pc0 + min((idx * CODE_LINES + lane) * 128, CODE_MAX));
    const u32x2 ev = __builtin_amdgcn_raw_buffer_load_b64(rs, 0, 0, 16);
    const int rec = (2 * ct + (w >> 1)) * 64 + 2 * mt + (w & 1);
    unsigned o_kp = (unsigned)(WS_KVP + rec * 1024 + lane * 16), o_zp = (unsigned)(WS_ZP + rec * 64 + g * 16);
    ST128((u32x4{~0u, ~0u, ~0u, ~0u}), o_kp);
    if (c == 0) ST128((u32x4{~0u, ~0u, ~0u, ~0u}), o_zp);
    SB();
    const int rh = w & 1, kt = w >> 1;
    const int kf = 32 * ct + 16 * kt + c;
    u32x4 xf[4][4][2];
    unsigned bb[2][4];
    {
      const unsigned fl = (unsigned)((c >> 2) | ((g & 1) << 2));
      const unsigned bu = (unsigned)((8 * g + (c >> 2)) * 256 + 8 * (c & 3)) + (((unsigned)(2 * kt) ^ fl) << 5);
#pragma unroll
      for (int u = 0; u < 4; ++u) {
        bb[0][u] = bu ^ (unsigned)((u & 1) * 32 + (u >> 1) * 128);
        bb[1][u] = bb[0][u] + 65536u;
      }
    }
    u32x4 fb[3][4], evq;
    auto ldf = [&](int n) {
#pragma unroll
      for (int u = 0; u < 4; ++u) {
        const int slot = (4 + (n >> 1)) & 7;
        fb[n % 3][u] = trfrag(bb[slot >> 2][u], (slot & 3) * 16384 + (n & 1) * 8192, 1024);
      }
    };
    u64 tag;
    {
      u32x4 fc[4], fs[4];
      {
        const float kk = (float)kf * (1.f / 512.f);
        const float a0 = __builtin_amdgcn_fractf(kk * (float)(8 * g + 128 * ((c0 + 1) & 3)));
#pragma unroll
        for (int ks = 0; ks < 4; ++ks) {
          const float ak = __builtin_fmaf(kk, (float)(((ks & 2) ^ (flip << 1)) * 32), a0);
#pragma unroll
          for (int i = 0; i < 4; ++i) {
            const float p0 = __builtin_fmaf(kk, (float)(32 * (ks & 1) + 2 * i), ak), p1 = __builtin_fmaf(kk, (float)(32 * (ks & 1) + 2 * i + 1), ak);
            fc[ks][i] = pk2(__builtin_amdgcn_cosf(p0), __builtin_amdgcn_cosf(p1));
            fs[ks][i] = kf == 0 ? 0xBF803F80u
                                : pk2(__builtin_amdgcn_sinf(-p0), __builtin_amdgcn_sinf(-p1));
          }
          PIN(fc[ks]); PIN(fs[ks]);
        }
      }
      float ra = (c & 1) ? 0.f : ((c & 2) ? -1.f : 1.f), rbi = (c & 1) ? ((c & 2) ? 1.f : -1.f) : 0.f;
      unsigned l_sc = L_SCR + ((tid & 3) * 512 + ((tid >> 2) ^ ((tid & 3) << 3))) * 4;
      unsigned l_rd = L_SCR + (tid >> 4) * 256 + (((tid & 15) ^ ((tid >> 7) << 1)) << 4);
      unsigned o_wt = (unsigned)(WS_WT + (32 * ct + (tid >> 4)) * 8192 + (64 * mt + 4 * (tid & 15)) * 4);
      unsigned o_xt = (unsigned)(WS_XT + kf * 8192 + mt * 256 + rh * 64 + g * 16);
      unsigned xa[4];
#pragma unroll
      for (int ks = 0; ks < 4; ++ks) { xa[ks] = rh * 8192 + c * 256 + (((4 * ks + g) ^ (flip << 3) ^ c) << 4); PIN(xa[ks]); }
#pragma unroll
      for (int u = 0; u < 4; ++u) { PIN(bb[0][u]); PIN(bb[1][u]); }
      PIN(l_sc); PIN(l_rd); PIN(o_wt); PIN(o_xt); PIN(ra); PIN(rbi); PIN(o_kp); PIN(o_zp);
      f32x4 xr[4][2], xi[4][2];
      auto ldx = [&](int s) {
#pragma unroll
        for (int ks = 0; ks < 4; ++ks)
#pragma unroll
          for (int tt = 0; tt < 2; ++tt) xf[s][ks][tt] = ld16(xa[ks], s * 16384 + tt * 4096);
      };
      auto mmf = [&](int s) {
#pragma unroll
        for (int tt = 0; tt < 2; ++tt) { xr[s][tt] = zero; xi[s][tt] = zero; }
#pragma unroll
        for (int ks = 0; ks < 4; ++ks)
#pragma unroll
          for (int tt = 0; tt < 2; ++tt) {
            xr[s][tt] = mfma32(xf[s][ks][tt], fc[ks], xr[s][tt]);
            xi[s][tt] = mfma32(xf[s][ks][tt], fs[ks], xi[s][tt]);
          }
      };
      auto horner = [&](int s) {
#pragma unroll
        for (int tt = 0; tt < 2; ++tt) {
          xr[s][tt] = (xr[s][tt] + xr[s - 1][tt] * ra) - xi[s - 1][tt] * rbi;
          xi[s][tt] = (xi[s][tt] + xr[s - 1][tt] * rbi) + xi[s - 1][tt] * ra;
        }
      };
      auto mixv = [&]() {
#pragma unroll
        for (int i = 0; i < 16; ++i) { __builtin_amdgcn_sched_group_barrier(0x8, 1, 0); __builtin_amdgcn_sched_group_barrier(0x2 | 0x80 | 0x40, 2, 0); }
        SB();
      };
      BAR();
      ldx(0);
      {
        PIN(pf); PIN(nyq); PIN(nyq2);
        tag = TAG_OF(((u64)(unsigned)__builtin_amdgcn_readfirstlane(ev.y) << 32) | (unsigned)__builtin_amdgcn_readfirstlane(ev.x));
        if (w == 3) *LP(u32x2, L_TAG) = u32x2{(unsigned)tag, (unsigned)(tag >> 32)};
        LAS unsigned* sc = LP(unsigned, l_sc);
#pragma unroll
        for (int j = 0; j < 4; ++j) {
          sc[(2 * j) * 64] = __builtin_amdgcn_perm(wiv[j], wrv[j], 0x05040100u);
          sc[(2 * j + 1) * 64] = __builtin_amdgcn_perm(wiv[j], wrv[j], 0x07060302u);
        }
        if (ct == 0 && w == 0) ST32(nyq | (nyq2 << 16), WS_WT + 256 * 8192 + (64 * mt + lane) * 4);
        if (mt == 0 && w == 1 && lane < 4 * REP)
          put_flag(ws, LN_CZ + (lane >> 1) * 16 + 2 * ct + (lane & 1), tag);
      }
      BAR();
      const u32x4 wt0 = ld16(l_rd, 0), wt1 = ld16(l_rd ^ 64u, 4096);
      ldx(1); SB(); mmf(0);
      BAR();
      ldx(2); SB(); mmf(1);
      BAR();
      ldx(3); SB();
      mmf(2); horner(1);
      ST128(wt0, o_wt); ST128(wt1, o_wt + 16 * 8192);
      mixv();
      mmf(3); horner(2);
      mixv();
      ldf(0); ldf(1);
      horner(3);
      ST128(pack8(xr[3][0], xr[3][1]), o_xt);
      ST128(pack8(xi[3][0], xi[3][1]), o_xt + 128);
    }
    auto mix = [&]() {
#pragma unroll
      for (int i = 0; i < 8; ++i) { __builtin_amdgcn_sched_group_barrier(0x100, 1, 0); __builtin_amdgcn_sched_group_barrier(0x8, 1, 0); }
      SB();
    };
    {
      f32x4 acc[2][4];
#pragma unroll
      for (int t = 0; t < 2; ++t)
#pragma unroll
        for (int u = 0; u < 4; ++u) acc[t][u] = u < 2 ? eps4 : zero;
#pragma unroll
      for (int n = 0; n < 16; ++n) {
        if (n == 8) DRAIN();
        if (n == 0 || n == 4 || n == 8) BAR();
        if (n == 8) { if (tid == 0) put_flag(ws, LN_FB + ct * 32 + mt, tag); SB(); }
        if (n == 12) { SIG(E_G); SB(); }
        if (n == 14) { SIG(E_S1); SB(); }
        if (n < 14) ldf(n + 2);
#pragma unroll
        for (int u = 0; u < 4; ++u)
#pragma unroll
          for (int t = 0; t < 2; ++t) acc[t][u] = mfma32(xf[n >> 2][n & 3][t], fb[n % 3][u], acc[t][u]);
        mix();
      }
      CFENCE();
      evq = *(const volatile LAS u32x4*)(size_t)(L_SYNC + E_QL * 16);
      CFENCE();
      ldf(0); ldf(1);
      SB();
      const u32x4 ones = {0x3f803f80u, 0x3f803f80u, 0x3f803f80u, 0x3f803f80u};
      f32x4 kv[2], zz[2];
#pragma unroll
      for (int n = 0; n < 2; ++n) {
        const u32x4 pk = pack8(phi4(acc[0][n]), phi4(acc[1][n]));
        kv[n] = mfma32(pk, pack8(acc[0][2 + n], acc[1][2 + n]), zero);
        zz[n] = mfma32(pk, ones, zero);
      }
      u32x4 kvo = pack8(kv[0], kv[1]), zo = pack8(zz[0], zz[1]);
#pragma unroll
      for (int e = 0; e < 4; ++e) {
        kvo[e] = min(kvo[e], 0xfffffffeu);
        zo[e] = min(zo[e], 0xfffffffeu);
      }
      ST128(kvo, o_kp);
      if (c == 0) ST128(zo, o_zp);
    }
    {
      const int hp = 2 * ct + (mt >> 4), sub = mt & 15;
      u32x4 pv[4];
      unsigned pz;
      auto ldp = [&]() {
#pragma unroll
        for (int i = 0; i < 4; ++i)
          pv[i] = __builtin_amdgcn_raw_buffer_load_b128(rs, WS_KVP + (hp * 64 + (lane >> 2)) * 1024 + sub * 64 + (lane & 3) * 16, i * 16 * 1024, 16);
        pz = LD32(WS_ZP + (hp * 64 + lane) * 64 + sub * 4);
      };
      f32x4 acc[2][2] = {{eps4, eps4}, {eps4, eps4}};
      if (!__builtin_amdgcn_readfirstlane(evq.x & evq.y & evq.z & evq.w)) { ev_wait<15>(E_QL); ldf(0); ldf(1); }
      SB();
#pragma unroll
      for (int n = 0; n < 8; ++n) {
        if (n < 6) ldf(n + 2);
        if (n == 6) { SIG(E_QR); SB(); }
        if (n == 5) {
          if (w == 0) { ev_wait<15>(E_FB); ldp(); __builtin_amdgcn_s_setprio(3); }
          SB();
        }
#pragma unroll
        for (int u = 0; u < 4; ++u)
#pragma unroll
          for (int t = 0; t < 2; ++t) acc[u & 1][t] = mfma32(fb[n % 3][u], xf[n >> 1][2 * (u >> 1) + (n & 1)][t], acc[u & 1][t]);
        mix();
      }
      auto st_pq = [&]() {
#pragma unroll
        for (int n = 0; n < 2; ++n)
          ST128(pack8(phi4(acc[n][0]), phi4(acc[n][1])), WS_PQ + ((2 * mt + rh) * 32 + 4 * ct + 2 * kt + n) * 1024 + lane * 16);
        SB();
      };
      st_pq();
      if (w == 0) {
        for (unsigned spins = 0; spins < SPIN_CAP; ++spins) {
          unsigned m = pz;
#pragma unroll
          for (int i = 0; i < 4; ++i) m = max(max(m, max(pv[i][0], pv[i][1])), max(pv[i][2], pv[i][3]));
          if (__all(m != ~0u)) break;
          CFENCE();
          ldp();
        }
        const unsigned one = (c & 1) ? 0x3f800000u : 0x00003f80u;
        const u32x4 sel = {(c >> 1) == 0 ? one : 0u, (c >> 1) == 1 ? one : 0u, (c >> 1) == 2 ? one : 0u, (c >> 1) == 3 ? one : 0u};
        f32x4 s = zero;
#pragma unroll
        for (int i = 0; i < 4; ++i) s = mfma32(sel, pv[i], s);
        const f32x4 zq = mfma32(sel, u32x4{pz, 0u, 0u, 0u}, zero);
#pragma unroll
        for (int e = 0; e < 4; ++e) s[e] = dpp_add<0x128>(dpp_add<0x124>(s[e]));
        const float z0 = row_sum16(zq[0]), z1 = row_sum16(zq[1]);
        if ((lane & 44) == 0) ST64((u32x2{pk2(s[0], s[1]), pk2(s[2], s[3])}), WS_KVF + hp * 1024 + (sub * 4 + c) * 16 + g * 8);
        if (lane == 0) ST32(pk2(z0, z1), WS_ZF + hp * 64 + sub * 4);
        SB();
      }
      if (w == 0) {
        VMW(2);
        ev_wait<14>(E_PQ);
        if (lane == 0) put_flag(ws, LN_FQ + mt * 8 + ct, tag);
        VMW(1);
        if (lane < REP) __hip_atomic_fetch_add((GAS u64*)(ws + (size_t)(LN_CK + lane * 16 + 2 * ct + (mt >> 4)) * 128), 1ull, RLX_AGENT);
      } else {
        unsigned voff[4];
#pragma unroll
        for (int i = 0; i < 4; ++i) {
          const int r = 4 * i + g, sl = c ^ ((r & 7) << 1);
          voff[i] = (unsigned)(r * 1024 + (sl >> 3) * 512 + ((sl & 7) << 4) + 4096 - i * 1024);
        }
        ev_wait<15>(E_QR);
#pragma unroll 1
        for (int gj = 15 + w; gj < ((ct | mt) == 0 ? 36 : 32); gj += 3) {
          const int gi = gj & 31;
          if (gj >= 32 && (ct | mt) == 0) ev_wait<15>(E_X2);
          dma4(voff, wo - 4096 + cb * 128 + ((gi + 4 * i8) & 31) * 16384, gi * 4096);
          VMW(4);
          if (gj < 19) SIG(E_PQ);
        }
        DRAIN();
        SIG(E_WL);
      }
    }
    __builtin_amdgcn_s_setprio(1);
    u32x4 wa[16][2], pq[8], kf4[4], zf4[4];
    float gate[2];
    unsigned l_o[4];
#pragma unroll
    for (int q = 0; q < 4; ++q) { l_o[q] = 65536u + ((w + q) & 3) * 8192 + lane * 16; PIN(l_o[q]); }
    unsigned o_kf = (unsigned)(WS_KVF + 4 * w * 1024 + lane * 16), o_zf = (unsigned)(WS_ZF + 4 * w * 64 + g * 16), l_or = 65536u + lane * 16;
    unsigned o_pq = (unsigned)(WS_PQ + (rb * 32 + 8 * w) * 1024 + lane * 16), o_g = (unsigned)(WS_G + (32 * rb + c) * 4);
    unsigned o_fl = lane < 24 ? (unsigned)(lane < 16 ? LN_FQ + (rb >> 1) * 8 + (lane >> 1) : LN_CK + (bid % REP) * 16 + 4 * w + ((lane >> 1) & 3)) * 128u + (lane & 1) * 4
                              : (unsigned)(WS_END + 4096);
    unsigned want = (unsigned)((lane < 16 ? tag : tag + 16) >> ((lane & 1) * 32));
    PIN(o_kf); PIN(o_zf); PIN(l_or); PIN(o_pq); PIN(o_g); PIN(o_fl); PIN(want);
    {
      const unsigned f7 = (unsigned)((4 * g + (c >> 2)) & 7);
      const unsigned pw0 = (4 * g + (c >> 2)) * 256 + 8 * (c & 3) + (((unsigned)w ^ f7) << 5);
      ev_wait<15>(E_X1);
      ev_wait<14>(E_WL);
      unsigned fv = LOOK(o_fl);
      SB();
#pragma unroll
      for (int sp = 0; sp < 8; ++sp) {
        const unsigned p0 = pw0 + (unsigned)((2 * w + sp - i8) & 7) * 16384u, p1 = p0 ^ 128u;
#pragma unroll
        for (int i = 0; i < 2; ++i) { wa[2 * sp + i][0] = trfrag(p0, i * 8192, 4096); wa[2 * sp + i][1] = trfrag(p1, i * 8192, 4096); }
      }
      SIG(E_WR);
      SB();
      unsigned ok = (unsigned)__ballot(fv == want);
      for (unsigned spins = 0; (ok & 0xffffu) != 0xffffu && spins < SPIN_CAP; ++spins) {
        __builtin_amdgcn_s_sleep(POLL_SLEEP);
        ok = (unsigned)__ballot(LOOK(o_fl) == want);
      }
      CFENCE();
      fv = LOOK(((ok >> 16) & 0xffu) == 0xffu ? (unsigned)(WS_END + 4096) : o_fl);
      SB();
#pragma unroll
      for (int i = 0; i < 8; ++i) pq[i] = __builtin_amdgcn_raw_buffer_load_b128(rs, o_pq + (i & 3) * 1024, (i >> 2) * 4096, 16);
#pragma unroll
      for (int tt = 0; tt < 2; ++tt) gate[tt] = __uint_as_float(LD32(o_g + 64 * tt));
      SB();
      if (((ok >> 16) & 0xffu) != 0xffu) {
        ok = (unsigned)__ballot(fv == want);
        for (unsigned spins = 0; ((ok >> 16) & 0xffu) != 0xffu && spins < SPIN_CAP; ++spins) ok = (unsigned)__ballot(LOOK(o_fl) == want);
      }
      CFENCE();
    }
    u32x4 ob[2][4][2];
    unsigned o_out = (unsigned)(((rb >> 1) + 32 * (32 * (rb & 1) + c)) * 1024 + (64 * cb + 16 * w + 4 * g) * 2);
    {
#pragma unroll
      for (int i = 0; i < 4; ++i) { kf4[i] = LD128(o_kf + i * 1024); zf4[i] = LD128(o_zf + i * 64); }
      PIN(o_out);
      ev_wait<15>(E_WR);
      f32x4 num[2][2][2], den[2][2][2];
      auto mo = [&](int hp) {
#pragma unroll
        for (int tt = 0; tt < 2; ++tt)
#pragma unroll
          for (int n = 0; n < 2; ++n) {
            const u32x4 pp = pq[2 * hp + n];
            const u32x2 b = tt ? u32x2{pp.z, pp.w} : u32x2{pp.x, pp.y};
            const u32x2 ka = n ? u32x2{kf4[hp].z, kf4[hp].w} : u32x2{kf4[hp].x, kf4[hp].y};
            const u32x2 za = n ? u32x2{zf4[hp].z, zf4[hp].w} : u32x2{zf4[hp].x, zf4[hp].y};
            den[hp & 1][tt][n] = mfma16(za, b, f32x4{1e-6f, 1e-6f, 1e-6f, 1e-6f});
            num[hp & 1][tt][n] = mfma16(ka, b, zero);
          }
      };
      mo(0);
      SB();
#pragma unroll
      for (int hp = 0; hp < 4; ++hp) {
        if (hp < 3) mo(hp + 1);
        SB();
#pragma unroll
        for (int tt = 0; tt < 2; ++tt) {
          ob[0][hp][tt] = pack8(num[hp & 1][tt][0] * __builtin_amdgcn_rcpf(den[hp & 1][tt][0][0]),
                                num[hp & 1][tt][1] * __builtin_amdgcn_rcpf(den[hp & 1][tt][1][0]));
          *(LAS u32x4*)(LP(char, l_o[0]) + (hp * 2 + tt) * 1024) = ob[0][hp][tt];
        }
        SB();
      }
    }
    SIG(E_O);
    f32x4 acc[2][2] = {{zero, zero}, {zero, zero}};
#pragma unroll
    for (int q = 0; q < 4; ++q) {
      const volatile LAS unsigned* fp = (const volatile LAS unsigned*)(size_t)(L_SYNC + E_O * 16 + ((w + q + 1) & 3) * 4);
      unsigned up = 1u;
      if (q < 3) {
        CFENCE();
        up = *fp;
        CFENCE();
#pragma unroll
        for (int i = 0; i < 8; ++i) ob[(q + 1) & 1][i >> 1][i & 1] = ld16(l_o[q + 1], i * 1024);
        SB();
      }
#pragma unroll
      for (int i = 0; i < 8; ++i) {
        acc[0][i & 1] = mfma32(wa[4 * q + (i >> 1)][0], ob[q & 1][i >> 1][i & 1], acc[0][i & 1]);
        acc[1][i & 1] = mfma32(wa[4 * q + (i >> 1)][1], ob[q & 1][i >> 1][i & 1], acc[1][i & 1]);
      }
      SB();
      if (q < 3 && !__builtin_amdgcn_readfirstlane(up)) {
        while (!__builtin_amdgcn_readfirstlane(*fp)) __builtin_amdgcn_s_sleep(1);
        CFENCE();
#pragma unroll
        for (int i = 0; i < 8; ++i) ob[(q + 1) & 1][i >> 1][i & 1] = ld16(l_o[q + 1], i * 1024);
      }
    }
    {
      const volatile LAS unsigned* fp = (const volatile LAS unsigned*)(size_t)(L_SYNC + E_A * 16 + w * 4);
      while (!__builtin_amdgcn_readfirstlane(*fp)) __builtin_amdgcn_s_sleep(1);
      CFENCE();
#pragma unroll
      for (int tt = 0; tt < 2; ++tt) {
        const float sc = (1.f - gate[tt]) * (2.f / (2048.f * 512.f));
#pragma unroll
        for (int hf = 0; hf < 2; ++hf) {
          const f32x4 bv = __builtin_bit_cast(f32x4, ld16(l_or + w * 8192, 32768 + (hf * 2 + tt) * 1024));
          const f32x4 r = acc[hf][tt] * gate[tt] + bv * sc;
          *(u32x2*)(out + o_out + tt * (16 * 32768) + 512 * hf) = u32x2{pk2(r[0], r[1]), pk2(r[2], r[3])};
        }
      }
    }
    if (bid == 0 && tid == 0) ST64((u32x2{ev.x + 1u, ev.y}), 0);
  }
}

extern "C" void kernel_launch(void* const* d_in, const int* in_sizes, int n_in, void* d_out, int out_size,
                              void* d_ws, size_t ws_size, hipStream_t stream) {
  (void)hipFuncSetAttribute((const void*)fused_kernel, hipFuncAttributeMaxDynamicSharedMemorySize, LDS_BYTES);
  fused_kernel<<<256, 512, LDS_BYTES, stream>>>((const char*)d_in[0], (const char*)d_in[1], (const char*)d_in[2],
                                                (const char*)d_in[4], (const char*)d_in[5], (const char*)d_in[6],
                                                (char*)d_ws, (char*)d_out);
}
```
